```python
import torch

def _softmax(x, axis=-1):
    return torch.softmax(x, dim=axis)
def _gelu(x):
    return torch.nn.functional.gelu(x, approximate="tanh")
def _silu(x):
    return torch.nn.functional.silu(x)
def _sigmoid(x):
    return torch.sigmoid(x)
def _layernorm(x, axis=-1, eps=1e-5):
    m = x.mean(dim=axis, keepdim=True)
    v = x.var(dim=axis, keepdim=True, unbiased=False)
    return (x - m) / torch.sqrt(v + eps)
def _scatter_add(tgt, idx, src, axis=0):
    return tgt.clone().index_add_(axis, idx, src)
def _segment_sum(src, seg, n):
    out = torch.zeros((n,) + src.shape[1:], dtype=src.dtype)
    return out.index_add_(0, seg, src)
def _pad(x, pw):
    flat = tuple(v for pair in reversed(pw) for v in pair)
    return torch.nn.functional.pad(x, flat)

kernel_name = 'interleave_layers__sliding_local_dilated__dense_activation_gelu__prompt'

def setup_inputs():

    _w = (lambda a: (lambda t: t.to(torch.bfloat16) if t.is_floating_point() else t)(torch.as_tensor(a)))
    def _A_setup():
        B, S, D = 1, 2048, 576
        return {
            "x": _w((lambda *s: torch.randn(*s, dtype=torch.bfloat16) * 0.05)(B, S, D)),
            "wqkv": _w((lambda *s: torch.randn(*s, dtype=torch.bfloat16) * 0.05)(D, 3 * D)),
            "wo": _w((lambda *s: torch.randn(*s, dtype=torch.bfloat16) * 0.05)(D, D)),
        }
    _A_in = _A_setup()

    def _B_setup():
        B, S, D, F = 1, 2048, 576, 4608
        return {
            "x": _w((lambda *s: torch.randn(*s, dtype=torch.bfloat16) * 0.05)(B, S, D)),
            "w1": _w((lambda *s: torch.randn(*s, dtype=torch.bfloat16) * 0.8)(D, F) * (D ** -0.5)),
            "b1": _w((lambda *s: torch.randn(*s, dtype=torch.bfloat16) * 0.8)(F) * 0.01),
            "w2": _w((lambda *s: torch.randn(*s, dtype=torch.bfloat16) * 0.8)(F, D) * (F ** -0.5)),
            "b2": _w((lambda *s: torch.randn(*s, dtype=torch.bfloat16) * 0.8)(D) * 0.01),
        }
    _B_in = _B_setup()

    return {
        "A_x": _A_in["x"],
        "A_wqkv": _A_in["wqkv"],
        "A_wo": _A_in["wo"],
        "B_x": _B_in["x"],
        "B_w1": _B_in["w1"],
        "B_b1": _B_in["b1"],
        "B_w2": _B_in["w2"],
        "B_b2": _B_in["b2"],
    }

def reference(A_x, A_wqkv, A_wo, B_x, B_w1, B_b1, B_w2, B_b2):
    def _A_ref(x, wqkv, wo):
        B, S, D = x.shape
        H = 32; Dh = D // H
        W, dil = 8, 2
        qkv = (lambda x, *d: x.permute(*d))((x @ wqkv).reshape(B, S, 3, H, Dh), 2, 0, 3, 1, 4)
        q, k, v = qkv[0], qkv[1], qkv[2]
        scale = Dh ** -0.5
        pos = torch.arange(S)
        off = torch.arange(W) * dil
        idx = pos[:, None] - off[None, :]
        mask = idx >= 0
        idx_c = torch.where(mask, idx, 0).reshape(-1)
        kg = k[:, :, idx_c, :].reshape(B, H, S, W, Dh)
        vg = v[:, :, idx_c, :].reshape(B, H, S, W, Dh)
        logits = torch.einsum("bhsd,bhswd->bhsw", q, kg) * scale
        logits = torch.where(mask[None, None, :, :], logits, -1e9)
        attn = _softmax(logits, axis=-1)
        o = torch.einsum("bhsw,bhswd->bhsd", attn, vg)
        return (lambda x, *d: x.permute(*d))(o, 0, 2, 1, 3).reshape(B, S, D) @ wo

    def _B_ref(x, w1, b1, w2, b2):
        h = torch.einsum("bsd,df->bsf", x, w1) + b1
        h = _gelu(h)
        return torch.einsum("bsf,fd->bsd", h, w2) + b2

    x = A_x
    x = x + _A_ref(_layernorm(x, axis=-1), A_wqkv, A_wo)
    x = x + _B_ref(_layernorm(x, axis=-1), B_w1, B_b1, B_w2, B_b2)
    x = x + _A_ref(_layernorm(x, axis=-1), A_wqkv, A_wo)
    x = x + _B_ref(_layernorm(x, axis=-1), B_w1, B_b1, B_w2, B_b2)
    return x
```

```cpp
#include <hip/hip_runtime.h>
#include <hip/hip_bf16.h>
typedef unsigned short u16;
__device__ __forceinline__ float bf2f(u16 b){ return __uint_as_float(((unsigned)b)<<16); }
__device__ __forceinline__ u16 f2bf(float f){ unsigned u=__float_as_uint(f); u += 0x7FFFu + ((u>>16)&1u); return (u16)(u>>16); }
constexpr int S=2048, D=576, F=4608, H=32, DH=18;
__global__ void ln_k(const u16* x, u16* y){
  int r=blockIdx.x; int t=threadIdx.x;
  float v[9]; float s=0;
  for(int i=0;i<9;i++){ v[i]=bf2f(x[r*D+t+64*i]); s+=v[i]; }
  for(int o=32;o;o>>=1) s+=__shfl_xor(s,o);
  float m=s/D; float q=0;
  for(int i=0;i<9;i++){ float d=v[i]-m; q+=d*d; }
  for(int o=32;o;o>>=1) q+=__shfl_xor(q,o);
  float rs=rsqrtf(q/D+1e-5f);
  for(int i=0;i<9;i++) y[r*D+t+64*i]=f2bf((v[i]-m)*rs);
}
template<int MODE> __global__ void gemm_k(const u16* A,const u16* B,const u16* bias,const u16* res,u16* C,int N,int K){
  __shared__ float As[16][33]; __shared__ float Bs[32][17];
  int tx=threadIdx.x&15, ty=threadIdx.x>>4; int r=blockIdx.y*16+ty, c=blockIdx.x*16+tx; float acc=0;
  for(int k0=0;k0<K;k0+=32){
    As[ty][tx]=bf2f(A[(size_t)r*K+k0+tx]); As[ty][tx+16]=bf2f(A[(size_t)r*K+k0+tx+16]);
    Bs[ty][tx]=bf2f(B[(size_t)(k0+ty)*N+c]); Bs[ty+16][tx]=bf2f(B[(size_t)(k0+ty+16)*N+c]);
    __syncthreads();
    for(int k=0;k<32;k++) acc+=As[ty][k]*Bs[k][tx];
    __syncthreads();
  }
  if(MODE&1) acc+=bf2f(bias[c]);
  if(MODE&2){ float u=acc; float t=tanhf(0.7978845608028654f*(u+0.044715f*u*u*u)); acc=0.5f*u*(1.f+t);}
  if(MODE&4) acc+=bf2f(res[(size_t)r*N+c]);
  C[(size_t)r*N+c]=f2bf(acc);
}
__global__ void attn_k(const u16* qkv,u16* o){
  int id=blockIdx.x*blockDim.x+threadIdx.x; int s=id/H, h=id%H;
  float q[DH]; for(int d=0;d<DH;d++) q[d]=bf2f(qkv[(size_t)s*3*D+h*DH+d]);
  float l[8]; float mx=-1e30f;
  for(int w=0;w<8;w++){ int p=s-2*w; float a=-1e9f; if(p>=0){ a=0; for(int d=0;d<DH;d++) a+=q[d]*bf2f(qkv[(size_t)p*3*D+D+h*DH+d]); a*=0.23570226039551584f;} l[w]=a; mx=fmaxf(mx,a);}
  float sum=0; for(int w=0;w<8;w++){ l[w]=__expf(l[w]-mx); sum+=l[w]; }
  float inv=1.f/sum;
  for(int d=0;d<DH;d++){ float a=0; for(int w=0;w<8;w++){ int p=s-2*w; if(p<0)p=0; a+=l[w]*bf2f(qkv[(size_t)p*3*D+2*D+h*DH+d]); } o[(size_t)s*D+h*DH+d]=f2bf(a*inv);}
}
extern "C" void kernel_launch(void* const* d_in, const int* in_sizes, int n_in, void* d_out, int out_size, void* d_ws, size_t ws_size, hipStream_t stream){
  const u16* x=(const u16*)d_in[0]; const u16* wqkv=(const u16*)d_in[1]; const u16* wo=(const u16*)d_in[2];
  const u16* w1=(const u16*)d_in[4]; const u16* b1=(const u16*)d_in[5]; const u16* w2=(const u16*)d_in[6]; const u16* b2=(const u16*)d_in[7];
  u16* ws=(u16*)d_ws; u16* xn=ws; u16* qkv=xn+S*D; u16* o=qkv+S*3*D; u16* x1=o+S*D; u16* h=x1+S*D; u16* x2=h+(size_t)S*F;
  const u16* cur=x;
  for(int L=0;L<2;L++){
    ln_k<<<S,64,0,stream>>>(cur,xn);
    gemm_k<0><<<dim3(3*D/16,S/16),256,0,stream>>>(xn,wqkv,nullptr,nullptr,qkv,3*D,D);
    attn_k<<<S*H/256,256,0,stream>>>(qkv,o);
    gemm_k<4><<<dim3(D/16,S/16),256,0,stream>>>(o,wo,nullptr,cur,x1,D,D);
    ln_k<<<S,64,0,stream>>>(x1,xn);
    gemm_k<3><<<dim3(F/16,S/16),256,0,stream>>>(xn,w1,b1,nullptr,h,F,D);
    u16* dst = L==0? x2 : (u16*)d_out;
    gemm_k<5><<<dim3(D/16,S/16),256,0,stream>>>(h,w2,b2,x1,dst,D,F);
    cur=x2;
  }
}
```

```cpp
#include <hip/hip_runtime.h>
#include <hip/hip_bf16.h>
#include <type_traits>

#define GAS __attribute__((address_space(1)))
#define LAS __attribute__((address_space(3)))
typedef unsigned short u16;
typedef unsigned v4u __attribute__((ext_vector_type(4)));
typedef unsigned v2u __attribute__((ext_vector_type(2)));
typedef float f32x4 __attribute__((ext_vector_type(4)));
typedef float f32x2 __attribute__((ext_vector_type(2)));
typedef short bf16x8 __attribute__((ext_vector_type(8)));
typedef __bf16 bf16x2 __attribute__((ext_vector_type(2)));
typedef GAS unsigned gu32;
typedef unsigned long long u64;
typedef GAS u64 gu64;
typedef __amdgpu_buffer_rsrc_t rsrc_t;
#define RLX_AGENT __ATOMIC_RELAXED, __HIP_MEMORY_SCOPE_AGENT

#define TRICKLE 4
#define G2PK 0
#define GSPREAD 4

constexpr int S_ = 2048, D_ = 576, F_ = 4608;
constexpr int REV = 18 * 1024;
constexpr int XBC = 4 * REV;
constexpr int XBSZ = 32 * XBC;
constexpr int FL_OFF = 0;
constexpr int WQ_OFF = 131072;
constexpr int WO_OFF = WQ_OFF + 128 * REV;
constexpr int MS_OFF = WO_OFF + 36 * REV;
constexpr int MSJ = 72 * REV;
constexpr int CSP_OFF = MS_OFF + 8 * MSJ;
constexpr int X2_OFF = CSP_OFF + 8 * 18 * 2304 + 4096 - (8 * 18 * 2304) % 4096;
constexpr int OB_OFF = X2_OFF + XBSZ;
constexpr int X1_OFF = OB_OFF + 2 * XBSZ;
constexpr int PB_OFF = X1_OFF + 2 * XBSZ;
constexpr int PBSZ = 32 * 8 * 8 * 10240;
constexpr int ST_OFF = PB_OFF + 2 * PBSZ;
constexpr int ST2_OFF = ST_OFF + 256 * 64 * 8;
constexpr int WS_BYTES = ST2_OFF + 2 * 256 * 8 * 64 * 8;
constexpr int L_X = 0, L_H = 73728, L_ATT = 92160, ATT_STR = 10240, A_Q = 0, A_K = 2560, A_V = 5120, A_E = 7680;
constexpr int L_RES = 137216, L_OWN = 147456, L_CS1 = L_OWN + 8192, L_B1 = L_CS1 + 2304, L_STR = L_B1 + 2304,
              L_STM = L_STR + 320, L_B2 = L_STM + 320, L_W = L_B2 + 320, L_END = L_W + 64;
static_assert(L_END <= 163840, "lds");
constexpr int LDS_BYTES = 163840;

__device__ __forceinline__ float bf2f(u16 b) { return __uint_as_float(((unsigned)b) << 16); }
__device__ __forceinline__ float bflo(unsigned u) { return __uint_as_float(u << 16); }
__device__ __forceinline__ float bfhi(unsigned u) { return __uint_as_float(u & 0xffff0000u); }
__device__ __forceinline__ unsigned pk2(float a, float b) {
  f32x2 v = {a, b};
  return __builtin_bit_cast(unsigned, __builtin_convertvector(v, bf16x2));
}
__device__ __forceinline__ v2u pk4(f32x4 v) { return v2u{pk2(v[0], v[1]), pk2(v[2], v[3])}; }
__device__ __forceinline__ f32x4 unpk4(unsigned a, unsigned b) { return f32x4{bflo(a), bfhi(a), bflo(b), bfhi(b)}; }
__device__ __forceinline__ f32x4 mfma(v4u a, v4u b, f32x4 c) {
  return __builtin_amdgcn_mfma_f32_16x16x32_bf16(__builtin_bit_cast(bf16x8, a), __builtin_bit_cast(bf16x8, b), c, 0, 0, 0);
}
__device__ __forceinline__ v4u ldw(rsrc_t r, int voff, int soff) { return __builtin_amdgcn_raw_buffer_load_b128(r, voff, soff, 0); }
__device__ __forceinline__ v4u ldc(rsrc_t r, int voff, int soff) { return __builtin_amdgcn_raw_buffer_load_b128(r, voff, soff, 16); }
__device__ __forceinline__ void stc128(v4u v, rsrc_t r, int voff, int soff) { __builtin_amdgcn_raw_buffer_store_b128(v, r, voff, soff, 16); }
__device__ __forceinline__ void stc64(v2u v, rsrc_t r, int voff, int soff) { __builtin_amdgcn_raw_buffer_store_b64(v, r, voff, soff, 16); }
__device__ __forceinline__ void stc32(unsigned v, rsrc_t r, int voff, int soff) { __builtin_amdgcn_raw_buffer_store_b32(v, r, voff, soff, 16); }
#define SB() __builtin_amdgcn_sched_barrier(0)
__device__ __forceinline__ void bar() {
  asm volatile("s_waitcnt lgkmcnt(0)" ::: "memory");
  __builtin_amdgcn_s_barrier();
  asm volatile("" ::: "memory");
}
__device__ __forceinline__ float red16max(float x) {
  auto r = __builtin_amdgcn_permlane16_swap(__float_as_uint(x), __float_as_uint(x), false, false);
  x = fmaxf(__uint_as_float(r[0]), __uint_as_float(r[1]));
  auto q = __builtin_amdgcn_permlane32_swap(__float_as_uint(x), __float_as_uint(x), false, false);
  return fmaxf(__uint_as_float(q[0]), __uint_as_float(q[1]));
}
__device__ __forceinline__ float red16sum(float x) {
  auto r = __builtin_amdgcn_permlane16_swap(__float_as_uint(x), __float_as_uint(x), false, false);
  x = __uint_as_float(r[0]) + __uint_as_float(r[1]);
  auto q = __builtin_amdgcn_permlane32_swap(__float_as_uint(x), __float_as_uint(x), false, false);
  return __uint_as_float(q[0]) + __uint_as_float(q[1]);
}
__device__ __forceinline__ int ms_wave(int w) { return 162 * 1024 * w; }
__device__ __forceinline__ int kperm(int s, int g, int e) { return 32 * s + 4 * g + (e & 3) + ((e >> 2) << 4); }

__device__ __forceinline__ int ms1(int T, int s) {
  if (T < 16) return 162 * (T >> 2) + 4 * s + (T & 3);
  if (T < 32) return 162 * (4 + (((T - 16) >> 1) & 3)) + 5 * s + (T & 1) + (T >= 24 ? 2 : 0);
  return 162 * (T - 28) + 5 * s + 4;
}
__device__ __forceinline__ int ms2(int j, int T, int s) {
  const int d = T >= 32 ? j : T >> 2, i = T >= 32 ? T - 32 : T & 3;
  if (T < 32 && d == j) return 162 * i + 144 + s;
  const int r = i >> 1, w = 4 * r + (d >> 1), t = ((d & 1) << 1) | (i & 1);
  if (r) return 162 * w + 90 + 36 * (t >> 1) + (t & 1) + 2 * s;
  return 162 * w + (s < 8 ? 72 + 4 * s + t : 88 + 20 * (t >> 1) + (t & 1) + 2 * s);
}

#define STAMP(i) do {} while (0)
#define CST(i) do {} while (0)

__device__ __forceinline__ gu64* flagp(char* ws, int line, int i) { return (gu64*)(ws + FL_OFF + 256 + line * 256 + i * 8); }
__device__ __forceinline__ void publish(char* ws, int line, int i, u64 tag, int tid) {
  asm volatile("s_waitcnt vmcnt(0)" ::: "memory");
  bar();
  if (tid == 0) __hip_atomic_store(flagp(ws, line, i), tag, RLX_AGENT);
}
template <int SL> __device__ __forceinline__ void poll(gu64* p, u64 tag) {
  for (unsigned spins = 0; spins < (1u << 22); ++spins) {
    const u64 v = __hip_atomic_load(p, RLX_AGENT);
    if (__all(v == tag)) break;
    if (SL) __builtin_amdgcn_s_sleep(SL);
  }
  __builtin_amdgcn_fence(__ATOMIC_ACQUIRE, "wavefront");
}
template <int SL> __device__ __forceinline__ void poll8(char* ws, int ph, int c, int c2, u64 tag, int lane) {
  poll<SL>(flagp(ws, ph * 32 + ((lane & 8) ? c2 : c), lane & 7), tag);
}
__device__ __forceinline__ void await(char* ws, int ph, int c, int c2, u64 tag, int w, int lane) {
  if (w == 0) poll8<0>(ws, ph, c, c2, tag, lane);
  bar();
}

template <bool RAW> __device__ __forceinline__ void stage_put(v4u (&x)[18], LAS char* L, int t5, bool ones_row, int lane) {
  float s1 = 0.f, s2 = 0.f;
  const bf16x2 ones = __builtin_bit_cast(bf16x2, 0x3f803f80u);
  const int g = lane >> 4, m = lane & 15;
  LAS char* dst = L + L_X + t5 * REV + (RAW ? ((g & 1) * 32 + m) * 16 + (g >> 1) * 8 : lane * 16);
#pragma unroll
  for (int s = 0; s < 18; ++s) {
    if (RAW) { *(LAS v2u*)(dst + s * 1024) = v2u{x[s][0], x[s][1]}; *(LAS v2u*)(dst + s * 1024 + 256) = v2u{x[s][2], x[s][3]}; }
    else *(LAS v4u*)(dst + s * 1024) = x[s];
#pragma unroll
    for (int i = 0; i < 4; ++i) {
      const unsigned u = x[s][i];
      const bf16x2 v = __builtin_bit_cast(bf16x2, u);
      s1 = __builtin_amdgcn_fdot2_f32_bf16(v, ones, s1, false);
      s2 = __builtin_amdgcn_fdot2_f32_bf16(v, v, s2, false);
    }
    SB();
  }
  if (ones_row && m == 0) {
#pragma unroll
    for (int s = 0; s < 18; ++s) *(LAS v4u*)(L + L_X + s * 1024 + lane * 16) = v4u{0x3f803f80u, 0x3f803f80u, 0x3f803f80u, 0x3f803f80u};
  }
  s1 = red16sum(s1); s2 = red16sum(s2);
  const float mean = s1 * (1.0f / D_);
  const float var = fmaxf(s2 * (1.0f / D_) - mean * mean, 0.f);
  const float rstd = __builtin_amdgcn_rsqf(var + 1e-5f);
  if (lane < 16) {
    *(LAS float*)(L + L_STR + t5 * 64 + lane * 4) = rstd;
    *(LAS float*)(L + L_STM + t5 * 64 + lane * 4) = mean * rstd;
  }
}
__device__ __forceinline__ void stage_tile(rsrc_t rs, int soff, LAS char* L, int t5, bool ones_row, int lane) {
  v4u x[18];
#pragma unroll
  for (int s = 0; s < 18; ++s) x[s] = ldc(rs, lane * 16 + (s & 3) * 1024, soff + (s >> 2) * 4096);
  SB();
  stage_put<false>(x, L, t5, ones_row, lane);
}

#define RING 28
constexpr int R_ = RING;
constexpr int RB = 256 - 4 * R_;
template <int SLOT, int OFF> __device__ __forceinline__ void rload(v4u rs, int voff, int so) {
  asm volatile("buffer_load_dwordx4 v[%c3:%c4], %0, %1, %2 offen offset:%c5 sc1"
               :: "v"(voff), "s"(rs), "s"(so), "i"(RB + 4 * SLOT), "i"(RB + 4 * SLOT + 3), "i"(OFF));
}
template <int N> __device__ __forceinline__ void rwait() { asm volatile("s_waitcnt vmcnt(%c0)" :: "i"(N)); }
template <int SLOT> __device__ __forceinline__ void rmfma(f32x4& acc, v4u x) {
  asm volatile("v_mfma_f32_16x16x32_bf16 %0, v[%c2:%c3], %1, %0" : "+v"(acc) : "v"(x), "i"(RB + 4 * SLOT), "i"(RB + 4 * SLOT + 3));
}
template <int SLOT> __device__ __forceinline__ void rmfma0(f32x4& acc, v4u x) {
  asm volatile("v_mfma_f32_16x16x32_bf16 %0, v[%c2:%c3], %1, 0" : "=&v"(acc) : "v"(x), "i"(RB + 4 * SLOT), "i"(RB + 4 * SLOT + 3));
}
template <int I, int N, class Fn> __device__ __forceinline__ void sfor(Fn&& f) {
  if constexpr (I < N) { f(std::integral_constant<int, I>{}); sfor<I + 1, N>(f); }
}
constexpr int cmin(int a, int b) { return a < b ? a : b; }
template <int P, int TOTAL> __device__ __forceinline__ void refill(v4u r, int mb, int voff) {
  constexpr int Q = P + R_;
  if constexpr (Q < TOTAL) rload<P % R_, (Q & 3) * 1024>(r, voff, mb + (Q >> 2) * 4096);
}
struct NoHook { template <int S> static constexpr int extra() { return 0; } template <int S> __device__ __forceinline__ void at() {} };
template <int NT, int NK, int S0, int S1, int P0, int TOTAL, bool INIT, class Hook = NoHook>
__device__ __forceinline__ void seg(f32x4* const (&acc)[NT], const LAS char* xb, v4u r, int mb, int voff, Hook hook = Hook{}) {
  constexpr int XD = NT >= 4 ? 1 : (NT == 1 ? 4 : 2);
  v4u xf[XD][NK];
#pragma unroll
  for (int d = 0; d < XD; ++d)
#pragma unroll
    for (int tt = 0; tt < NK; ++tt)
      if (S0 + d < S1) xf[d][tt] = *(const LAS v4u*)(xb + tt * REV + (S0 + d) * 1024);
  SB();
  asm volatile("s_nop 1");
  sfor<S0, S1>([&](auto sc) {
    constexpr int s = sc.value, p = P0 + (s - S0) * NT, d = (s - S0) % XD;
    constexpr int issued = cmin(TOTAL, R_ + (s == S0 ? P0 : p - NT));
    rwait<issued - (p + NT) + Hook::template extra<s>()>();
    hook.template at<s>();
    sfor<0, NK>([&](auto kc) {
      constexpr int tt = kc.value;
      sfor<0, NT>([&](auto tc) {
        constexpr int t = tc.value;
        if constexpr (INIT && s == S0) rmfma0<(p + t) % R_>(acc[t][tt], xf[d][tt]);
        else rmfma<(p + t) % R_>(acc[t][tt], xf[d][tt]);
        constexpr int idx = tt * NT + t;
        if constexpr (s > S0 && idx % NK == 0 && idx / NK < NT) refill<p - NT + idx / NK, TOTAL>(r, mb, voff);
      });
      SB();
      if constexpr (s + XD < S1) { xf[d][tt] = *(const LAS v4u*)(xb + tt * REV + (s + XD) * 1024); SB(); }
    });
  });
  sfor<0, NT>([&](auto tc) { refill<P0 + (S1 - 1 - S0) * NT + tc.value, TOTAL>(r, mb, voff); });
  asm volatile("s_nop 7\n\ts_nop 1");
  SB();
}
template <int LO> __device__ __forceinline__ void fillh(v4u r, int n, int voff) {
  sfor<LO ? R_ / 2 : 0, LO ? R_ : R_ / 2>([&](auto sc) { rload<sc.value, (sc.value & 3) * 1024>(r, voff, n + (sc.value >> 2) * 4096); });
  SB();
}
__device__ __forceinline__ v2u trread(const LAS char* p) {
  typedef short s4 __attribute__((ext_vector_type(4)));
  return __builtin_bit_cast(v2u, __builtin_amdgcn_ds_read_tr16_b64_v4i16((LAS s4*)p));
}
constexpr float AL = 0.46866869f, KK = 2.3022082f / AL;
__device__ __forceinline__ float sfma(float a, float b, float c) { float d; asm("v_fma_f32 %0, %1, %2, %3" : "=v"(d) : "v"(a), "v"(b), "v"(c)); return d; }
__device__ __forceinline__ float sfnma(float a, float b, float c) { float d; asm("v_fma_f32 %0, -%1, %2, %3" : "=v"(d) : "v"(a), "v"(b), "v"(c)); return d; }
__device__ __forceinline__ float sfmas(float a, float s) { float d; asm("v_fma_f32 %0, %1, %2, %2" : "=v"(d) : "v"(a), "s"(s)); return d; }
__device__ __forceinline__ float ssqs(float a, float s) { float d; asm("v_fma_f32 %0, %1, %1, %2" : "=v"(d) : "v"(a), "s"(s)); return d; }
__device__ __forceinline__ float smul(float a, float b) { float d; asm("v_mul_f32 %0, %1, %2" : "=v"(d) : "v"(a), "v"(b)); return d; }
#define EACH16 _Pragma("unroll") for (int tt = 0; tt < 4; ++tt) _Pragma("unroll") for (int r = 0; r < 4; ++r)
__device__ __forceinline__ void gelu16(f32x4 (&a)[4], const float (&rstd)[4], const float (&mr)[4], f32x4 cs, f32x4 bb, v2u (&o)[4]) {
  float e[4][4], u[4][4];
  EACH16 e[tt][r] = sfnma(cs[r], mr[tt], bb[r]);
  SB();
  EACH16 u[tt][r] = sfma(a[tt][r], rstd[tt], e[tt][r]);
  SB();
  EACH16 e[tt][r] = ssqs(u[tt][r], KK);
  SB();
  EACH16 e[tt][r] = smul(e[tt][r], u[tt][r]);
  SB();
  EACH16 e[tt][r] = __builtin_amdgcn_exp2f(-e[tt][r]);
  SB();
  EACH16 e[tt][r] = sfmas(e[tt][r], AL);
  SB();
  EACH16 e[tt][r] = __builtin_amdgcn_rcpf(e[tt][r]);
  SB();
  EACH16 u[tt][r] = smul(u[tt][r], e[tt][r]);
  SB();
#pragma unroll
  for (int tt = 0; tt < 4; ++tt) o[tt] = v2u{pk2(u[tt][0], u[tt][1]), pk2(u[tt][2], u[tt][3])};
  SB();
}
__device__ __forceinline__ void gelu16p(f32x4 (&a)[4], const float (&rstd)[4], const float (&mr)[4], f32x4 cs, f32x4 bb, v2u (&o)[4]) {
  f32x4 e[4];
#pragma unroll
  for (int tt = 0; tt < 4; ++tt) e[tt] = bb - cs * mr[tt];
  SB();
#pragma unroll
  for (int tt = 0; tt < 4; ++tt) a[tt] = a[tt] * rstd[tt] + e[tt];
  SB();
#pragma unroll
  for (int tt = 0; tt < 4; ++tt) e[tt] = a[tt] * a[tt] + KK;
  SB();
#pragma unroll
  for (int tt = 0; tt < 4; ++tt) e[tt] = e[tt] * a[tt];
  SB();
  EACH16 e[tt][r] = __builtin_amdgcn_exp2f(-e[tt][r]);
  SB();
#pragma unroll
  for (int tt = 0; tt < 4; ++tt) e[tt] = e[tt] * AL + AL;
  SB();
  EACH16 e[tt][r] = __builtin_amdgcn_rcpf(e[tt][r]);
  SB();
#pragma unroll
  for (int tt = 0; tt < 4; ++tt) a[tt] = a[tt] * e[tt];
  SB();
#pragma unroll
  for (int tt = 0; tt < 4; ++tt) o[tt] = pk4(a[tt]);
  SB();
}
struct StoreHook {
  v4u d[4]; rsrc_t rs; int voff, po0, po1;
  template <int S> static constexpr int extra() { return S <= 6 ? 0 : S <= 9 ? 1 : S <= 12 ? 2 : S <= 15 ? 3 : 4; }
  template <int S> __device__ __forceinline__ void at() {
    if constexpr (S == 6 || S == 9 || S == 12 || S == 15) {
      constexpr int q = (S - 6) / 3;
      SB(); stc128(d[q], rs, voff + (q & 1) * 1024, q < 2 ? po0 : po1); SB();
    }
  }
};
template <int SL> __device__ __forceinline__ void lds_wait_ge(LAS char* L, int word, unsigned want) {
  volatile LAS unsigned* p = (volatile LAS unsigned*)(L + L_W + word * 4);
  for (unsigned spins = 0; spins < (1u << 22); ++spins) {
    if (__builtin_amdgcn_readfirstlane(*p) >= want) break;
    __builtin_amdgcn_s_sleep(SL);
  }
  asm volatile("" ::: "memory");
}
__device__ __forceinline__ void arrive4(char* ws, LAS char* L, int word, int ph, int c, int j, u64 tag, int lane) {
  asm volatile("s_waitcnt vmcnt(0)" ::: "memory");
  if (lane == 0) {
    const unsigned old = __hip_atomic_fetch_add((LAS unsigned*)(L + L_W + word * 4), 1u, __ATOMIC_RELAXED, __HIP_MEMORY_SCOPE_WORKGROUP);
    if ((old & 3u) == 3u) __hip_atomic_store(flagp(ws, ph * 32 + c, j), tag, RLX_AGENT);
  }
}
__device__ __forceinline__ float sadd(float a, float b) { float d; asm("v_add_f32 %0, %1, %2" : "=v"(d) : "v"(a), "v"(b)); return d; }
__device__ __forceinline__ void acc8(f32x4& a, f32x4& b, v4u p) {
  a[0] = sadd(a[0], bflo(p[0])); a[1] = sadd(a[1], bfhi(p[0])); a[2] = sadd(a[2], bflo(p[1])); a[3] = sadd(a[3], bfhi(p[1]));
  b[0] = sadd(b[0], bflo(p[2])); b[1] = sadd(b[1], bfhi(p[2])); b[2] = sadd(b[2], bflo(p[3])); b[3] = sadd(b[3], bfhi(p[3]));
}

#define FRESH() int lane = lane0, c = c0, j = j0, w = w0; asm volatile("" : "+v"(lane)); asm volatile("" : "+s"(c), "+s"(j), "+s"(w)); \
  const int g = lane >> 4, m = lane & 15, voff = lane * 16, role = w >> 2, hl = w & 3; (void)g; (void)m; (void)voff; (void)role; (void)hl;

__device__ __forceinline__ void gather8(unsigned (&h)[8], const u16* src, int ld, int s, int g, bool omode) {
#pragma unroll
  for (int e = 0; e < 8; ++e) {
    int k;
    if (!omode) k = kperm(s, g, e);
    else if (s < 16) k = 18 * (2 * s + (e >> 2)) + 4 * g + (e & 3);
    else k = 18 * (4 * (4 * (s - 16) + g) + (e >> 1)) + 16 + (e & 1);
    h[e] = src[k * ld];
  }
}
__device__ __forceinline__ v4u pack8(const unsigned (&h)[8]) { return v4u{h[0] | (h[1] << 16), h[2] | (h[3] << 16), h[4] | (h[5] << 16), h[6] | (h[7] << 16)}; }

__global__ __launch_bounds__(512, 2) __attribute__((amdgpu_num_vgpr(RB / 2)))
void k_main(char* __restrict__ ws, const u16* __restrict__ xin, const u16* __restrict__ wqkv, const u16* __restrict__ wo,
            const u16* __restrict__ w1, const u16* __restrict__ b1, const u16* __restrict__ w2, const u16* __restrict__ b2,
            u16* __restrict__ out) {
  extern __shared__ __attribute__((aligned(16))) char smem[];
  LAS char* L = (LAS char*)smem;
  const int tid = threadIdx.x, lane0 = tid & 63, w0 = __builtin_amdgcn_readfirstlane(tid >> 6);
  int c0, j0;
  {
    constexpr int G = GSPREAD, per = 8 / G;
    const int b = blockIdx.x, x = b & 7, r = b >> 3;
    j0 = (x % G) * per + (r % per);
    c0 = (x / G) * (4 * G) + r / per;
  }
  const rsrc_t rs = __builtin_amdgcn_make_buffer_rsrc((void*)ws, 0, WS_BYTES, 0x00020000);
  const v4u rw = {(unsigned)(size_t)ws, (unsigned)((size_t)ws >> 32) & 0xffffu, (unsigned)WS_BYTES, 0x00020000u};
  asm volatile("" ::: "v255");
  STAMP(0);
  u64 tag;
  {
    FRESH();
    const u64 gen = __hip_atomic_load((gu64*)(ws + FL_OFF), RLX_AGENT);
    auto qsrc = [&](int f, int& s, int& dst, bool& valid) {
      s = f >> 4;
      const int t16 = f & 15, h = 4 * j + (t16 >> 2), part = t16 & 3;
      valid = part < 3 || m < 6;
      const int col = part < 3 ? part * D_ + 18 * h + m : (valid ? (m >> 1) * D_ + 18 * h + 16 + (m & 1) : 0);
      dst = WQ_OFF + (h * 2 + (part >> 1)) * 2 * REV + (2 * s + (part & 1)) * 1024;
      return wqkv + col;
    };
    auto jload = [&](int job, v4u (&r)[4]) {
      const int q = job < 162 ? job : job - 162, s = q / 9, B = q - 9 * s;
      const u16* src = job < 162 ? w1 + (size_t)(32 * s) * F_ + 576 * j + 64 * B : w2 + (size_t)(576 * j + 32 * s) * D_ + 64 * B;
      const int ld = job < 162 ? F_ : D_;
#pragma unroll
      for (int i = 0; i < 4; ++i) r[i] = *(const v4u*)(src + (8 * i + (lane >> 3)) * ld + (lane & 7) * 8);
    };
    auto jput = [&](int job, v4u (&r)[4]) {
      const int q = job < 162 ? job : job - 162, s = q / 9, B = q - 9 * s;
      LAS char* scr = L + L_ATT + w * 4096;
#pragma unroll
      for (int i = 0; i < 4; ++i) *(LAS v4u*)(scr + i * 1024 + voff) = r[i];
      const bf16x2 ones = __builtin_bit_cast(bf16x2, 0x3f803f80u);
      float csum = 0.f;
#pragma unroll
      for (int t = 0; t < 4; ++t) {
        const LAS char* p = scr + (4 * g + (m >> 2)) * 128 + t * 32 + (m & 3) * 8;
        const v2u lo = trread(p), hi = trread(p + 2048);
        const int T = 4 * B + t;
        stc128(v4u{lo[0], lo[1], hi[0], hi[1]}, rs, voff, MS_OFF + j * MSJ + (job < 162 ? ms1(T, s) : ms2(j, T, s)) * 1024);
        float a = 0.f;
        const unsigned u0 = lo[0], u1 = lo[1], u2 = hi[0], u3 = hi[1];
        a = __builtin_amdgcn_fdot2_f32_bf16(__builtin_bit_cast(bf16x2, u0), ones, a, false);
        a = __builtin_amdgcn_fdot2_f32_bf16(__builtin_bit_cast(bf16x2, u1), ones, a, false);
        a = __builtin_amdgcn_fdot2_f32_bf16(__builtin_bit_cast(bf16x2, u2), ones, a, false);
        a = __builtin_amdgcn_fdot2_f32_bf16(__builtin_bit_cast(bf16x2, u3), ones, a, false);
        a = red16sum(a);
        csum = g == t ? a : csum;
      }
      if (job < 162) stc32(__float_as_uint(csum), rs, lane * 4, CSP_OFF + (j * 18 + s) * 2304 + B * 256);
    };
    unsigned hq[8];
    int qs, qd; bool qv;
    { const u16* p = qsrc(c * 9 + w, qs, qd, qv); gather8(hq, p, 3 * D_, qs, g, false); }
    v4u jr[4];
    if (w < 5) {
      const int cc = (w == 4 && c >= 2) ? c - 2 : c, tt = w == 4 ? 3 : w;
      const u16* src = xin + (size_t)(2 * (64 * (cc >> 1) + 16 * tt + m) + (cc & 1)) * D_ + 8 * g;
      v4u x[18];
#pragma unroll
      for (int s = 0; s < 18; ++s) x[s] = *(const v4u*)(src + 32 * s);
      jload(c * 8 + w, jr);
      SB();
      stc128(qv ? pack8(hq) : v4u{0u, 0u, 0u, 0u}, rs, voff, qd);
      SB();
      stage_put<true>(x, L, w == 4 ? 0 : w + 1, w == 4, lane);
      tag = gen + 1;
      publish(ws, 320 + j, c, tag, tid);
      jput(c * 8 + w, jr);
    } else {
      unsigned hq2[8], ho[8];
      int qs2 = 0, qd2 = 0; bool qv2 = false;
      if (w == 7) { const u16* p = qsrc(c * 9 + 8, qs2, qd2, qv2); gather8(hq2, p, 3 * D_, qs2, g, false); }
      const int e3 = c * 3 + w - 5, n5 = j < 4 ? 5 : 4, os = e3 / n5, oi = e3 - os * n5, oT = oi < 4 ? 4 * j + oi : 32 + j;
      const bool hasO = os < 18, hasJ = e3 < 68;
      v4u jr2[4];
      jload(c * 8 + w, jr);
      if (hasJ) jload(256 + e3, jr2);
      if (hasO) gather8(ho, wo + 16 * oT + m, D_, os, g, true);
      const int t = tid - 320;
      float bv[3], b2v = 0.f;
#pragma unroll
      for (int i = 0; i < 3; ++i) bv[i] = bf2f(b1[576 * j + t + 192 * i]);
      if (t < 80) b2v = bf2f(b2[t < 64 ? 64 * j + t : 512 + 16 * (j & 3) + (t - 64)]);
      SB();
      stc128(qv ? pack8(hq) : v4u{0u, 0u, 0u, 0u}, rs, voff, qd);
      if (w == 7) stc128(qv2 ? pack8(hq2) : v4u{0u, 0u, 0u, 0u}, rs, voff, qd2);
      SB();
#pragma unroll
      for (int i = 0; i < 3; ++i) *(LAS float*)(L + L_B1 + (t + 192 * i) * 4) = AL * bv[i];
      if (t < 80) *(LAS float*)(L + L_B2 + t * 4) = b2v;
      if (t < 16) *(LAS unsigned*)(L + L_W + t * 4) = 0u;
      tag = gen + 1;
      publish(ws, 320 + j, c, tag, tid);
      jput(c * 8 + w, jr);
      if (hasJ) jput(256 + e3, jr2);
      if (hasO) stc128(pack8(ho), rs, voff, WO_OFF + oT * REV + os * 1024);
    }
    tag = ((u64)(unsigned)__builtin_amdgcn_readfirstlane((unsigned)(tag >> 32)) << 32) | (unsigned)__builtin_amdgcn_readfirstlane((unsigned)tag);
    STAMP(1);
    if (w == 0) poll<0>(flagp(ws, 320 + j, lane & 31), tag);
    bar();
    const int wq0 = WQ_OFF + ((4 * j + hl) * 4 + 2 * role) * REV;
    fillh<0>(rw, wq0, voff); fillh<1>(rw, wq0, voff);
  }

#pragma unroll 1
  for (int Ly = 0; Ly < 2; ++Ly) {
    bar();
    STAMP(2 + 16 * Ly);
    {
      FRESH();
      if (role == 0) {
#pragma unroll
        for (int i = 0; i < 5; ++i) {
          const int T = i < 4 ? 4 * j + i : 32 + (j & 3);
          v2u v = *(LAS v2u*)(L + L_X + (hl + 1) * REV + (T >> 1) * 1024 + voff + 8 * (T & 1));
          *(LAS v2u*)(L + L_RES + (i * 4 + hl) * 512 + lane * 8) = v;
        }
      }
      f32x4 aq[2][5];
      f32x4 (&a0)[5] = aq[0], (&a1)[5] = aq[1];
      CST(0 + 32 * Ly);
      seg<2, 5, 0, 18, 0, 36, true>({aq[0], aq[1]}, L + L_X + voff, rw, WQ_OFF + ((4 * j + hl) * 4 + 2 * role) * REV, voff);
      CST(1 + 32 * Ly);
      if (Ly) fillh<0>(rw, MS_OFF + j * MSJ + ms_wave(w), voff);
      LAS char* A = L + L_ATT + hl * ATT_STR + role * 5120 + (role ? m * 32 + g * 8 : lane * 8);
      f32x4 cs0, cs1;
#pragma unroll
      for (int r = 0; r < 4; ++r) {
        cs0[r] = __uint_as_float(__builtin_amdgcn_update_dpp(0u, __float_as_uint(a0[0][r]), 0x150, 0xf, 0xf, false));
        cs1[r] = __uint_as_float(__builtin_amdgcn_update_dpp(0u, __float_as_uint(a1[0][r]), 0x150, 0xf, 0xf, false));
      }
#pragma unroll
      for (int t = 0; t < 5; ++t) {
        const float rstd = *(LAS float*)(L + L_STR + t * 64 + m * 4), mr = *(LAS float*)(L + L_STM + t * 64 + m * 4);
        *(LAS v2u*)(A + t * 512) = pk4(a0[t] * rstd - cs0 * mr);
        *(LAS v2u*)(A + 2560 + t * 512) = pk4(a1[t] * rstd - cs1 * mr);
      }
    }
    CST(2 + 32 * Ly);
    if (Ly == 0) publish(ws, 328 + j0, c0, tag, tid); else bar();
    CST(3 + 32 * Ly);
    STAMP(3 + 16 * Ly);
    {
      FRESH();
      const LAS char* A = L + L_ATT + hl * ATT_STR + role * 1024;
      const float sc = 0.23570226039551584f * 1.4426950408889634f;
#pragma unroll
      for (int q = 0; q < 2; ++q) {
        const int t = 2 * role + q;
        const LAS char* At = A + q * 512;
        const v2u qm = *(const LAS v2u*)(At + A_Q + 512 + lane * 8);
        const v2u kpm = *(const LAS v2u*)(At + A_K + lane * 8), kcm = *(const LAS v2u*)(At + A_K + 512 + lane * 8);
        const v2u xp = *(const LAS v2u*)(At + A_E + m * 32), xc = *(const LAS v2u*)(At + A_E + 512 + m * 32);
        const v2u vp = trread(At + A_V + lane * 8), vc = trread(At + A_V + 512 + lane * 8);
        const v2u ep = trread(At + A_E + lane * 8), ec = trread(At + A_E + 512 + lane * 8);
        const v4u qf = {qm[0], qm[1], g == 0 ? xc[0] : 0u, 0u};
        const v4u kp = {kpm[0], kpm[1], g == 0 ? xp[1] : 0u, 0u}, kc = {kcm[0], kcm[1], g == 0 ? xc[1] : 0u, 0u};
        const f32x4 z = {0.f, 0.f, 0.f, 0.f};
        f32x4 sp = mfma(kp, qf, z), scur = mfma(kc, qf, z);
        const bool noprev = (c < 2) && (t == 0);
        float mx = -3.0e38f;
        bool okp[4], okc[4];
#pragma unroll
        for (int r = 0; r < 4; ++r) {
          const int kk = 4 * g + r;
          okp[r] = (kk >= m + 9) && !noprev;
          okc[r] = (kk <= m) && (kk >= m - 7);
          mx = fmaxf(mx, okp[r] ? sp[r] : -3.0e38f);
          mx = fmaxf(mx, okc[r] ? scur[r] : -3.0e38f);
        }
        mx = red16max(mx);
        float sum = 0.f;
#pragma unroll
        for (int r = 0; r < 4; ++r) {
          sp[r] = okp[r] ? __builtin_amdgcn_exp2f((sp[r] - mx) * sc) : 0.f;
          scur[r] = okc[r] ? __builtin_amdgcn_exp2f((scur[r] - mx) * sc) : 0.f;
          sum += sp[r] + scur[r];
        }
        sum = red16sum(sum);
        const float inv = __builtin_amdgcn_rcpf(sum);
        const v2u pp = pk4(sp * inv), pc = pk4(scur * inv);
        const v4u pf = {pp[0], pp[1], pc[0], pc[1]};
        const f32x4 o = mfma(v4u{vp[0], vp[1], vc[0], vc[1]}, pf, z);
        const f32x4 oe = mfma(v4u{ep[0], ep[1], ec[0], ec[1]}, pf, z);
        const int ob = OB_OFF + Ly * XBSZ + c * XBC + t * REV;
        stc64(pk4(o), rs, voff + 8 * (hl & 1), ob + (2 * j + (hl >> 1)) * 1024);
        if (g == 1) stc32(pk2(oe[0], oe[1]), rs, m * 16 + 4 * hl, ob + (16 + (j >> 2)) * 1024 + (j & 3) * 256);
      }
      CST(4 + 32 * Ly);
      STAMP(4 + 16 * Ly);
      publish(ws, (5 * Ly + 0) * 32 + c, j, tag, tid);
      if (Ly == 0) {
        if (w == 1) { poll<1>(flagp(ws, 328 + j, lane & 31), tag); *(volatile LAS unsigned*)(L + L_W + 32) = 1u; }
        else if (w) lds_wait_ge<1>(L, 8, 1u);
      }
      if (w) {
        const int nblk = j < 4 ? 90 : 72;
        v4u wv[13];
#pragma unroll
        for (int i = 0; i < 13; ++i) {
          const int b = min(w - 1 + 7 * i, nblk - 1);
          wv[i] = ldc(rs, voff, b < 72 ? WO_OFF + 4 * j * REV + b * 1024 : WO_OFF + (32 + j) * REV + (b - 72) * 1024);
        }
        SB();
#pragma unroll
        for (int i = 0; i < 13; ++i) *(LAS v4u*)(L + L_X + min(w - 1 + 7 * i, nblk - 1) * 1024 + voff) = wv[i];
      }
      await(ws, 5 * Ly + 0, c, c, tag, w, lane);
      if (Ly == 0 && blockIdx.x == 0 && tid == 0) __hip_atomic_store((gu64*)(ws + FL_OFF), tag, RLX_AGENT);
    }
    STAMP(5 + 16 * Ly);
    {
      FRESH();
      v4u of[9];
      const int ob = OB_OFF + Ly * XBSZ + c * XBC + hl * REV + role * 9 * 1024;
#pragma unroll
      for (int s = 0; s < 9; ++s) of[s] = ldc(rs, voff + (s & 3) * 1024, ob + (s >> 2) * 4096);
      SB();
      f32x4 acc[5];
      const LAS char* wb = L + L_X + role * 9 * 1024 + voff;
#pragma unroll
      for (int i = 0; i < 5; ++i) acc[i] = f32x4{0.f, 0.f, 0.f, 0.f};
#pragma unroll
      for (int s = 0; s < 9; ++s) {
#pragma unroll
        for (int i = 0; i < 4; ++i) acc[i] = mfma(*(const LAS v4u*)(wb + i * REV + s * 1024), of[s], acc[i]);
        if (s % 3 == 2) SB();
      }
      if (j < 4) {
#pragma unroll
        for (int s = 0; s < 9; ++s) acc[4] = mfma(*(const LAS v4u*)(wb + 4 * REV + s * 1024), of[s], acc[4]);
      }
      LAS char* R = L + L_ATT + hl * 5120 + voff;
      if (role == 1) {
#pragma unroll
        for (int i = 0; i < 5; ++i) *(LAS f32x4*)(R + i * 1024) = acc[i];
      }
      bar();
      if (role == 0) {
        v2u o[5];
#pragma unroll
        for (int i = 0; i < 5; ++i) {
          const v2u rv = *(LAS v2u*)(L + L_RES + (i * 4 + hl) * 512 + lane * 8);
          o[i] = pk4(acc[i] + *(LAS f32x4*)(R + i * 1024) + unpk4(rv[0], rv[1]));
        }
        const int xb = X1_OFF + Ly * XBSZ + c * XBC + hl * REV;
        stc128(v4u{o[0][0], o[0][1], o[1][0], o[1][1]}, rs, voff, xb + (2 * j) * 1024);
        stc128(v4u{o[2][0], o[2][1], o[3][0], o[3][1]}, rs, voff, xb + (2 * j + 1) * 1024);
        if (j < 4) stc64(o[4], rs, voff + 8 * (j & 1), xb + (16 + (j >> 1)) * 1024);
      }
      STAMP(6 + 16 * Ly);
      publish(ws, (5 * Ly + 1) * 32 + c, j, tag, tid);
      if (Ly == 0) {
        fillh<0>(rw, MS_OFF + j * MSJ + ms_wave(w), voff);
        if (w >= 5) {
          const int t = (w - 5) * 64 + lane;
#pragma unroll
          for (int i = 0; i < 3; ++i) {
            float v[18], a = 0.f;
#pragma unroll
            for (int s = 0; s < 18; ++s) v[s] = __uint_as_float(__builtin_amdgcn_raw_buffer_load_b32(rs, (t + 192 * i) * 4, CSP_OFF + (j * 18 + s) * 2304, 16));
#pragma unroll
            for (int s = 0; s < 18; ++s) a += v[s];
            *(LAS float*)(L + L_CS1 + (t + 192 * i) * 4) = a;
          }
        }
      }
      if (w >= 4) fillh<1>(rw, MS_OFF + j * MSJ + ms_wave(w), voff);
      await(ws, 5 * Ly + 1, c, c, tag, w, lane);
    }
    STAMP(7 + 16 * Ly);
    {
      FRESH();
      if (w < 4) { stage_tile(rs, X1_OFF + Ly * XBSZ + c * XBC + w * REV, L, w, false, lane); fillh<1>(rw, MS_OFF + j * MSJ + ms_wave(w), voff); }
    }
    bar();
    STAMP(8 + 16 * Ly);
    {
      FRESH();
      const int mb = MS_OFF + j * MSJ + ms_wave(w);
      const int pbc = PB_OFF + Ly * PBSZ + c * (64 * 10240);
      const int wq0 = WQ_OFF + ((4 * j + hl) * 4 + 2 * role) * REV;
      const int ui = 2 * role + (hl >> 1), up = hl & 1;
      const bool five = (j < 4) && ((j >> 1) == role) && (hl < 2);
      auto stats = [&](float (&rstd)[4], float (&mr)[4]) {
#pragma unroll
        for (int tt = 0; tt < 4; ++tt) { rstd[tt] = AL * *(LAS float*)(L + L_STR + tt * 64 + m * 4); mr[tt] = AL * *(LAS float*)(L + L_STM + tt * 64 + m * 4); }
      };
      auto gtile = [&](auto pkc, f32x4 (&x)[4], const float (&rstd)[4], const float (&mr)[4], int T, v2u (&o)[4]) {
        const f32x4 cs = *(LAS f32x4*)(L + L_CS1 + T * 64 + g * 16), bb = *(LAS f32x4*)(L + L_B1 + T * 64 + g * 16);
        if constexpr (pkc.value) gelu16p(x, rstd, mr, cs, bb, o); else gelu16(x, rstd, mr, cs, bb, o);
      };
      auto hpair = [&](auto pk0, auto pk1, f32x4 (&x)[4], f32x4 (&y)[4], const float (&rstd)[4], const float (&mr)[4], int T) {
        v2u oa[4], ob[4];
        gtile(pk0, x, rstd, mr, T, oa);
        gtile(pk1, y, rstd, mr, T + 1, ob);
#pragma unroll
        for (int tt = 0; tt < 4; ++tt) *(LAS v4u*)(L + L_H + tt * REV + (T >> 1) * 1024 + voff) = v4u{oa[tt][0], oa[tt][1], ob[tt][0], ob[tt][1]};
        SB();
      };
      auto poff = [&](int t) {
        const int d = 2 * hl + (t >> 1), i = 2 * role + (t & 1);
        return pbc + (((d != j ? d : i) * 8 + j) * 5 + (d != j ? i : 4)) * 2048;
      };
      auto ppack = [&](f32x4 (&x)[4], int p) { const v2u a = pk4(x[2 * p]), b = pk4(x[2 * p + 1]); return v4u{a[0], a[1], b[0], b[1]}; };
      auto pstore2 = [&](f32x4 (&x)[4], f32x4 (&y)[4], int t0, auto slc) {
#pragma unroll
        for (int q = 0; q < 4; ++q) {
          stc128(ppack(q < 2 ? x : y, q & 1), rs, voff + (q & 1) * 1024, poff(t0 + (q >> 1)));
          if (slc.value) { SB(); __builtin_amdgcn_s_sleep(slc.value); SB(); }
        }
        SB();
      };
      constexpr std::integral_constant<int, 0> I0{};
      constexpr std::integral_constant<int, 1> I1{};
      auto gather = [&](f32x4& sa, f32x4& sb, f32x4& ea, f32x4& eb) {
        v4u pv[7];
        const int po = pbc + (j * 8 * 5) * 2048 + up * 1024;
#pragma unroll
        for (int i = 0; i < 7; ++i) { const int src = i + (i >= j ? 1 : 0); pv[i] = ldc(rs, voff, po + (src * 5 + ui) * 2048); }
        SB();
        ea = *(LAS f32x4*)(L + L_B2 + 4 * 64 + g * 16); eb = ea;
        if (five) {
          v4u pe[8];
#pragma unroll
          for (int i = 0; i < 8; ++i) pe[i] = ldc(rs, voff, po + (i * 5 + 4) * 2048);
          SB();
#pragma unroll
          for (int i = 0; i < 8; ++i) acc8(ea, eb, pe[i]);
        }
        sa = *(LAS f32x4*)(L + L_B2 + ui * 64 + g * 16); sb = sa;
#pragma unroll
        for (int i = 0; i < 7; ++i) acc8(sa, sb, pv[i]);
      };
      auto finish = [&](f32x4 a, f32x4 b, int T) {
#pragma unroll
        for (int p = 0; p < 2; ++p) {
          const int tt = 2 * up + p;
          const int fo = tt * REV + (T >> 1) * 1024 + 8 * (T & 1);
          const v2u rv = *(LAS v2u*)(L + L_X + fo + voff);
          const v2u o = pk4((p ? b : a) + unpk4(rv[0], rv[1]));
          if (Ly == 0) stc64(o, rs, voff, X2_OFF + c * XBC + fo);
          else {
            const int tok = 2 * (64 * (c >> 1) + 16 * tt + m) + (c & 1);
            *(v2u*)(out + (size_t)tok * D_ + 16 * T + 4 * g) = o;
          }
        }
      };
      auto tail = [&](f32x4 sa, f32x4 sb, f32x4 ea, f32x4 eb) {
        if (five) finish(ea, eb, 32 + j);
        CST(19 + 32 * Ly);
        bar();
        const v2u oa = *(LAS v2u*)(L + L_OWN + (ui * 4 + 2 * up) * 512 + lane * 8), ob = *(LAS v2u*)(L + L_OWN + (ui * 4 + 2 * up + 1) * 512 + lane * 8);
        finish(sa + unpk4(oa[0], oa[1]), sb + unpk4(ob[0], ob[1]), 4 * j + ui);
      };
      CST(8 + 32 * Ly);
      if (role == 0) {
        __builtin_amdgcn_s_setprio(1);
        {
          f32x4 acc[4][4];
          seg<4, 4, 0, 18, 0, 162, true>({acc[0], acc[1], acc[2], acc[3]}, L + L_X + voff, rw, mb, voff);
          CST(9 + 32 * Ly);
          float rstd[4], mr[4];
          stats(rstd, mr);
          hpair(I0, I0, acc[0], acc[1], rstd, mr, 4 * hl);
          hpair(I0, I0, acc[2], acc[3], rstd, mr, 4 * hl + 2);
        }
        CST(10 + 32 * Ly);
        bar();
        CST(11 + 32 * Ly);
        {
          f32x4 acc[4][4];
          seg<4, 4, 0, 8, 72, 162, true>({acc[0], acc[1], acc[2], acc[3]}, L + L_H + voff, rw, mb, voff);
          CST(12 + 32 * Ly);
          bar();
          CST(13 + 32 * Ly);
          seg<2, 4, 8, 18, 104, 162, false>({acc[0], acc[1]}, L + L_H + voff, rw, mb, voff);
          pstore2(acc[0], acc[1], 0, std::integral_constant<int, TRICKLE>{});
          CST(20 + 32 * Ly);
          seg<2, 4, 8, 18, 124, 162, false>({acc[2], acc[3]}, L + L_H + voff, rw, mb, voff);
          __builtin_amdgcn_s_setprio(0);
          pstore2(acc[2], acc[3], 2, std::integral_constant<int, TRICKLE>{});
          CST(14 + 32 * Ly);
        }
        arrive4(ws, L, 0, 5 * Ly + 2, c, j, tag, lane);
        CST(15 + 32 * Ly);
        if (hl == 0) { poll8<4>(ws, 5 * Ly + 2, c, c, tag, lane); *(volatile LAS unsigned*)(L + L_W + 8) = Ly + 1; }
        else lds_wait_ge<6>(L, 2, Ly + 1);
        CST(16 + 32 * Ly);
        f32x4 sa, sb, ea, eb;
        gather(sa, sb, ea, eb);
        CST(17 + 32 * Ly);
        lds_wait_ge<3>(L, 4 + hl, Ly + 1);
        CST(18 + 32 * Ly);
        f32x4 own[4];
        seg<1, 4, 0, 18, 144, 162, true>({own}, L + L_H + voff, rw, mb, voff);
#pragma unroll
        for (int tt = 0; tt < 4; ++tt) *(LAS v2u*)(L + L_OWN + (hl * 4 + tt) * 512 + lane * 8) = pk4(own[tt]);
        if (Ly == 0) fillh<0>(rw, wq0, voff);
        tail(sa, sb, ea, eb);
      }
      int role2 = role;
      asm volatile("" : "+s"(role2));
      if (role2 != 0) {
        {
          f32x4 acc[5][4];
          seg<5, 4, 0, 18, 0, 162, true>({acc[0], acc[1], acc[2], acc[3], acc[4]}, L + L_X + voff, rw, mb, voff);
          CST(9 + 32 * Ly);
          bar();
          CST(11 + 32 * Ly);
          float rstd[4], mr[4];
          stats(rstd, mr);
          hpair(I0, std::integral_constant<int, G2PK>{}, acc[0], acc[1], rstd, mr, 16 + 2 * hl);
          CST(10 + 32 * Ly);
          hpair(I1, I1, acc[2], acc[3], rstd, mr, 24 + 2 * hl);
          v2u o[4];
          gtile(I1, acc[4], rstd, mr, 32 + hl, o);
#pragma unroll
          for (int tt = 0; tt < 4; ++tt) *(LAS v2u*)(L + L_H + tt * REV + (16 + (hl >> 1)) * 1024 + voff + 8 * (hl & 1)) = o[tt];
          CST(12 + 32 * Ly);
        }
        bar();
        CST(13 + 32 * Ly);
        {
          f32x4 acc[2][4];
          seg<2, 4, 0, 18, 90, 162, true>({acc[0], acc[1]}, L + L_H + voff, rw, mb, voff);
          CST(20 + 32 * Ly);
          __builtin_amdgcn_s_setprio(1);
          StoreHook hk{{ppack(acc[0], 0), ppack(acc[0], 1), ppack(acc[1], 0), ppack(acc[1], 1)}, rs, voff, poff(0), poff(1)};
          seg<2, 4, 0, 18, 126, 162, true>({acc[0], acc[1]}, L + L_H + voff, rw, mb, voff, hk);
          *(volatile LAS unsigned*)(L + L_W + 16 + hl * 4) = Ly + 1;
          CST(23 + 32 * Ly);
          pstore2(acc[0], acc[1], 2, I0);
          CST(14 + 32 * Ly);
        }
        arrive4(ws, L, 1, 5 * Ly + 3, c, j, tag, lane);
        CST(15 + 32 * Ly);
        if (hl == 0) { poll8<0>(ws, 5 * Ly + 3, c, c, tag, lane); *(volatile LAS unsigned*)(L + L_W + 12) = Ly + 1; }
        else lds_wait_ge<1>(L, 3, Ly + 1);
        CST(16 + 32 * Ly);
        f32x4 sa, sb, ea, eb;
        gather(sa, sb, ea, eb);
        tail(sa, sb, ea, eb);
        __builtin_amdgcn_s_setprio(0);
      }
      CST(22 + 32 * Ly);
      STAMP(12 + 16 * Ly);
      if (Ly == 0) {
        const int cprev = c >= 2 ? c - 2 : c;
        publish(ws, 4 * 32 + c, j, tag, tid);
        if (role) fillh<0>(rw, wq0, voff);
        fillh<1>(rw, wq0, voff);
        await(ws, 4, c, cprev, tag, w, lane);
        STAMP(17);
        if (w < 5) stage_tile(rs, X2_OFF + (w == 4 ? cprev * XBC + 3 * REV : c * XBC + w * REV), L, w == 4 ? 0 : w + 1, w == 4, lane);
      }
    }
  }
}

extern "C" void kernel_launch(void* const* d_in, const int* in_sizes, int n_in, void* d_out, int out_size, void* d_ws,
                              size_t ws_size, hipStream_t stream) {
  const u16* x = (const u16*)d_in[0];
  const u16* wqkv = (const u16*)d_in[1];
  const u16* wo = (const u16*)d_in[2];
  const u16* w1 = (const u16*)d_in[4];
  const u16* b1 = (const u16*)d_in[5];
  const u16* w2 = (const u16*)d_in[6];
  const u16* b2 = (const u16*)d_in[7];
  (void)hipFuncSetAttribute((const void*)k_main, hipFuncAttributeMaxDynamicSharedMemorySize, LDS_BYTES);
  k_main<<<256, 512, LDS_BYTES, stream>>>((char*)d_ws, x, wqkv, wo, w1, b1, w2, b2, (u16*)d_out);
}
```

```cpp
#include <hip/hip_runtime.h>
#include <hip/hip_bf16.h>
#include <type_traits>

#define GAS __attribute__((address_space(1)))
#define LAS __attribute__((address_space(3)))
typedef unsigned short u16;
typedef unsigned v4u __attribute__((ext_vector_type(4)));
typedef unsigned v2u __attribute__((ext_vector_type(2)));
typedef float f32x4 __attribute__((ext_vector_type(4)));
typedef float f32x2 __attribute__((ext_vector_type(2)));
typedef short bf16x8 __attribute__((ext_vector_type(8)));
typedef __bf16 bf16x2 __attribute__((ext_vector_type(2)));
typedef GAS unsigned gu32;
typedef unsigned long long u64;
typedef GAS u64 gu64;
typedef __amdgpu_buffer_rsrc_t rsrc_t;
#define RLX_AGENT __ATOMIC_RELAXED, __HIP_MEMORY_SCOPE_AGENT

#define TRICKLE 4
#define G2PK 0
#define TSH 20
#define GSPREAD 4

constexpr int S_ = 2048, D_ = 576, F_ = 4608;
constexpr int REV = 18 * 1024;
constexpr int XBC = 4 * REV;
constexpr int XBSZ = 32 * XBC;
constexpr int FL_OFF = 0;
constexpr int WQ_OFF = 131072;
constexpr int WO_OFF = WQ_OFF + 128 * REV;
constexpr int MS_OFF = WO_OFF + 36 * REV;
constexpr int MSJ = 72 * REV;
constexpr int CSP_OFF = MS_OFF + 8 * MSJ;
constexpr int X2_OFF = CSP_OFF + 8 * 18 * 2304 + 4096 - (8 * 18 * 2304) % 4096;
constexpr int OB_OFF = X2_OFF + XBSZ;
constexpr int X1_OFF = OB_OFF + 2 * XBSZ;
constexpr int PB_OFF = X1_OFF + 2 * XBSZ;
constexpr int PBSZ = 32 * 8 * 8 * 10240;
constexpr int ST_OFF = PB_OFF + 2 * PBSZ;
constexpr int ST2_OFF = ST_OFF + 256 * 64 * 8;
constexpr int WS_BYTES = ST2_OFF + 2 * 256 * 8 * 64 * 8;
constexpr int L_X = 0, L_H = 73728, L_ATT = 92160, ATT_STR = 10240, A_Q = 0, A_K = 2560, A_V = 5120, A_E = 7680;
constexpr int L_RES = 137216, L_OWN = 147456, L_CS1 = L_OWN + 8192, L_B1 = L_CS1 + 2304, L_STR = L_B1 + 2304,
              L_STM = L_STR + 320, L_B2 = L_STM + 320, L_W = L_B2 + 320, L_END = L_W + 64;
static_assert(L_END <= 163840, "lds");
constexpr int LDS_BYTES = 163840;

__device__ __forceinline__ float bf2f(u16 b) { return __uint_as_float(((unsigned)b) << 16); }
__device__ __forceinline__ float bflo(unsigned u) { return __uint_as_float(u << 16); }
__device__ __forceinline__ float bfhi(unsigned u) { return __uint_as_float(u & 0xffff0000u); }
__device__ __forceinline__ unsigned pk2(float a, float b) {
  f32x2 v = {a, b};
  return __builtin_bit_cast(unsigned, __builtin_convertvector(v, bf16x2));
}
__device__ __forceinline__ v2u pk4(f32x4 v) { return v2u{pk2(v[0], v[1]), pk2(v[2], v[3])}; }
__device__ __forceinline__ f32x4 unpk4(unsigned a, unsigned b) { return f32x4{bflo(a), bfhi(a), bflo(b), bfhi(b)}; }
__device__ __forceinline__ f32x4 mfma(v4u a, v4u b, f32x4 c) {
  return __builtin_amdgcn_mfma_f32_16x16x32_bf16(__builtin_bit_cast(bf16x8, a), __builtin_bit_cast(bf16x8, b), c, 0, 0, 0);
}
__device__ __forceinline__ v4u ldw(rsrc_t r, int voff, int soff) { return __builtin_amdgcn_raw_buffer_load_b128(r, voff, soff, 0); }
__device__ __forceinline__ v4u ldc(rsrc_t r, int voff, int soff) { return __builtin_amdgcn_raw_buffer_load_b128(r, voff, soff, 16); }
__device__ __forceinline__ void stc128(v4u v, rsrc_t r, int voff, int soff) {
  __builtin_amdgcn_raw_buffer_store_b128(v, r, voff, soff, 16);
  asm volatile("s_nop 1" :: "v"(v));
}
__device__ __forceinline__ void stc64(v2u v, rsrc_t r, int voff, int soff) { __builtin_amdgcn_raw_buffer_store_b64(v, r, voff, soff, 16); }
__device__ __forceinline__ void stc32(unsigned v, rsrc_t r, int voff, int soff) { __builtin_amdgcn_raw_buffer_store_b32(v, r, voff, soff, 16); }
#define SB() __builtin_amdgcn_sched_barrier(0)
__device__ __forceinline__ void bar() {
  asm volatile("s_waitcnt lgkmcnt(0)" ::: "memory");
  __builtin_amdgcn_s_barrier();
  asm volatile("" ::: "memory");
}
__device__ __forceinline__ float red16max(float x) {
  auto r = __builtin_amdgcn_permlane16_swap(__float_as_uint(x), __float_as_uint(x), false, false);
  x = fmaxf(__uint_as_float(r[0]), __uint_as_float(r[1]));
  auto q = __builtin_amdgcn_permlane32_swap(__float_as_uint(x), __float_as_uint(x), false, false);
  return fmaxf(__uint_as_float(q[0]), __uint_as_float(q[1]));
}
__device__ __forceinline__ float red16sum(float x) {
  auto r = __builtin_amdgcn_permlane16_swap(__float_as_uint(x), __float_as_uint(x), false, false);
  x = __uint_as_float(r[0]) + __uint_as_float(r[1]);
  auto q = __builtin_amdgcn_permlane32_swap(__float_as_uint(x), __float_as_uint(x), false, false);
  return __uint_as_float(q[0]) + __uint_as_float(q[1]);
}
__device__ __forceinline__ int ms_wave(int w) { return 162 * 1024 * w; }
__device__ __forceinline__ int kperm(int s, int g, int e) { return 32 * s + 4 * g + (e & 3) + ((e >> 2) << 4); }

#define STAMP(i) do {} while (0)
#define CST(i) do {} while (0)

__device__ __forceinline__ gu64* flagp(char* ws, int line, int i) { return (gu64*)(ws + FL_OFF + 256 + line * 256 + i * 8); }
__device__ __forceinline__ void publish(char* ws, int line, int i, u64 tag, int tid) {
  asm volatile("s_waitcnt vmcnt(0)" ::: "memory");
  bar();
  if (tid == 0) __hip_atomic_store(flagp(ws, line, i), tag, RLX_AGENT);
}
template <int SL> __device__ __forceinline__ void poll(gu64* p, u64 tag) {
  for (unsigned spins = 0; spins < (1u << 22); ++spins) {
    const u64 v = __hip_atomic_load(p, RLX_AGENT);
    if (__all(v == tag)) break;
    if (SL) __builtin_amdgcn_s_sleep(SL);
  }
  __builtin_amdgcn_fence(__ATOMIC_ACQUIRE, "wavefront");
}
template <int SL> __device__ __forceinline__ void poll8(char* ws, int ph, int c, int c2, u64 tag, int lane) {
  poll<SL>(flagp(ws, ph * 32 + ((lane & 8) ? c2 : c), lane & 7), tag);
}
__device__ __forceinline__ void await(char* ws, int ph, int c, int c2, u64 tag, int w, int lane) {
  if (w == 0) poll8<0>(ws, ph, c, c2, tag, lane);
  bar();
}

__device__ __forceinline__ void stat2(v4u x, float& s1, float& s2) {
  const bf16x2 ones = __builtin_bit_cast(bf16x2, 0x3f803f80u);
#pragma unroll
  for (int i = 0; i < 4; ++i) {
    const unsigned u = x[i];
    const bf16x2 v = __builtin_bit_cast(bf16x2, u);
    s1 = __builtin_amdgcn_fdot2_f32_bf16(v, ones, s1, false);
    s2 = __builtin_amdgcn_fdot2_f32_bf16(v, v, s2, false);
  }
}
__device__ __forceinline__ void stat_put(LAS char* L, int idx, float s1, float s2) {
  const float mean = s1 * (1.0f / D_);
  const float var = fmaxf(s2 * (1.0f / D_) - mean * mean, 0.f);
  const float rstd = __builtin_amdgcn_rsqf(var + 1e-5f);
  *(LAS float*)(L + L_STR + idx * 4) = rstd;
  *(LAS float*)(L + L_STM + idx * 4) = mean * rstd;
}
__device__ __forceinline__ void ones_row(LAS char* L, int lane) {
  if ((lane & 15) == 0) {
#pragma unroll
    for (int s = 0; s < 18; ++s) *(LAS v4u*)(L + L_X + s * 1024 + lane * 16) = v4u{0x3f803f80u, 0x3f803f80u, 0x3f803f80u, 0x3f803f80u};
  }
}
template <int CTRL> __device__ __forceinline__ float dppadd(float x) {
  return x + __uint_as_float(__builtin_amdgcn_update_dpp(0u, __float_as_uint(x), CTRL, 0xf, 0xf, true));
}
__device__ __forceinline__ float red8sum(float x) { return dppadd<0x141>(dppadd<0x4E>(dppadd<0xB1>(x))); }
__device__ __forceinline__ void stage_tile(rsrc_t rs, int soff, LAS char* L, int t5, bool halo, int lane) {
  v4u x[18];
#pragma unroll
  for (int s = 0; s < 18; ++s) x[s] = ldc(rs, lane * 16 + (s & 3) * 1024, soff + (s >> 2) * 4096);
  SB();
  float s1 = 0.f, s2 = 0.f;
#pragma unroll
  for (int s = 0; s < 18; ++s) {
    *(LAS v4u*)(L + L_X + t5 * REV + s * 1024 + lane * 16) = x[s];
    stat2(x[s], s1, s2);
    SB();
  }
  if (halo) ones_row(L, lane);
  s1 = red16sum(s1); s2 = red16sum(s2);
  if (lane < 16) stat_put(L, t5 * 16 + lane, s1, s2);
}

#define RING 28
constexpr int R_ = RING;
constexpr int RB = 256 - 4 * R_;
template <int SLOT, int OFF> __device__ __forceinline__ void rload(v4u rs, int voff, int so) {
  asm volatile("buffer_load_dwordx4 v[%c3:%c4], %0, %1, %2 offen offset:%c5 sc1"
               :: "v"(voff), "s"(rs), "s"(so), "i"(RB + 4 * SLOT), "i"(RB + 4 * SLOT + 3), "i"(OFF));
}
template <int N> __device__ __forceinline__ void rwait() { asm volatile("s_waitcnt vmcnt(%c0)" :: "i"(N)); }
template <int SLOT> __device__ __forceinline__ void rmfma(f32x4& acc, v4u x) {
  asm volatile("v_mfma_f32_16x16x32_bf16 %0, v[%c2:%c3], %1, %0" : "+v"(acc) : "v"(x), "i"(RB + 4 * SLOT), "i"(RB + 4 * SLOT + 3));
}
template <int SLOT> __device__ __forceinline__ void rmfma0(f32x4& acc, v4u x) {
  asm volatile("v_mfma_f32_16x16x32_bf16 %0, v[%c2:%c3], %1, 0" : "=&v"(acc) : "v"(x), "i"(RB + 4 * SLOT), "i"(RB + 4 * SLOT + 3));
}
template <int I, int N, class Fn> __device__ __forceinline__ void sfor(Fn&& f) {
  if constexpr (I < N) { f(std::integral_constant<int, I>{}); sfor<I + 1, N>(f); }
}
constexpr int cmin(int a, int b) { return a < b ? a : b; }
template <int P, int TOTAL> __device__ __forceinline__ void refill(v4u r, int mb, int voff) {
  constexpr int Q = P + R_;
  if constexpr (Q < TOTAL) rload<P % R_, (Q & 3) * 1024>(r, voff, mb + (Q >> 2) * 4096);
}
struct NoHook { template <int S> static constexpr int extra() { return 0; } template <int S> __device__ __forceinline__ void at() {} };
template <int NT, int NK, int S0, int S1, int P0, int TOTAL, bool INIT, int I0 = R_, int NKL = NK, class Hook = NoHook>
__device__ __forceinline__ void segx(f32x4* const (&acc)[NT], const LAS char* const (&xb)[NK], v4u r, int mb, int voff, Hook& hook) {
  constexpr int XD = NT >= 4 ? 1 : (NT == 1 ? 4 : 2);
  v4u xf[XD][NK];
#pragma unroll
  for (int d = 0; d < XD; ++d)
#pragma unroll
    for (int tt = 0; tt < NK; ++tt)
      if (S0 + d < S1) xf[d][tt] = *(const LAS v4u*)(xb[tt] + (S0 + d) * 1024);
  SB();
  asm volatile("s_nop 1");
  sfor<S0, S1>([&](auto sc) {
    constexpr int s = sc.value, p = P0 + (s - S0) * NT, d = (s - S0) % XD;
    constexpr int RS = (R_ - I0 + 3) / 4;
    constexpr int lag = RS > 1 ? RS : 1;
    constexpr int k = s - S0;
    constexpr int issued = k < RS ? I0 + 4 * k : cmin(TOTAL, R_ + (k < lag ? P0 : p - lag * NT));
    rwait<issued - (p + NT) + Hook::template extra<s>()>();
    if constexpr (k < RS) sfor<I0 + 4 * k, cmin(R_, I0 + 4 * k + 4)>([&](auto qc) { rload<qc.value, (qc.value & 3) * 1024>(r, voff, mb + (qc.value >> 2) * 4096); });
    hook.template at<s>();
    sfor<0, NK>([&](auto kc) {
      constexpr int tt = kc.value;
      sfor<0, NT>([&](auto tc) {
        constexpr int t = tc.value;
        if constexpr (t == NT - 1 && tt >= NKL) {}
        else if constexpr (INIT && s == S0) rmfma0<(p + t) % R_>(acc[t][tt], xf[d][tt]);
        else rmfma<(p + t) % R_>(acc[t][tt], xf[d][tt]);
        constexpr int idx = tt * NT + t;
        if constexpr (k >= lag && idx % NK == 0 && idx / NK < NT) refill<p - lag * NT + idx / NK, TOTAL>(r, mb, voff);
      });
      SB();
      if constexpr (s + XD < S1) { xf[d][tt] = *(const LAS v4u*)(xb[tt] + (s + XD) * 1024); SB(); }
    });
  });
  static_assert(I0 == R_ || P0 + (S1 - S0 - (R_ - I0 + 3) / 4) * NT + R_ >= TOTAL, "ramp: all refills are out inside the loop");
  sfor<0, NT>([&](auto tc) { refill<P0 + (S1 - 1 - S0) * NT + tc.value, TOTAL>(r, mb, voff); });
  asm volatile("s_nop 7\n\ts_nop 1");
  SB();
}
template <int NT, int NK, int S0, int S1, int P0, int TOTAL, bool INIT, class Hook = NoHook>
__device__ __forceinline__ void seg(f32x4* const (&acc)[NT], const LAS char* xb, v4u r, int mb, int voff, Hook hook = Hook{}) {
  const LAS char* x2 = xb + 2 * REV;
  asm volatile("" : "+v"(x2));
  segx<NT, 4, S0, S1, P0, TOTAL, INIT, R_, 4, Hook>(acc, {xb, xb + REV, x2, x2 + REV}, r, mb, voff, hook);
}
template <int LO> __device__ __forceinline__ void fillh(v4u r, int n, int voff) {
  sfor<LO ? R_ / 2 : 0, LO ? R_ : R_ / 2>([&](auto sc) { rload<sc.value, (sc.value & 3) * 1024>(r, voff, n + (sc.value >> 2) * 4096); });
  SB();
}
__device__ __forceinline__ v2u trread(const LAS char* p) {
  typedef short s4 __attribute__((ext_vector_type(4)));
  return __builtin_bit_cast(v2u, __builtin_amdgcn_ds_read_tr16_b64_v4i16((LAS s4*)p));
}
constexpr float AL = 0.46866869f, KK = 2.3022082f / AL;
__device__ __forceinline__ float sfma(float a, float b, float c) { float d; asm("v_fma_f32 %0, %1, %2, %3" : "=v"(d) : "v"(a), "v"(b), "v"(c)); return d; }
__device__ __forceinline__ float sfnma(float a, float b, float c) { float d; asm("v_fma_f32 %0, -%1, %2, %3" : "=v"(d) : "v"(a), "v"(b), "v"(c)); return d; }
__device__ __forceinline__ float sfmas(float a, float s) { float d; asm("v_fma_f32 %0, %1, %2, %2" : "=v"(d) : "v"(a), "s"(s)); return d; }
__device__ __forceinline__ float ssqs(float a, float s) { float d; asm("v_fma_f32 %0, %1, %1, %2" : "=v"(d) : "v"(a), "s"(s)); return d; }
__device__ __forceinline__ float smul(float a, float b) { float d; asm("v_mul_f32 %0, %1, %2" : "=v"(d) : "v"(a), "v"(b)); return d; }
#define EACH16 _Pragma("unroll") for (int tt = 0; tt < 4; ++tt) _Pragma("unroll") for (int r = 0; r < 4; ++r)
__device__ __forceinline__ void gelu16(f32x4 (&a)[4], const float (&rstd)[4], const float (&mr)[4], f32x4 cs, f32x4 bb, v2u (&o)[4]) {
  float e[4][4], u[4][4];
  EACH16 e[tt][r] = sfnma(cs[r], mr[tt], bb[r]);
  SB();
  EACH16 u[tt][r] = sfma(a[tt][r], rstd[tt], e[tt][r]);
  SB();
  EACH16 e[tt][r] = ssqs(u[tt][r], KK);
  SB();
  EACH16 e[tt][r] = smul(e[tt][r], u[tt][r]);
  SB();
  EACH16 e[tt][r] = __builtin_amdgcn_exp2f(-e[tt][r]);
  SB();
  EACH16 e[tt][r] = sfmas(e[tt][r], AL);
  SB();
  EACH16 e[tt][r] = __builtin_amdgcn_rcpf(e[tt][r]);
  SB();
  EACH16 u[tt][r] = smul(u[tt][r], e[tt][r]);
  SB();
#pragma unroll
  for (int tt = 0; tt < 4; ++tt) o[tt] = v2u{pk2(u[tt][0], u[tt][1]), pk2(u[tt][2], u[tt][3])};
  SB();
}
__device__ __forceinline__ void gelu16p(f32x4 (&a)[4], const float (&rstd)[4], const float (&mr)[4], f32x4 cs, f32x4 bb, v2u (&o)[4]) {
  f32x4 e[4];
#pragma unroll
  for (int tt = 0; tt < 4; ++tt) e[tt] = bb - cs * mr[tt];
  SB();
#pragma unroll
  for (int tt = 0; tt < 4; ++tt) a[tt] = a[tt] * rstd[tt] + e[tt];
  SB();
#pragma unroll
  for (int tt = 0; tt < 4; ++tt) e[tt] = a[tt] * a[tt] + KK;
  SB();
#pragma unroll
  for (int tt = 0; tt < 4; ++tt) e[tt] = e[tt] * a[tt];
  SB();
  EACH16 e[tt][r] = __builtin_amdgcn_exp2f(-e[tt][r]);
  SB();
#pragma unroll
  for (int tt = 0; tt < 4; ++tt) e[tt] = e[tt] * AL + AL;
  SB();
  EACH16 e[tt][r] = __builtin_amdgcn_rcpf(e[tt][r]);
  SB();
#pragma unroll
  for (int tt = 0; tt < 4; ++tt) a[tt] = a[tt] * e[tt];
  SB();
#pragma unroll
  for (int tt = 0; tt < 4; ++tt) o[tt] = pk4(a[tt]);
  SB();
}
struct StoreHook {
  v4u d[4]; rsrc_t rs; int voff, po0, po1;
  template <int S> static constexpr int extra() { return S <= 6 ? 0 : S <= 9 ? 1 : S <= 12 ? 2 : S <= 15 ? 3 : 4; }
  template <int S> __device__ __forceinline__ void at() {
    if constexpr (S == 6 || S == 9 || S == 12 || S == 15) {
      constexpr int q = (S - 6) / 3;
      SB(); stc128(d[q], rs, voff + (q & 1) * 1024, q < 2 ? po0 : po1); SB();
    }
  }
};
template <int SL> __device__ __forceinline__ void lds_wait_ge(LAS char* L, int word, unsigned want) {
  volatile LAS unsigned* p = (volatile LAS unsigned*)(L + L_W + word * 4);
  for (unsigned spins = 0; spins < (1u << 22); ++spins) {
    if (__builtin_amdgcn_readfirstlane(*p) >= want) break;
    __builtin_amdgcn_s_sleep(SL);
  }
  asm volatile("" ::: "memory");
}
template <int N> __device__ __forceinline__ void arrive(char* ws, LAS char* L, int word, int line, int i, u64 tag, int lane) {
  asm volatile("s_waitcnt vmcnt(0)" ::: "memory");
  if (lane == 0) {
    const unsigned old = __hip_atomic_fetch_add((LAS unsigned*)(L + L_W + word * 4), 1u, __ATOMIC_RELAXED, __HIP_MEMORY_SCOPE_WORKGROUP);
    if (old % N == N - 1) __hip_atomic_store(flagp(ws, line, i), tag, RLX_AGENT);
  }
}
__device__ __forceinline__ float sadd(float a, float b) { float d; asm("v_add_f32 %0, %1, %2" : "=v"(d) : "v"(a), "v"(b)); return d; }
__device__ __forceinline__ void acc8(f32x4& a, f32x4& b, v4u p) {
  a[0] = sadd(a[0], bflo(p[0])); a[1] = sadd(a[1], bfhi(p[0])); a[2] = sadd(a[2], bflo(p[1])); a[3] = sadd(a[3], bfhi(p[1]));
  b[0] = sadd(b[0], bflo(p[2])); b[1] = sadd(b[1], bfhi(p[2])); b[2] = sadd(b[2], bflo(p[3])); b[3] = sadd(b[3], bfhi(p[3]));
}

#define FRESH() int lane = lane0, c = c0, j = j0, w = w0; asm volatile("" : "+v"(lane)); asm volatile("" : "+s"(c), "+s"(j), "+s"(w)); \
  const int g = lane >> 4, m = lane & 15, voff = lane * 16, role = w >> 2, hl = w & 3; (void)g; (void)m; (void)voff; (void)role; (void)hl;

__device__ __forceinline__ void jload(int job, v4u (&r)[4], const u16* w1, const u16* w2, const u16* wo, int j, int lane) {
      const int kind = job < 162 ? 0 : (job < 324 ? 1 : 2);
      const int q = job - 162 * kind, s9 = q / 9, s = kind < 2 ? s9 : (q < 18 ? q : q - 18), B = kind < 2 ? q - 9 * s9 : (q < 18 ? j : 8);
      const u16* src = kind == 0 ? w1 + (32 * s * F_ + 576 * j) : (kind == 1 ? w2 + (576 * j + 32 * s) * D_ : wo);
      const int ld = kind == 0 ? F_ : D_;
#pragma unroll
      for (int i = 0; i < 4; ++i) {
        const int rho = 8 * i + (lane >> 3);
        const int orow = s < 16 ? 36 * s + rho + (i >= 2 ? 4 : 0) : 36 * (8 * (s - 16) + 2 * ((rho >> 2) & 3) + (i >> 1)) + 16 + (rho & 3);
        r[i] = *(const v4u*)(src + (kind < 2 ? rho : orow) * ld + 64 * B + (lane & 7) * 8);
      }
}
__device__ __forceinline__ void jput(int job, v4u (&r)[4], LAS char* scr, rsrc_t rs, int j, int lane) {
      const int g = lane >> 4, m = lane & 15, voff = lane * 16;
      const int kind = job < 162 ? 0 : (job < 324 ? 1 : 2);
      const bool one = kind == 0;
      const int q = job - 162 * kind, s9 = q / 9, s = kind < 2 ? s9 : (q < 18 ? q : q - 18), B = kind < 2 ? q - 9 * s9 : (q < 18 ? j : 8);
      const int d = B < 8 ? B : j;
      const int e0 = 162 * (d >> 1) + (s < 8 ? 72 + 4 * s + 2 * (d & 1) : 88 + 20 * (d & 1) + 2 * s), e1 = 162 * (4 + (d >> 1)) + 90 + 36 * (d & 1) + 2 * s;
      const bool spread = one ? B == 8 : B == j;
      int base = one ? (B < 4 ? 162 * B + 4 * s : (B < 8 ? 162 * (4 + 2 * (B & 1)) + 5 * s + (B >= 6 ? 2 : 0) : 652 + 5 * s)) : (spread ? 144 + s : e0);
      int c1 = spread ? 162 : 1, c2 = spread ? 324 : (one ? (B < 4 ? 2 : 162) : e1 - e0);
      base = kind < 2 ? (MS_OFF + j * MSJ) / 1024 + base : WO_OFF / 1024 + 72 * B + s;
      c1 = kind < 2 ? c1 : 18; c2 = kind < 2 ? c2 : 36;
      v2u lo[4], hi[4];
      const LAS char* p = scr + (4 * g + (m >> 2)) * 128 + (m & 3) * 8;
      *(LAS v4u*)(scr + voff) = r[0]; *(LAS v4u*)(scr + 1024 + voff) = r[1];
#pragma unroll
      for (int t = 0; t < 4; ++t) lo[t] = trread(p + t * 32);
      SB();
      *(LAS v4u*)(scr + voff) = r[2]; *(LAS v4u*)(scr + 1024 + voff) = r[3];
#pragma unroll
      for (int t = 0; t < 4; ++t) hi[t] = trread(p + t * 32);
      float cs4[4];
#pragma unroll
      for (int t = 0; t < 4; ++t) {
        const v4u f = {lo[t][0], lo[t][1], hi[t][0], hi[t][1]};
        stc128(f, rs, voff, (base + (t & 1) * c1 + (t >> 1) * c2) * 1024);
        float a = 0.f, dummy = 0.f;
        stat2(f, a, dummy);
        cs4[t] = red16sum(a);
      }
      const float csum = g < 2 ? (g == 0 ? cs4[0] : cs4[1]) : (g == 2 ? cs4[2] : cs4[3]);
      if (one) stc32(__float_as_uint(csum), rs, lane * 4, CSP_OFF + (j * 18 + s) * 2304 + B * 256);
}

struct JobHook {
  v4u jr[3][4]; const u16 *w1, *w2; int job0, j, lane;
  template <int S> static constexpr int extra() { return S <= 9 ? 0 : S <= 10 ? 4 : S <= 11 ? 8 : 12; }
  template <int S> __device__ __forceinline__ void at() {
    if constexpr (S >= 9 && S <= 11) { SB(); jload(min(job0 + 128 * (S - 9), 323), jr[S - 9], w1, w2, w1, j, lane); SB(); }
  }
};

__global__ __launch_bounds__(512, 2) __attribute__((amdgpu_num_vgpr(RB / 2)))
void k_main(char* __restrict__ ws, const u16* __restrict__ xin, const u16* __restrict__ wqkv, const u16* __restrict__ wo,
            const u16* __restrict__ w1, const u16* __restrict__ b1, const u16* __restrict__ w2, const u16* __restrict__ b2,
            u16* __restrict__ out) {
  extern __shared__ __attribute__((aligned(16))) char smem[];
  LAS char* L = (LAS char*)smem;
  const int tid = threadIdx.x, lane0 = tid & 63, w0 = __builtin_amdgcn_readfirstlane(tid >> 6);
  int c0, j0;
  {
    constexpr int G = GSPREAD, per = 8 / G;
    const int b = blockIdx.x, x = b & 7, r = b >> 3;
    j0 = (x % G) * per + (r % per);
    c0 = (x / G) * (4 * G) + r / per;
  }
  const rsrc_t rs = __builtin_amdgcn_make_buffer_rsrc((void*)ws, 0, WS_BYTES, 0x00020000);
  const v4u rw = {(unsigned)(size_t)ws, (unsigned)((size_t)ws >> 32) & 0xffffu, (unsigned)WS_BYTES, 0x00020000u};
  asm volatile("" ::: "v255");
  STAMP(0);
  CST(24);
  u64 tag = 0;
  auto qkv = [&](auto lc) {
    constexpr int Ly = lc.value;
    STAMP(2 + 16 * Ly);
    FRESH();
    if (role == 0) {
#pragma unroll
      for (int i = 0; i < 5; ++i) {
        const int T = i < 4 ? 4 * j + i : 32 + (j & 3);
        v2u v = *(LAS v2u*)(L + L_X + (hl + 1) * REV + (T >> 1) * 1024 + voff + 8 * (T & 1));
        *(LAS v2u*)(L + L_RES + (i * 4 + hl) * 512 + lane * 8) = v;
      }
    }
    const int qb = WQ_OFF + ((4 * j + hl) * 4 + 2 * role) * REV;
    constexpr int I0 = Ly == 0 ? 8 : R_;
    auto fix = [&](f32x4 a, const float (&cs)[4], float rstd, float mr) {
      float u[4];
#pragma unroll
      for (int r = 0; r < 4; ++r) u[r] = smul(a[r], rstd);
#pragma unroll
      for (int r = 0; r < 4; ++r) u[r] = sfnma(cs[r], mr, u[r]);
      return v2u{pk2(u[0], u[1]), pk2(u[2], u[3])};
    };
    auto bcast = [&](f32x4 a, float (&cs)[4]) {
#pragma unroll
      for (int r = 0; r < 4; ++r) cs[r] = __uint_as_float(__builtin_amdgcn_update_dpp(0u, __float_as_uint(a[r]), 0x150, 0xf, 0xf, false));
    };
    CST(0 + 32 * Ly);
    if (role == 0) {
      f32x4 a0[5], a1[5];
      const LAS char* xa = L + L_X + voff, *xc = xa + 3 * REV;
      asm volatile("" : "+v"(xc));
      NoHook hk;
      segx<2, 5, 0, 18, 0, 36, true, I0, 5>({a0, a1}, {xa, xa + REV, xa + 2 * REV, xc, xc + REV}, rw, qb, voff, hk);
      CST(1 + 32 * Ly);
      LAS char* A = L + L_ATT + hl * ATT_STR + lane * 8;
      float rstd[5], mr[5], cs0[4], cs1[4];
#pragma unroll
      for (int t = 0; t < 5; ++t) { rstd[t] = *(LAS float*)(L + L_STR + t * 64 + m * 4); mr[t] = *(LAS float*)(L + L_STM + t * 64 + m * 4); }
      bcast(a0[0], cs0); bcast(a1[0], cs1);
      SB();
#pragma unroll
      for (int t = 0; t < 5; ++t) {
        if (t) *(LAS v2u*)(A + A_Q + t * 512) = fix(a0[t], cs0, rstd[t], mr[t]);
        *(LAS v2u*)(A + A_K + t * 512) = fix(a1[t], cs1, rstd[t], mr[t]);
      }
    }
    int role2 = role;
    asm volatile("" : "+s"(role2));
    if (role2) {
      f32x4 a0[5], a1[5];
      const int ta = (hl & 1) ? 3 : 1, tb = 4 - ta;
      const LAS char* x0 = L + L_X + voff, *xa = x0 + ta * REV, *xc = x0 + tb * REV;
      std::conditional_t<Ly == 0, JobHook, NoHook> hk;
      if constexpr (Ly == 0) { hk.w1 = w1; hk.w2 = w2; hk.job0 = c * 4 + hl; hk.j = j; hk.lane = lane; }
      __builtin_amdgcn_s_setprio(1);
      segx<2, 5, 0, 18, 0, 36, true, I0, 3>({a0, a1}, {x0, xa, xa + REV, xc, xc + REV}, rw, qb, voff, hk);
      __builtin_amdgcn_s_setprio(0);
      CST(1 + 32 * Ly);
      LAS char* A = L + L_ATT + hl * ATT_STR + A_V + m * 32 + g * 8, *E = L + L_ATT + (hl & 2) * ATT_STR + A_E + m * 32 + g * 8;
      float rstd[5], mr[5], cs0[4], cs1[4];
      int to[5] = {0, ta, ta + 1, tb, tb + 1};
#pragma unroll
      for (int t = 0; t < 5; ++t) { rstd[t] = *(LAS float*)(L + L_STR + to[t] * 64 + m * 4); mr[t] = *(LAS float*)(L + L_STM + to[t] * 64 + m * 4); }
      bcast(a0[0], cs0); bcast(a1[0], cs1);
      SB();
#pragma unroll
      for (int t = 0; t < 5; ++t) {
        *(LAS v2u*)(A + to[t] * 512) = fix(a0[t], cs0, rstd[t], mr[t]);
        if (t < 3) *(LAS v2u*)(E + to[t] * 512) = fix(a1[t], cs1, rstd[t], mr[t]);
      }
      if (Ly) fillh<0>(rw, MS_OFF + j * MSJ + ms_wave(w), voff);
      if constexpr (Ly == 0) {
#pragma unroll
        for (int i = 0; i < 3; ++i) jput(min(c * 4 + hl + 128 * i, 323), hk.jr[i], L + L_OWN + hl * 2048, rs, j, lane);
      }
    }
  };
  {
    FRESH();
    u64 gen = 0;
    if (w >= 5) gen = __hip_atomic_load((gu64*)(ws + FL_OFF), RLX_AGENT);
    if (w == 4) {
      u64 pc;
      asm volatile("s_getpc_b64 %0" : "=s"(pc));
      const char* cp = (const char*)(pc & ~(u64)127) + (blockIdx.x >> 3) * 1536 + min(lane, 11) * 128;
      asm volatile("global_load_dword v255, %0, off" :: "v"(cp));
      asm volatile("buffer_load_dword v254, %0, %1, 0 offen sc1" :: "v"((lane << TSH) + 65536), "s"(rw));
    }
    if (w >= 5 && c < 18) {
      const int sec = w - 5;
      const u16* src = wqkv + (32 * c) * (3 * D_) + 72 * j;
      v4u qr[5];
      v2u xr[2];
#pragma unroll
      for (int i = 0; i < 5; ++i) {
        const int ch = min(64 * i + lane, 287), row = (ch * 7282) >> 16;
        qr[i] = *(const v4u*)(src + row * (3 * D_) + sec * D_ + (ch - 9 * row) * 8);
      }
#pragma unroll
      for (int i = 0; i < 2; ++i) xr[i] = *(const v2u*)(src + (16 * i + (lane >> 2)) * (3 * D_) + min(lane & 3, 2) * D_ + 36 * (sec & 1) + 16);
      SB();
      bar();
      CST(25);
      LAS char* scr = L + L_ATT + 40960 + sec * 4736;
#pragma unroll
      for (int i = 0; i < 5; ++i) *(LAS v4u*)(scr + min(64 * i + lane, 287) * 16) = qr[i];
      const int qd = WQ_OFF + 16 * j * REV + (sec >> 1) * 2 * REV + (2 * c + (sec & 1)) * 1024;
#pragma unroll
      for (int h = 0; h < 4; ++h) {
        const LAS char* p = scr + (4 * g + (m >> 2)) * 144 + (h >> 1) * 72 + (h & 1) * 40 + (m & 3) * 8;
        const v2u lo = trread(p), hi = trread(p + 16 * 144);
        stc128(v4u{lo[0], lo[1], hi[0], hi[1]}, rs, voff, qd + h * 4 * REV);
      }
      if (sec < 2) {
#pragma unroll
        for (int i = 0; i < 2; ++i) *(LAS v2u*)(scr + i * 512 + lane * 8) = (lane & 3) == 3 ? v2u{0u, 0u} : xr[i];
        const v2u lo = trread(scr + lane * 8), hi = trread(scr + 512 + lane * 8);
        const int md = WQ_OFF + (16 * j + 8 * sec + 2) * REV + (2 * c + 1) * 1024;
        stc128(v4u{lo[0], lo[1], hi[0], hi[1]}, rs, voff, md);
        stc128(v4u{lo[0], lo[1], hi[0], hi[1]}, rs, voff, md + 4 * REV);
      }
      CST(26);
      arrive<3>(ws, L, 9, 320 + j, c, gen + 1, lane);
      CST(27);
    }
    int wl = w;
    asm volatile("" : "+s"(wl));
    if (wl >= 5 && c >= 18) {
      const int job = min(324 + (c - 18) * 3 + w - 5, j < 4 ? 359 : 341);
      v4u jo[4];
      jload(job, jo, w1, w2, wo, j, lane);
      bar();
      jput(job, jo, L + L_ATT + 40960 + (w - 5) * 4096, rs, j, lane);
      arrive<3>(ws, L, 9, 320 + j, c, gen + 1, lane);
    }
    asm volatile("" : "+s"(wl));
    if (wl < 5) {
      const int cc = (w == 4 && c >= 2) ? c - 2 : c, tt = w == 4 ? 3 : w, t5 = w == 4 ? 0 : w + 1;
      const int r8 = lane >> 3, q8 = lane & 7;
      const u16* src = xin + (size_t)(2 * (64 * (cc >> 1) + 16 * tt + r8) + (cc & 1)) * D_ + 8 * q8;
      if (tid < 16) *(LAS unsigned*)(L + L_W + tid * 4) = 0u;
      bar();
      v4u x[2][9];
#pragma unroll
      for (int i = 0; i < 9; ++i) { x[0][i] = *(const v4u*)(src + 64 * i); x[1][i] = *(const v4u*)(src + 16 * D_ + 64 * i); }
      SB();
      CST(25);
      float s1[2] = {0.f, 0.f}, s2[2] = {0.f, 0.f};
      LAS char* dst = L + L_X + t5 * REV + (q8 >> 2) * 1024 + ((q8 & 1) * 32 + r8) * 16 + ((q8 >> 1) & 1) * 8;
#pragma unroll
      for (int i = 0; i < 9; ++i)
#pragma unroll
        for (int h = 0; h < 2; ++h) {
          *(LAS v2u*)(dst + i * 2048 + h * 128) = v2u{x[h][i][0], x[h][i][1]};
          *(LAS v2u*)(dst + i * 2048 + h * 128 + 256) = v2u{x[h][i][2], x[h][i][3]};
          stat2(x[h][i], s1[h], s2[h]);
          SB();
        }
      if (w == 4) ones_row(L, lane);
#pragma unroll
      for (int h = 0; h < 2; ++h) { s1[h] = red8sum(s1[h]); s2[h] = red8sum(s2[h]); }
      if (q8 < 2) stat_put(L, t5 * 16 + q8 * 8 + r8, q8 ? s1[1] : s1[0], q8 ? s2[1] : s2[0]);
      CST(26);
    }
    if (w == 6) {
      unsigned bv[5];
#pragma unroll
      for (int i = 0; i < 5; ++i) bv[i] = *(const unsigned*)(b1 + 576 * j + min(lane + 64 * i, 287) * 2);
      const unsigned b2v = *(const unsigned*)(b2 + (lane < 32 ? 64 * j + 2 * lane : 512 + 16 * (j & 3) + 2 * (lane & 7)));
#pragma unroll
      for (int i = 0; i < 5; ++i) *(LAS f32x2*)(L + L_B1 + min(lane + 64 * i, 287) * 8) = f32x2{AL * bflo(bv[i]), AL * bfhi(bv[i])};
      if (lane < 40) *(LAS f32x2*)(L + L_B2 + lane * 8) = f32x2{bflo(b2v), bfhi(b2v)};
    }
    STAMP(1);
    CST(29);
    if (w == 7) {
      poll<0>(flagp(ws, 320 + j, min(lane, 17)), gen + 1);
      if (lane == 0) *(LAS u64*)(L + L_W + 48) = gen + 1;
    }
    CST(30);
    bar();
    CST(31);
    sfor<0, 8>([&](auto qc) { rload<qc.value, (qc.value & 3) * 1024>(rw, voff, WQ_OFF + ((4 * j + hl) * 4 + 2 * role) * REV + (qc.value >> 2) * 4096); });
    tag = *(LAS u64*)(L + L_W + 48);
    tag = ((u64)(unsigned)__builtin_amdgcn_readfirstlane((unsigned)(tag >> 32)) << 32) | (unsigned)__builtin_amdgcn_readfirstlane((unsigned)tag);
    qkv(std::integral_constant<int, 0>{});
  }

#pragma unroll 1
  for (int Ly = 0; Ly < 2; ++Ly) {
    CST(2 + 32 * Ly);
    bar();
    CST(3 + 32 * Ly);
    STAMP(3 + 16 * Ly);
    {
      FRESH();
      const LAS char* A = L + L_ATT + hl * ATT_STR + role * 1024;
      const float sc = 0.23570226039551584f * 1.4426950408889634f;
#pragma unroll
      for (int q = 0; q < 2; ++q) {
        const int t = 2 * role + q;
        const LAS char* At = A + q * 512;
        const v2u qm = *(const LAS v2u*)(At + A_Q + 512 + lane * 8);
        const v2u kpm = *(const LAS v2u*)(At + A_K + lane * 8), kcm = *(const LAS v2u*)(At + A_K + 512 + lane * 8);
        const LAS char* Et = At + A_E - (hl & 1) * ATT_STR;
        const LAS char* Ae = Et + m * 32 + (hl & 1) * 4;
        const unsigned xq = *(const LAS unsigned*)(Ae + 512), xkp = *(const LAS unsigned*)(Ae + 8), xkc = *(const LAS unsigned*)(Ae + 512 + 8);
        const v2u vp = trread(At + A_V + lane * 8), vc = trread(At + A_V + 512 + lane * 8);
        const v2u ep = trread(Et + lane * 8), ec = trread(Et + 512 + lane * 8);
        const v4u qf = {qm[0], qm[1], g == 0 ? xq : 0u, 0u};
        const v4u kp = {kpm[0], kpm[1], g == 0 ? xkp : 0u, 0u}, kc = {kcm[0], kcm[1], g == 0 ? xkc : 0u, 0u};
        const f32x4 z = {0.f, 0.f, 0.f, 0.f};
        f32x4 sp = mfma(kp, qf, z), scur = mfma(kc, qf, z);
        const bool noprev = (c < 2) && (t == 0);
        float mx = -3.0e38f;
        bool okp[4], okc[4];
#pragma unroll
        for (int r = 0; r < 4; ++r) {
          const int kk = 4 * g + r;
          okp[r] = (kk >= m + 9) && !noprev;
          okc[r] = (kk <= m) && (kk >= m - 7);
          mx = fmaxf(mx, okp[r] ? sp[r] : -3.0e38f);
          mx = fmaxf(mx, okc[r] ? scur[r] : -3.0e38f);
        }
        mx = red16max(mx);
        float sum = 0.f;
#pragma unroll
        for (int r = 0; r < 4; ++r) {
          sp[r] = okp[r] ? __builtin_amdgcn_exp2f((sp[r] - mx) * sc) : 0.f;
          scur[r] = okc[r] ? __builtin_amdgcn_exp2f((scur[r] - mx) * sc) : 0.f;
          sum += sp[r] + scur[r];
        }
        sum = red16sum(sum);
        const float inv = __builtin_amdgcn_rcpf(sum);
        const v2u pp = pk4(sp * inv), pc = pk4(scur * inv);
        const v4u pf = {pp[0], pp[1], pc[0], pc[1]};
        const f32x4 o = mfma(v4u{vp[0], vp[1], vc[0], vc[1]}, pf, z);
        const f32x4 oe = mfma(v4u{ep[0], ep[1], ec[0], ec[1]}, pf, z);
        const int ob = OB_OFF + Ly * XBSZ + c * XBC + t * REV;
        stc64(pk4(o), rs, voff + 8 * (hl & 1), ob + (2 * j + (hl >> 1)) * 1024);
        if (g == 2) stc32((hl & 1) ? pk2(oe[2], oe[3]) : pk2(oe[0], oe[1]), rs, m * 16 + 4 * hl, ob + (16 + (j >> 2)) * 1024 + (j & 3) * 256);
      }
      CST(4 + 32 * Ly);
      STAMP(4 + 16 * Ly);
      publish(ws, (5 * Ly + 0) * 32 + c, j, tag, tid);
      if (Ly == 0) {
        if (tid == 0) __hip_atomic_store(flagp(ws, 328 + j, c), tag, RLX_AGENT);
        if (w == 1) { poll<1>(flagp(ws, 320 + j, lane & 31), tag); *(volatile LAS unsigned*)(L + L_W + 32) = 1u; }
        else if (w) lds_wait_ge<1>(L, 8, 1u);
      }
      if (w) {
        const int nblk = j < 4 ? 90 : 72;
        v4u wv[13];
#pragma unroll
        for (int i = 0; i < 13; ++i) {
          const int b = min(w - 1 + 7 * i, nblk - 1);
          wv[i] = ldc(rs, voff, b < 72 ? WO_OFF + 4 * j * REV + b * 1024 : WO_OFF + (32 + j) * REV + (b - 72) * 1024);
        }
        SB();
#pragma unroll
        for (int i = 0; i < 13; ++i) *(LAS v4u*)(L + L_X + min(w - 1 + 7 * i, nblk - 1) * 1024 + voff) = wv[i];
      }
      if (Ly && role == 0 && w) fillh<0>(rw, MS_OFF + j * MSJ + ms_wave(w), voff);
      await(ws, 5 * Ly + 0, c, c, tag, w, lane);
    }
    STAMP(5 + 16 * Ly);
    {
      FRESH();
      v4u of[9];
      const int ob = OB_OFF + Ly * XBSZ + c * XBC + hl * REV + role * 9 * 1024;
#pragma unroll
      for (int s = 0; s < 9; ++s) of[s] = ldc(rs, voff + (s & 3) * 1024, ob + (s >> 2) * 4096);
      SB();
      f32x4 acc[5];
      const LAS char* wb = L + L_X + role * 9 * 1024 + voff;
#pragma unroll
      for (int i = 0; i < 5; ++i) acc[i] = f32x4{0.f, 0.f, 0.f, 0.f};
#pragma unroll
      for (int s = 0; s < 9; ++s) {
#pragma unroll
        for (int i = 0; i < 4; ++i) acc[i] = mfma(*(const LAS v4u*)(wb + i * REV + s * 1024), of[s], acc[i]);
        if (s % 3 == 2) SB();
      }
      if (j < 4) {
#pragma unroll
        for (int s = 0; s < 9; ++s) acc[4] = mfma(*(const LAS v4u*)(wb + 4 * REV + s * 1024), of[s], acc[4]);
      }
      LAS char* R = L + L_ATT + hl * 5120 + voff;
      if (role == 1) {
#pragma unroll
        for (int i = 0; i < 5; ++i) *(LAS f32x4*)(R + i * 1024) = acc[i];
      }
      bar();
      if (role == 0) {
        v2u o[5];
#pragma unroll
        for (int i = 0; i < 5; ++i) {
          const v2u rv = *(LAS v2u*)(L + L_RES + (i * 4 + hl) * 512 + lane * 8);
          o[i] = pk4(acc[i] + *(LAS f32x4*)(R + i * 1024) + unpk4(rv[0], rv[1]));
        }
        const int xb = X1_OFF + Ly * XBSZ + c * XBC + hl * REV;
        stc128(v4u{o[0][0], o[0][1], o[1][0], o[1][1]}, rs, voff, xb + (2 * j) * 1024);
        stc128(v4u{o[2][0], o[2][1], o[3][0], o[3][1]}, rs, voff, xb + (2 * j + 1) * 1024);
        if (j < 4) stc64(o[4], rs, voff + 8 * (j & 1), xb + (16 + (j >> 1)) * 1024);
      }
      STAMP(6 + 16 * Ly);
      publish(ws, (5 * Ly + 1) * 32 + c, j, tag, tid);
      if (Ly == 0) {
        if (w == 1) { poll<1>(flagp(ws, 328 + j, lane & 31), tag); *(volatile LAS unsigned*)(L + L_W + 32) = 2u; }
        else lds_wait_ge<1>(L, 8, 2u);
        fillh<0>(rw, MS_OFF + j * MSJ + ms_wave(w), voff);
        if (w >= 5) {
          const int t = (w - 5) * 64 + lane;
#pragma unroll
          for (int i = 0; i < 3; ++i) {
            float v[18], a = 0.f;
#pragma unroll
            for (int s = 0; s < 18; ++s) v[s] = __uint_as_float(__builtin_amdgcn_raw_buffer_load_b32(rs, (t + 192 * i) * 4, CSP_OFF + (j * 18 + s) * 2304, 16));
#pragma unroll
            for (int s = 0; s < 18; ++s) a += v[s];
            *(LAS float*)(L + L_CS1 + (t + 192 * i) * 4) = a;
          }
        }
      }
      if (Ly && w == 0) fillh<0>(rw, MS_OFF + j * MSJ + ms_wave(w), voff);
      if (w >= 4) fillh<1>(rw, MS_OFF + j * MSJ + ms_wave(w), voff);
      await(ws, 5 * Ly + 1, c, c, tag, w, lane);
      if (Ly == 0 && blockIdx.x == 0 && tid == 0) __hip_atomic_store((gu64*)(ws + FL_OFF), tag, RLX_AGENT);
    }
    STAMP(7 + 16 * Ly);
    {
      FRESH();
      if (w < 4) { stage_tile(rs, X1_OFF + Ly * XBSZ + c * XBC + w * REV, L, w, false, lane); fillh<1>(rw, MS_OFF + j * MSJ + ms_wave(w), voff); }
    }
    bar();
    STAMP(8 + 16 * Ly);
    {
      FRESH();
      const int mb = MS_OFF + j * MSJ + ms_wave(w);
      const int pbc = PB_OFF + Ly * PBSZ + c * (64 * 10240);
      const int wq0 = WQ_OFF + ((4 * j + hl) * 4 + 2 * role) * REV;
      const int ui = 2 * role + (hl >> 1), up = hl & 1;
      const bool five = (j < 4) && ((j >> 1) == role) && (hl < 2);
      auto stats = [&](float (&rstd)[4], float (&mr)[4]) {
#pragma unroll
        for (int tt = 0; tt < 4; ++tt) { rstd[tt] = AL * *(LAS float*)(L + L_STR + tt * 64 + m * 4); mr[tt] = AL * *(LAS float*)(L + L_STM + tt * 64 + m * 4); }
      };
      auto gtile = [&](auto pkc, f32x4 (&x)[4], const float (&rstd)[4], const float (&mr)[4], int T, v2u (&o)[4]) {
        const f32x4 cs = *(LAS f32x4*)(L + L_CS1 + T * 64 + g * 16), bb = *(LAS f32x4*)(L + L_B1 + T * 64 + g * 16);
        if constexpr (pkc.value) gelu16p(x, rstd, mr, cs, bb, o); else gelu16(x, rstd, mr, cs, bb, o);
      };
      auto hpair = [&](auto pk0, auto pk1, f32x4 (&x)[4], f32x4 (&y)[4], const float (&rstd)[4], const float (&mr)[4], int T) {
        v2u oa[4], ob[4];
        gtile(pk0, x, rstd, mr, T, oa);
        gtile(pk1, y, rstd, mr, T + 1, ob);
#pragma unroll
        for (int tt = 0; tt < 4; ++tt) *(LAS v4u*)(L + L_H + tt * REV + (T >> 1) * 1024 + voff) = v4u{oa[tt][0], oa[tt][1], ob[tt][0], ob[tt][1]};
        SB();
      };
      auto poff = [&](int t) {
        const int d = 2 * hl + (t >> 1), i = 2 * role + (t & 1);
        return pbc + (((d != j ? d : i) * 8 + j) * 5 + (d != j ? i : 4)) * 2048;
      };
      auto ppack = [&](f32x4 (&x)[4], int p) { const v2u a = pk4(x[2 * p]), b = pk4(x[2 * p + 1]); return v4u{a[0], a[1], b[0], b[1]}; };
      auto pstore2 = [&](f32x4 (&x)[4], f32x4 (&y)[4], int t0, auto slc) {
#pragma unroll
        for (int q = 0; q < 4; ++q) {
          stc128(ppack(q < 2 ? x : y, q & 1), rs, voff + (q & 1) * 1024, poff(t0 + (q >> 1)));
          if (slc.value) { SB(); __builtin_amdgcn_s_sleep(slc.value); SB(); }
        }
        SB();
      };
      constexpr std::integral_constant<int, 0> I0{};
      constexpr std::integral_constant<int, 1> I1{};
      auto gather = [&](f32x4& sa, f32x4& sb, f32x4& ea, f32x4& eb) {
        v4u pv[7];
        const int po = pbc + (j * 8 * 5) * 2048 + up * 1024;
#pragma unroll
        for (int i = 0; i < 7; ++i) { const int src = i + (i >= j ? 1 : 0); pv[i] = ldc(rs, voff, po + (src * 5 + ui) * 2048); }
        SB();
        ea = *(LAS f32x4*)(L + L_B2 + 4 * 64 + g * 16); eb = ea;
        if (five) {
          v4u pe[8];
#pragma unroll
          for (int i = 0; i < 8; ++i) pe[i] = ldc(rs, voff, po + (i * 5 + 4) * 2048);
          SB();
#pragma unroll
          for (int i = 0; i < 8; ++i) acc8(ea, eb, pe[i]);
        }
        sa = *(LAS f32x4*)(L + L_B2 + ui * 64 + g * 16); sb = sa;
#pragma unroll
        for (int i = 0; i < 7; ++i) acc8(sa, sb, pv[i]);
      };
      auto finish = [&](f32x4 a, f32x4 b, int T) {
#pragma unroll
        for (int p = 0; p < 2; ++p) {
          const int tt = 2 * up + p;
          const int fo = tt * REV + (T >> 1) * 1024 + 8 * (T & 1);
          const v2u rv = *(LAS v2u*)(L + L_X + fo + voff);
          const v2u o = pk4((p ? b : a) + unpk4(rv[0], rv[1]));
          if (Ly == 0) stc64(o, rs, voff, X2_OFF + c * XBC + fo);
          else {
            const int tok = 2 * (64 * (c >> 1) + 16 * tt + m) + (c & 1);
            *(v2u*)(out + (size_t)tok * D_ + 16 * T + 4 * g) = o;
          }
        }
      };
      auto tail = [&](f32x4 sa, f32x4 sb, f32x4 ea, f32x4 eb) {
        if (five) finish(ea, eb, 32 + j);
        CST(19 + 32 * Ly);
        bar();
        const v2u oa = *(LAS v2u*)(L + L_OWN + (ui * 4 + 2 * up) * 512 + lane * 8), ob = *(LAS v2u*)(L + L_OWN + (ui * 4 + 2 * up + 1) * 512 + lane * 8);
        finish(sa + unpk4(oa[0], oa[1]), sb + unpk4(ob[0], ob[1]), 4 * j + ui);
      };
      CST(8 + 32 * Ly);
      if (role == 0) {
        __builtin_amdgcn_s_setprio(1);
        {
          f32x4 acc[4][4];
          seg<4, 4, 0, 18, 0, 162, true>({acc[0], acc[1], acc[2], acc[3]}, L + L_X + voff, rw, mb, voff);
          CST(9 + 32 * Ly);
          float rstd[4], mr[4];
          stats(rstd, mr);
          hpair(I0, I0, acc[0], acc[1], rstd, mr, 4 * hl);
          hpair(I0, I0, acc[2], acc[3], rstd, mr, 4 * hl + 2);
        }
        CST(10 + 32 * Ly);
        bar();
        CST(11 + 32 * Ly);
        {
          f32x4 acc[4][4];
          seg<4, 4, 0, 8, 72, 162, true>({acc[0], acc[1], acc[2], acc[3]}, L + L_H + voff, rw, mb, voff);
          CST(12 + 32 * Ly);
          bar();
          CST(13 + 32 * Ly);
          seg<2, 4, 8, 18, 104, 162, false>({acc[0], acc[1]}, L + L_H + voff, rw, mb, voff);
          pstore2(acc[0], acc[1], 0, std::integral_constant<int, TRICKLE>{});
          CST(20 + 32 * Ly);
          seg<2, 4, 8, 18, 124, 162, false>({acc[2], acc[3]}, L + L_H + voff, rw, mb, voff);
          __builtin_amdgcn_s_setprio(0);
          pstore2(acc[2], acc[3], 2, std::integral_constant<int, TRICKLE>{});
          CST(14 + 32 * Ly);
        }
        arrive<4>(ws, L, 0, (5 * Ly + 2) * 32 + c, j, tag, lane);
        CST(15 + 32 * Ly);
        if (hl == 0) { poll8<4>(ws, 5 * Ly + 2, c, c, tag, lane); *(volatile LAS unsigned*)(L + L_W + 8) = Ly + 1; }
        else lds_wait_ge<6>(L, 2, Ly + 1);
        CST(16 + 32 * Ly);
        f32x4 sa, sb, ea, eb;
        gather(sa, sb, ea, eb);
        CST(17 + 32 * Ly);
        lds_wait_ge<3>(L, 4 + hl, Ly + 1);
        CST(18 + 32 * Ly);
        f32x4 own[4];
        seg<1, 4, 0, 18, 144, 162, true>({own}, L + L_H + voff, rw, mb, voff);
#pragma unroll
        for (int tt = 0; tt < 4; ++tt) *(LAS v2u*)(L + L_OWN + (hl * 4 + tt) * 512 + lane * 8) = pk4(own[tt]);
        if (Ly == 0) fillh<0>(rw, wq0, voff);
        tail(sa, sb, ea, eb);
      }
      int role2 = role;
      asm volatile("" : "+s"(role2));
      if (role2 != 0) {
        {
          f32x4 acc[5][4];
          seg<5, 4, 0, 18, 0, 162, true>({acc[0], acc[1], acc[2], acc[3], acc[4]}, L + L_X + voff, rw, mb, voff);
          CST(9 + 32 * Ly);
          bar();
          CST(11 + 32 * Ly);
          float rstd[4], mr[4];
          stats(rstd, mr);
          hpair(I0, std::integral_constant<int, G2PK>{}, acc[0], acc[1], rstd, mr, 16 + 2 * hl);
          CST(10 + 32 * Ly);
          hpair(I1, I1, acc[2], acc[3], rstd, mr, 24 + 2 * hl);
          v2u o[4];
          gtile(I1, acc[4], rstd, mr, 32 + hl, o);
#pragma unroll
          for (int tt = 0; tt < 4; ++tt) *(LAS v2u*)(L + L_H + tt * REV + (16 + (hl >> 1)) * 1024 + voff + 8 * (hl & 1)) = o[tt];
          CST(12 + 32 * Ly);
        }
        bar();
        CST(13 + 32 * Ly);
        {
          f32x4 acc[2][4];
          seg<2, 4, 0, 18, 90, 162, true>({acc[0], acc[1]}, L + L_H + voff, rw, mb, voff);
          CST(20 + 32 * Ly);
          __builtin_amdgcn_s_setprio(1);
          StoreHook hk{{ppack(acc[0], 0), ppack(acc[0], 1), ppack(acc[1], 0), ppack(acc[1], 1)}, rs, voff, poff(0), poff(1)};
          seg<2, 4, 0, 18, 126, 162, true>({acc[0], acc[1]}, L + L_H + voff, rw, mb, voff, hk);
          *(volatile LAS unsigned*)(L + L_W + 16 + hl * 4) = Ly + 1;
          CST(23 + 32 * Ly);
          pstore2(acc[0], acc[1], 2, I0);
          CST(14 + 32 * Ly);
        }
        arrive<4>(ws, L, 1, (5 * Ly + 3) * 32 + c, j, tag, lane);
        CST(15 + 32 * Ly);
        if (hl == 0) { poll8<0>(ws, 5 * Ly + 3, c, c, tag, lane); *(volatile LAS unsigned*)(L + L_W + 12) = Ly + 1; }
        else lds_wait_ge<1>(L, 3, Ly + 1);
        CST(16 + 32 * Ly);
        f32x4 sa, sb, ea, eb;
        gather(sa, sb, ea, eb);
        tail(sa, sb, ea, eb);
        __builtin_amdgcn_s_setprio(0);
      }
      CST(22 + 32 * Ly);
      STAMP(12 + 16 * Ly);
      if (Ly == 0) {
        const int cprev = c >= 2 ? c - 2 : c;
        publish(ws, 4 * 32 + c, j, tag, tid);
        if (role) fillh<0>(rw, wq0, voff);
        fillh<1>(rw, wq0, voff);
        await(ws, 4, c, cprev, tag, w, lane);
        STAMP(17);
        if (w < 5) stage_tile(rs, X2_OFF + (w == 4 ? cprev * XBC + 3 * REV : c * XBC + w * REV), L, w == 4 ? 0 : w + 1, w == 4, lane);
        bar();
        qkv(std::integral_constant<int, 1>{});
      }
    }
  }
}

extern "C" void kernel_launch(void* const* d_in, const int* in_sizes, int n_in, void* d_out, int out_size, void* d_ws,
                              size_t ws_size, hipStream_t stream) {
  const u16* x = (const u16*)d_in[0];
  const u16* wqkv = (const u16*)d_in[1];
  const u16* wo = (const u16*)d_in[2];
  const u16* w1 = (const u16*)d_in[4];
  const u16* b1 = (const u16*)d_in[5];
  const u16* w2 = (const u16*)d_in[6];
  const u16* b2 = (const u16*)d_in[7];
  (void)hipFuncSetAttribute((const void*)k_main, hipFuncAttributeMaxDynamicSharedMemorySize, LDS_BYTES);
  k_main<<<256, 512, LDS_BYTES, stream>>>((char*)d_ws, x, wqkv, wo, w1, b1, w2, b2, (u16*)d_out);
}
```

```cpp
#define PADAT 5
#define PADUS 40
#include <hip/hip_runtime.h>
#include <hip/hip_bf16.h>
#include <type_traits>

#define GAS __attribute__((address_space(1)))
#define LAS __attribute__((address_space(3)))
typedef unsigned short u16;
typedef unsigned v4u __attribute__((ext_vector_type(4)));
typedef unsigned v2u __attribute__((ext_vector_type(2)));
typedef float f32x4 __attribute__((ext_vector_type(4)));
typedef float f32x2 __attribute__((ext_vector_type(2)));
typedef short bf16x8 __attribute__((ext_vector_type(8)));
typedef __bf16 bf16x2 __attribute__((ext_vector_type(2)));
typedef GAS unsigned gu32;
typedef unsigned long long u64;
typedef GAS u64 gu64;
typedef __amdgpu_buffer_rsrc_t rsrc_t;
#define RLX_AGENT __ATOMIC_RELAXED, __HIP_MEMORY_SCOPE_AGENT

#define TRICKLE 4
#define G2PK 0
#define TSH 20
#define GSPREAD 4

constexpr int S_ = 2048, D_ = 576, F_ = 4608;
constexpr int REV = 18 * 1024;
constexpr int XBC = 4 * REV;
constexpr int XBSZ = 32 * XBC;
constexpr int FL_OFF = 0;
constexpr int WQ_OFF = 131072;
constexpr int WO_OFF = WQ_OFF + 128 * REV;
constexpr int MS_OFF = WO_OFF + 36 * REV;
constexpr int MSJ = 72 * REV;
constexpr int CSP_OFF = MS_OFF + 8 * MSJ;
constexpr int X2_OFF = CSP_OFF + 8 * 18 * 2304 + 4096 - (8 * 18 * 2304) % 4096;
constexpr int OB_OFF = X2_OFF + XBSZ;
constexpr int X1_OFF = OB_OFF + 2 * XBSZ;
constexpr int PB_OFF = X1_OFF + 2 * XBSZ;
constexpr int PBSZ = 32 * 8 * 8 * 10240;
constexpr int ST_OFF = PB_OFF + 2 * PBSZ;
constexpr int ST2_OFF = ST_OFF + 256 * 64 * 8;
constexpr int WS_BYTES = ST2_OFF + 2 * 256 * 8 * 64 * 8;
constexpr int L_X = 0, L_H = 73728, L_ATT = 92160, ATT_STR = 10240, A_Q = 0, A_K = 2560, A_V = 5120, A_E = 7680;
constexpr int L_RES = 137216, L_OWN = 147456, L_CS1 = L_OWN + 8192, L_B1 = L_CS1 + 2304, L_STR = L_B1 + 2304,
              L_STM = L_STR + 320, L_B2 = L_STM + 320, L_W = L_B2 + 320, L_END = L_W + 64;
static_assert(L_END <= 163840, "lds");
constexpr int LDS_BYTES = 163840;

__device__ __forceinline__ float bf2f(u16 b) { return __uint_as_float(((unsigned)b) << 16); }
__device__ __forceinline__ float bflo(unsigned u) { return __uint_as_float(u << 16); }
__device__ __forceinline__ float bfhi(unsigned u) { return __uint_as_float(u & 0xffff0000u); }
__device__ __forceinline__ unsigned pk2(float a, float b) {
  f32x2 v = {a, b};
  return __builtin_bit_cast(unsigned, __builtin_convertvector(v, bf16x2));
}
__device__ __forceinline__ v2u pk4(f32x4 v) { return v2u{pk2(v[0], v[1]), pk2(v[2], v[3])}; }
__device__ __forceinline__ f32x4 unpk4(unsigned a, unsigned b) { return f32x4{bflo(a), bfhi(a), bflo(b), bfhi(b)}; }
__device__ __forceinline__ f32x4 mfma(v4u a, v4u b, f32x4 c) {
  return __builtin_amdgcn_mfma_f32_16x16x32_bf16(__builtin_bit_cast(bf16x8, a), __builtin_bit_cast(bf16x8, b), c, 0, 0, 0);
}
__device__ __forceinline__ v4u ldw(rsrc_t r, int voff, int soff) { return __builtin_amdgcn_raw_buffer_load_b128(r, voff, soff, 0); }
__device__ __forceinline__ v4u ldc(rsrc_t r, int voff, int soff) { return __builtin_amdgcn_raw_buffer_load_b128(r, voff, soff, 16); }
__device__ __forceinline__ void stc128(v4u v, rsrc_t r, int voff, int soff) {
  __builtin_amdgcn_raw_buffer_store_b128(v, r, voff, soff, 16);
  asm volatile("s_nop 1" :: "v"(v));
}
__device__ __forceinline__ void stc64(v2u v, rsrc_t r, int voff, int soff) { __builtin_amdgcn_raw_buffer_store_b64(v, r, voff, soff, 16); }
__device__ __forceinline__ void stc32(unsigned v, rsrc_t r, int voff, int soff) { __builtin_amdgcn_raw_buffer_store_b32(v, r, voff, soff, 16); }
#define SB() __builtin_amdgcn_sched_barrier(0)
__device__ __forceinline__ void bar() {
  asm volatile("s_waitcnt lgkmcnt(0)" ::: "memory");
  __builtin_amdgcn_s_barrier();
  asm volatile("" ::: "memory");
}
__device__ __forceinline__ float red16max(float x) {
  auto r = __builtin_amdgcn_permlane16_swap(__float_as_uint(x), __float_as_uint(x), false, false);
  x = fmaxf(__uint_as_float(r[0]), __uint_as_float(r[1]));
  auto q = __builtin_amdgcn_permlane32_swap(__float_as_uint(x), __float_as_uint(x), false, false);
  return fmaxf(__uint_as_float(q[0]), __uint_as_float(q[1]));
}
__device__ __forceinline__ float red16sum(float x) {
  auto r = __builtin_amdgcn_permlane16_swap(__float_as_uint(x), __float_as_uint(x), false, false);
  x = __uint_as_float(r[0]) + __uint_as_float(r[1]);
  auto q = __builtin_amdgcn_permlane32_swap(__float_as_uint(x), __float_as_uint(x), false, false);
  return __uint_as_float(q[0]) + __uint_as_float(q[1]);
}
__device__ __forceinline__ int ms_wave(int w) { return 162 * 1024 * w; }
__device__ __forceinline__ int kperm(int s, int g, int e) { return 32 * s + 4 * g + (e & 3) + ((e >> 2) << 4); }

#define STAMP(i) do { if ((i) == 0) pad_t0 = __builtin_amdgcn_s_memrealtime(); else if ((i) == PADAT) { while (__builtin_amdgcn_s_memrealtime() - pad_t0 < PADUS * 100ull) __builtin_amdgcn_s_sleep(1); } } while (0)
#define CST(i) do {} while (0)

__device__ __forceinline__ gu64* flagp(char* ws, int line, int i) { return (gu64*)(ws + FL_OFF + 256 + line * 256 + i * 8); }
__device__ __forceinline__ void publish(char* ws, int line, int i, u64 tag, int tid) {
  asm volatile("s_waitcnt vmcnt(0)" ::: "memory");
  bar();
  if (tid == 0) __hip_atomic_store(flagp(ws, line, i), tag, RLX_AGENT);
}
template <int SL> __device__ __forceinline__ void poll(gu64* p, u64 tag) {
  for (unsigned spins = 0; spins < (1u << 22); ++spins) {
    const u64 v = __hip_atomic_load(p, RLX_AGENT);
    if (__all(v == tag)) break;
    if (SL) __builtin_amdgcn_s_sleep(SL);
  }
  __builtin_amdgcn_fence(__ATOMIC_ACQUIRE, "wavefront");
}
template <int SL> __device__ __forceinline__ void poll8(char* ws, int ph, int c, int c2, u64 tag, int lane) {
  poll<SL>(flagp(ws, ph * 32 + ((lane & 8) ? c2 : c), lane & 7), tag);
}
__device__ __forceinline__ void await(char* ws, int ph, int c, int c2, u64 tag, int w, int lane) {
  if (w == 0) poll8<0>(ws, ph, c, c2, tag, lane);
  bar();
}

__device__ __forceinline__ void stat2(v4u x, float& s1, float& s2) {
  const bf16x2 ones = __builtin_bit_cast(bf16x2, 0x3f803f80u);
#pragma unroll
  for (int i = 0; i < 4; ++i) {
    const unsigned u = x[i];
    const bf16x2 v = __builtin_bit_cast(bf16x2, u);
    s1 = __builtin_amdgcn_fdot2_f32_bf16(v, ones, s1, false);
    s2 = __builtin_amdgcn_fdot2_f32_bf16(v, v, s2, false);
  }
}
__device__ __forceinline__ void stat_put(LAS char* L, int idx, float s1, float s2) {
  const float mean = s1 * (1.0f / D_);
  const float var = fmaxf(s2 * (1.0f / D_) - mean * mean, 0.f);
  const float rstd = __builtin_amdgcn_rsqf(var + 1e-5f);
  *(LAS float*)(L + L_STR + idx * 4) = rstd;
  *(LAS float*)(L + L_STM + idx * 4) = mean * rstd;
}
__device__ __forceinline__ void ones_row(LAS char* L, int lane) {
  if ((lane & 15) == 0) {
#pragma unroll
    for (int s = 0; s < 18; ++s) *(LAS v4u*)(L + L_X + s * 1024 + lane * 16) = v4u{0x3f803f80u, 0x3f803f80u, 0x3f803f80u, 0x3f803f80u};
  }
}
template <int CTRL> __device__ __forceinline__ float dppadd(float x) {
  return x + __uint_as_float(__builtin_amdgcn_update_dpp(0u, __float_as_uint(x), CTRL, 0xf, 0xf, true));
}
__device__ __forceinline__ float red8sum(float x) { return dppadd<0x141>(dppadd<0x4E>(dppadd<0xB1>(x))); }
__device__ __forceinline__ void stage_tile(rsrc_t rs, int soff, LAS char* L, int t5, bool halo, int lane) {
  v4u x[18];
#pragma unroll
  for (int s = 0; s < 18; ++s) x[s] = ldc(rs, lane * 16 + (s & 3) * 1024, soff + (s >> 2) * 4096);
  SB();
  float s1 = 0.f, s2 = 0.f;
#pragma unroll
  for (int s = 0; s < 18; ++s) {
    *(LAS v4u*)(L + L_X + t5 * REV + s * 1024 + lane * 16) = x[s];
    stat2(x[s], s1, s2);
    SB();
  }
  if (halo) ones_row(L, lane);
  s1 = red16sum(s1); s2 = red16sum(s2);
  if (lane < 16) stat_put(L, t5 * 16 + lane, s1, s2);
}

#define RING 28
constexpr int R_ = RING;
constexpr int RB = 256 - 4 * R_;
template <int SLOT, int OFF> __device__ __forceinline__ void rload(v4u rs, int voff, int so) {
  asm volatile("buffer_load_dwordx4 v[%c3:%c4], %0, %1, %2 offen offset:%c5 sc1"
               :: "v"(voff), "s"(rs), "s"(so), "i"(RB + 4 * SLOT), "i"(RB + 4 * SLOT + 3), "i"(OFF));
}
template <int N> __device__ __forceinline__ void rwait() { asm volatile("s_waitcnt vmcnt(%c0)" :: "i"(N)); }
template <int SLOT> __device__ __forceinline__ void rmfma(f32x4& acc, v4u x) {
  asm volatile("v_mfma_f32_16x16x32_bf16 %0, v[%c2:%c3], %1, %0" : "+v"(acc) : "v"(x), "i"(RB + 4 * SLOT), "i"(RB + 4 * SLOT + 3));
}
template <int SLOT> __device__ __forceinline__ void rmfma0(f32x4& acc, v4u x) {
  asm volatile("v_mfma_f32_16x16x32_bf16 %0, v[%c2:%c3], %1, 0" : "=&v"(acc) : "v"(x), "i"(RB + 4 * SLOT), "i"(RB + 4 * SLOT + 3));
}
template <int I, int N, class Fn> __device__ __forceinline__ void sfor(Fn&& f) {
  if constexpr (I < N) { f(std::integral_constant<int, I>{}); sfor<I + 1, N>(f); }
}
constexpr int cmin(int a, int b) { return a < b ? a : b; }
template <int P, int TOTAL> __device__ __forceinline__ void refill(v4u r, int mb, int voff) {
  constexpr int Q = P + R_;
  if constexpr (Q < TOTAL) rload<P % R_, (Q & 3) * 1024>(r, voff, mb + (Q >> 2) * 4096);
}
struct NoHook { template <int S> static constexpr int extra() { return 0; } template <int S> __device__ __forceinline__ void at() {} };
template <int NT, int NK, int S0, int S1, int P0, int TOTAL, bool INIT, int I0 = R_, int NKL = NK, class Hook = NoHook>
__device__ __forceinline__ void segx(f32x4* const (&acc)[NT], const LAS char* const (&xb)[NK], v4u r, int mb, int voff, Hook& hook) {
  constexpr int XD = NT >= 4 ? 1 : (NT == 1 ? 4 : 2);
  v4u xf[XD][NK];
#pragma unroll
  for (int d = 0; d < XD; ++d)
#pragma unroll
    for (int tt = 0; tt < NK; ++tt)
      if (S0 + d < S1) xf[d][tt] = *(const LAS v4u*)(xb[tt] + (S0 + d) * 1024);
  SB();
  asm volatile("s_nop 1");
  sfor<S0, S1>([&](auto sc) {
    constexpr int s = sc.value, p = P0 + (s - S0) * NT, d = (s - S0) % XD;
    constexpr int RS = (R_ - I0 + 3) / 4;
    constexpr int lag = RS > 1 ? RS : 1;
    constexpr int k = s - S0;
    constexpr int issued = k < RS ? I0 + 4 * k : cmin(TOTAL, R_ + (k < lag ? P0 : p - lag * NT));
    rwait<issued - (p + NT) + Hook::template extra<s>()>();
    if constexpr (k < RS) sfor<I0 + 4 * k, cmin(R_, I0 + 4 * k + 4)>([&](auto qc) { rload<qc.value, (qc.value & 3) * 1024>(r, voff, mb + (qc.value >> 2) * 4096); });
    hook.template at<s>();
    sfor<0, NK>([&](auto kc) {
      constexpr int tt = kc.value;
      sfor<0, NT>([&](auto tc) {
        constexpr int t = tc.value;
        if constexpr (t == NT - 1 && tt >= NKL) {}
        else if constexpr (INIT && s == S0) rmfma0<(p + t) % R_>(acc[t][tt], xf[d][tt]);
        else rmfma<(p + t) % R_>(acc[t][tt], xf[d][tt]);
        constexpr int idx = tt * NT + t;
        if constexpr (k >= lag && idx % NK == 0 && idx / NK < NT) refill<p - lag * NT + idx / NK, TOTAL>(r, mb, voff);
      });
      SB();
      if constexpr (s + XD < S1) { xf[d][tt] = *(const LAS v4u*)(xb[tt] + (s + XD) * 1024); SB(); }
    });
  });
  static_assert(I0 == R_ || P0 + (S1 - S0 - (R_ - I0 + 3) / 4) * NT + R_ >= TOTAL, "ramp: all refills are out inside the loop");
  sfor<0, NT>([&](auto tc) { refill<P0 + (S1 - 1 - S0) * NT + tc.value, TOTAL>(r, mb, voff); });
  asm volatile("s_nop 7\n\ts_nop 1");
  SB();
}
template <int NT, int NK, int S0, int S1, int P0, int TOTAL, bool INIT, class Hook = NoHook>
__device__ __forceinline__ void seg(f32x4* const (&acc)[NT], const LAS char* xb, v4u r, int mb, int voff, Hook hook = Hook{}) {
  const LAS char* x2 = xb + 2 * REV;
  asm volatile("" : "+v"(x2));
  segx<NT, 4, S0, S1, P0, TOTAL, INIT, R_, 4, Hook>(acc, {xb, xb + REV, x2, x2 + REV}, r, mb, voff, hook);
}
template <int LO> __device__ __forceinline__ void fillh(v4u r, int n, int voff) {
  sfor<LO ? R_ / 2 : 0, LO ? R_ : R_ / 2>([&](auto sc) { rload<sc.value, (sc.value & 3) * 1024>(r, voff, n + (sc.value >> 2) * 4096); });
  SB();
}
__device__ __forceinline__ v2u trread(const LAS char* p) {
  typedef short s4 __attribute__((ext_vector_type(4)));
  return __builtin_bit_cast(v2u, __builtin_amdgcn_ds_read_tr16_b64_v4i16((LAS s4*)p));
}
constexpr float AL = 0.46866869f, KK = 2.3022082f / AL;
__device__ __forceinline__ float sfma(float a, float b, float c) { float d; asm("v_fma_f32 %0, %1, %2, %3" : "=v"(d) : "v"(a), "v"(b), "v"(c)); return d; }
__device__ __forceinline__ float sfnma(float a, float b, float c) { float d; asm("v_fma_f32 %0, -%1, %2, %3" : "=v"(d) : "v"(a), "v"(b), "v"(c)); return d; }
__device__ __forceinline__ float sfmas(float a, float s) { float d; asm("v_fma_f32 %0, %1, %2, %2" : "=v"(d) : "v"(a), "s"(s)); return d; }
__device__ __forceinline__ float ssqs(float a, float s) { float d; asm("v_fma_f32 %0, %1, %1, %2" : "=v"(d) : "v"(a), "s"(s)); return d; }
__device__ __forceinline__ float smul(float a, float b) { float d; asm("v_mul_f32 %0, %1, %2" : "=v"(d) : "v"(a), "v"(b)); return d; }
#define EACH16 _Pragma("unroll") for (int tt = 0; tt < 4; ++tt) _Pragma("unroll") for (int r = 0; r < 4; ++r)
__device__ __forceinline__ void gelu16(f32x4 (&a)[4], const float (&rstd)[4], const float (&mr)[4], f32x4 cs, f32x4 bb, v2u (&o)[4]) {
  float e[4][4], u[4][4];
  EACH16 e[tt][r] = sfnma(cs[r], mr[tt], bb[r]);
  SB();
  EACH16 u[tt][r] = sfma(a[tt][r], rstd[tt], e[tt][r]);
  SB();
  EACH16 e[tt][r] = ssqs(u[tt][r], KK);
  SB();
  EACH16 e[tt][r] = smul(e[tt][r], u[tt][r]);
  SB();
  EACH16 e[tt][r] = __builtin_amdgcn_exp2f(-e[tt][r]);
  SB();
  EACH16 e[tt][r] = sfmas(e[tt][r], AL);
  SB();
  EACH16 e[tt][r] = __builtin_amdgcn_rcpf(e[tt][r]);
  SB();
  EACH16 u[tt][r] = smul(u[tt][r], e[tt][r]);
  SB();
#pragma unroll
  for (int tt = 0; tt < 4; ++tt) o[tt] = v2u{pk2(u[tt][0], u[tt][1]), pk2(u[tt][2], u[tt][3])};
  SB();
}
__device__ __forceinline__ void gelu16p(f32x4 (&a)[4], const float (&rstd)[4], const float (&mr)[4], f32x4 cs, f32x4 bb, v2u (&o)[4]) {
  f32x4 e[4];
#pragma unroll
  for (int tt = 0; tt < 4; ++tt) e[tt] = bb - cs * mr[tt];
  SB();
#pragma unroll
  for (int tt = 0; tt < 4; ++tt) a[tt] = a[tt] * rstd[tt] + e[tt];
  SB();
#pragma unroll
  for (int tt = 0; tt < 4; ++tt) e[tt] = a[tt] * a[tt] + KK;
  SB();
#pragma unroll
  for (int tt = 0; tt < 4; ++tt) e[tt] = e[tt] * a[tt];
  SB();
  EACH16 e[tt][r] = __builtin_amdgcn_exp2f(-e[tt][r]);
  SB();
#pragma unroll
  for (int tt = 0; tt < 4; ++tt) e[tt] = e[tt] * AL + AL;
  SB();
  EACH16 e[tt][r] = __builtin_amdgcn_rcpf(e[tt][r]);
  SB();
#pragma unroll
  for (int tt = 0; tt < 4; ++tt) a[tt] = a[tt] * e[tt];
  SB();
#pragma unroll
  for (int tt = 0; tt < 4; ++tt) o[tt] = pk4(a[tt]);
  SB();
}
struct StoreHook {
  v4u d[4]; rsrc_t rs; int voff, po0, po1;
  template <int S> static constexpr int extra() { return S <= 6 ? 0 : S <= 9 ? 1 : S <= 12 ? 2 : S <= 15 ? 3 : 4; }
  template <int S> __device__ __forceinline__ void at() {
    if constexpr (S == 6 || S == 9 || S == 12 || S == 15) {
      constexpr int q = (S - 6) / 3;
      SB(); stc128(d[q], rs, voff + (q & 1) * 1024, q < 2 ? po0 : po1); SB();
    }
  }
};
template <int SL> __device__ __forceinline__ void lds_wait_ge(LAS char* L, int word, unsigned want) {
  volatile LAS unsigned* p = (volatile LAS unsigned*)(L + L_W + word * 4);
  for (unsigned spins = 0; spins < (1u << 22); ++spins) {
    if (__builtin_amdgcn_readfirstlane(*p) >= want) break;
    __builtin_amdgcn_s_sleep(SL);
  }
  asm volatile("" ::: "memory");
}
template <int N> __device__ __forceinline__ void arrive(char* ws, LAS char* L, int word, int line, int i, u64 tag, int lane) {
  asm volatile("s_waitcnt vmcnt(0)" ::: "memory");
  if (lane == 0) {
    const unsigned old = __hip_atomic_fetch_add((LAS unsigned*)(L + L_W + word * 4), 1u, __ATOMIC_RELAXED, __HIP_MEMORY_SCOPE_WORKGROUP);
    if (old % N == N - 1) __hip_atomic_store(flagp(ws, line, i), tag, RLX_AGENT);
  }
}
__device__ __forceinline__ float sadd(float a, float b) { float d; asm("v_add_f32 %0, %1, %2" : "=v"(d) : "v"(a), "v"(b)); return d; }
__device__ __forceinline__ void acc8(f32x4& a, f32x4& b, v4u p) {
  a[0] = sadd(a[0], bflo(p[0])); a[1] = sadd(a[1], bfhi(p[0])); a[2] = sadd(a[2], bflo(p[1])); a[3] = sadd(a[3], bfhi(p[1]));
  b[0] = sadd(b[0], bflo(p[2])); b[1] = sadd(b[1], bfhi(p[2])); b[2] = sadd(b[2], bflo(p[3])); b[3] = sadd(b[3], bfhi(p[3]));
}

#define FRESH() int lane = lane0, c = c0, j = j0, w = w0; asm volatile("" : "+v"(lane)); asm volatile("" : "+s"(c), "+s"(j), "+s"(w)); \
  const int g = lane >> 4, m = lane & 15, voff = lane * 16, role = w >> 2, hl = w & 3; (void)g; (void)m; (void)voff; (void)role; (void)hl;

__device__ __forceinline__ void jload(int job, v4u (&r)[4], const u16* w1, const u16* w2, const u16* wo, int j, int lane) {
      const int kind = job < 162 ? 0 : (job < 324 ? 1 : 2);
      const int q = job - 162 * kind, s9 = q / 9, s = kind < 2 ? s9 : (q < 18 ? q : q - 18), B = kind < 2 ? q - 9 * s9 : (q < 18 ? j : 8);
      const u16* src = kind == 0 ? w1 + (32 * s * F_ + 576 * j) : (kind == 1 ? w2 + (576 * j + 32 * s) * D_ : wo);
      const int ld = kind == 0 ? F_ : D_;
#pragma unroll
      for (int i = 0; i < 4; ++i) {
        const int rho = 8 * i + (lane >> 3);
        const int orow = s < 16 ? 36 * s + rho + (i >= 2 ? 4 : 0) : 36 * (8 * (s - 16) + 2 * ((rho >> 2) & 3) + (i >> 1)) + 16 + (rho & 3);
        r[i] = *(const v4u*)(src + (kind < 2 ? rho : orow) * ld + 64 * B + (lane & 7) * 8);
      }
}
__device__ __forceinline__ void jput(int job, v4u (&r)[4], LAS char* scr, rsrc_t rs, int j, int lane) {
      const int g = lane >> 4, m = lane & 15, voff = lane * 16;
      const int kind = job < 162 ? 0 : (job < 324 ? 1 : 2);
      const bool one = kind == 0;
      const int q = job - 162 * kind, s9 = q / 9, s = kind < 2 ? s9 : (q < 18 ? q : q - 18), B = kind < 2 ? q - 9 * s9 : (q < 18 ? j : 8);
      const int d = B < 8 ? B : j;
      const int e0 = 162 * (d >> 1) + (s < 8 ? 72 + 4 * s + 2 * (d & 1) : 88 + 20 * (d & 1) + 2 * s), e1 = 162 * (4 + (d >> 1)) + 90 + 36 * (d & 1) + 2 * s;
      const bool spread = one ? B == 8 : B == j;
      int base = one ? (B < 4 ? 162 * B + 4 * s : (B < 8 ? 162 * (4 + 2 * (B & 1)) + 5 * s + (B >= 6 ? 2 : 0) : 652 + 5 * s)) : (spread ? 144 + s : e0);
      int c1 = spread ? 162 : 1, c2 = spread ? 324 : (one ? (B < 4 ? 2 : 162) : e1 - e0);
      base = kind < 2 ? (MS_OFF + j * MSJ) / 1024 + base : WO_OFF / 1024 + 72 * B + s;
      c1 = kind < 2 ? c1 : 18; c2 = kind < 2 ? c2 : 36;
      v2u lo[4], hi[4];
      const LAS char* p = scr + (4 * g + (m >> 2)) * 128 + (m & 3) * 8;
      *(LAS v4u*)(scr + voff) = r[0]; *(LAS v4u*)(scr + 1024 + voff) = r[1];
#pragma unroll
      for (int t = 0; t < 4; ++t) lo[t] = trread(p + t * 32);
      SB();
      *(LAS v4u*)(scr + voff) = r[2]; *(LAS v4u*)(scr + 1024 + voff) = r[3];
#pragma unroll
      for (int t = 0; t < 4; ++t) hi[t] = trread(p + t * 32);
      float cs4[4];
#pragma unroll
      for (int t = 0; t < 4; ++t) {
        const v4u f = {lo[t][0], lo[t][1], hi[t][0], hi[t][1]};
        stc128(f, rs, voff, (base + (t & 1) * c1 + (t >> 1) * c2) * 1024);
        float a = 0.f, dummy = 0.f;
        stat2(f, a, dummy);
        cs4[t] = red16sum(a);
      }
      const float csum = g < 2 ? (g == 0 ? cs4[0] : cs4[1]) : (g == 2 ? cs4[2] : cs4[3]);
      if (one) stc32(__float_as_uint(csum), rs, lane * 4, CSP_OFF + (j * 18 + s) * 2304 + B * 256);
}

struct JobHook {
  v4u jr[3][4]; const u16 *w1, *w2; int job0, j, lane;
  template <int S> static constexpr int extra() { return S <= 9 ? 0 : S <= 10 ? 4 : S <= 11 ? 8 : 12; }
  template <int S> __device__ __forceinline__ void at() {
    if constexpr (S >= 9 && S <= 11) { SB(); jload(min(job0 + 128 * (S - 9), 323), jr[S - 9], w1, w2, w1, j, lane); SB(); }
  }
};

__global__ __launch_bounds__(512, 2) __attribute__((amdgpu_num_vgpr(RB / 2)))
void k_main(char* __restrict__ ws, const u16* __restrict__ xin, const u16* __restrict__ wqkv, const u16* __restrict__ wo,
            const u16* __restrict__ w1, const u16* __restrict__ b1, const u16* __restrict__ w2, const u16* __restrict__ b2,
            u16* __restrict__ out) {
  extern __shared__ __attribute__((aligned(16))) char smem[];
  LAS char* L = (LAS char*)smem;
  const int tid = threadIdx.x, lane0 = tid & 63, w0 = __builtin_amdgcn_readfirstlane(tid >> 6);
  int c0, j0;
  {
    constexpr int G = GSPREAD, per = 8 / G;
    const int b = blockIdx.x, x = b & 7, r = b >> 3;
    j0 = (x % G) * per + (r % per);
    c0 = (x / G) * (4 * G) + r / per;
  }
  const rsrc_t rs = __builtin_amdgcn_make_buffer_rsrc((void*)ws, 0, WS_BYTES, 0x00020000);
  const v4u rw = {(unsigned)(size_t)ws, (unsigned)((size_t)ws >> 32) & 0xffffu, (unsigned)WS_BYTES, 0x00020000u};
  asm volatile("" ::: "v255");
  unsigned long long pad_t0 = 0; (void)pad_t0;
  STAMP(0);
  CST(24);
  u64 tag = 0;
  auto qkv = [&](auto lc) {
    constexpr int Ly = lc.value;
    STAMP(2 + 16 * Ly);
    FRESH();
    if (role == 0) {
#pragma unroll
      for (int i = 0; i < 5; ++i) {
        const int T = i < 4 ? 4 * j + i : 32 + (j & 3);
        v2u v = *(LAS v2u*)(L + L_X + (hl + 1) * REV + (T >> 1) * 1024 + voff + 8 * (T & 1));
        *(LAS v2u*)(L + L_RES + (i * 4 + hl) * 512 + lane * 8) = v;
      }
    }
    const int qb = WQ_OFF + ((4 * j + hl) * 4 + 2 * role) * REV;
    constexpr int I0 = Ly == 0 ? 8 : R_;
    auto fix = [&](f32x4 a, const float (&cs)[4], float rstd, float mr) {
      float u[4];
#pragma unroll
      for (int r = 0; r < 4; ++r) u[r] = smul(a[r], rstd);
#pragma unroll
      for (int r = 0; r < 4; ++r) u[r] = sfnma(cs[r], mr, u[r]);
      return v2u{pk2(u[0], u[1]), pk2(u[2], u[3])};
    };
    auto bcast = [&](f32x4 a, float (&cs)[4]) {
#pragma unroll
      for (int r = 0; r < 4; ++r) cs[r] = __uint_as_float(__builtin_amdgcn_update_dpp(0u, __float_as_uint(a[r]), 0x150, 0xf, 0xf, false));
    };
    CST(0 + 32 * Ly);
    if (role == 0) {
      f32x4 a0[5], a1[5];
      const LAS char* xa = L + L_X + voff, *xc = xa + 3 * REV;
      asm volatile("" : "+v"(xc));
      NoHook hk;
      segx<2, 5, 0, 18, 0, 36, true, I0, 5>({a0, a1}, {xa, xa + REV, xa + 2 * REV, xc, xc + REV}, rw, qb, voff, hk);
      CST(1 + 32 * Ly);
      LAS char* A = L + L_ATT + hl * ATT_STR + lane * 8;
      float rstd[5], mr[5], cs0[4], cs1[4];
#pragma unroll
      for (int t = 0; t < 5; ++t) { rstd[t] = *(LAS float*)(L + L_STR + t * 64 + m * 4); mr[t] = *(LAS float*)(L + L_STM + t * 64 + m * 4); }
      bcast(a0[0], cs0); bcast(a1[0], cs1);
      SB();
#pragma unroll
      for (int t = 0; t < 5; ++t) {
        if (t) *(LAS v2u*)(A + A_Q + t * 512) = fix(a0[t], cs0, rstd[t], mr[t]);
        *(LAS v2u*)(A + A_K + t * 512) = fix(a1[t], cs1, rstd[t], mr[t]);
      }
    }
    int role2 = role;
    asm volatile("" : "+s"(role2));
    if (role2) {
      f32x4 a0[5], a1[5];
      const int ta = (hl & 1) ? 3 : 1, tb = 4 - ta;
      const LAS char* x0 = L + L_X + voff, *xa = x0 + ta * REV, *xc = x0 + tb * REV;
      std::conditional_t<Ly == 0, JobHook, NoHook> hk;
      if constexpr (Ly == 0) { hk.w1 = w1; hk.w2 = w2; hk.job0 = c * 4 + hl; hk.j = j; hk.lane = lane; }
      __builtin_amdgcn_s_setprio(1);
      segx<2, 5, 0, 18, 0, 36, true, I0, 3>({a0, a1}, {x0, xa, xa + REV, xc, xc + REV}, rw, qb, voff, hk);
      __builtin_amdgcn_s_setprio(0);
      CST(1 + 32 * Ly);
      LAS char* A = L + L_ATT + hl * ATT_STR + A_V + m * 32 + g * 8, *E = L + L_ATT + (hl & 2) * ATT_STR + A_E + m * 32 + g * 8;
      float rstd[5], mr[5], cs0[4], cs1[4];
      int to[5] = {0, ta, ta + 1, tb, tb + 1};
#pragma unroll
      for (int t = 0; t < 5; ++t) { rstd[t] = *(LAS float*)(L + L_STR + to[t] * 64 + m * 4); mr[t] = *(LAS float*)(L + L_STM + to[t] * 64 + m * 4); }
      bcast(a0[0], cs0); bcast(a1[0], cs1);
      SB();
#pragma unroll
      for (int t = 0; t < 5; ++t) {
        *(LAS v2u*)(A + to[t] * 512) = fix(a0[t], cs0, rstd[t], mr[t]);
        if (t < 3) *(LAS v2u*)(E + to[t] * 512) = fix(a1[t], cs1, rstd[t], mr[t]);
      }
      if (Ly) fillh<0>(rw, MS_OFF + j * MSJ + ms_wave(w), voff);
      if constexpr (Ly == 0) {
#pragma unroll
        for (int i = 0; i < 3; ++i) jput(min(c * 4 + hl + 128 * i, 323), hk.jr[i], L + L_OWN + hl * 2048, rs, j, lane);
      }
    }
  };
  {
    FRESH();
    u64 gen = 0;
    if (w >= 5) gen = __hip_atomic_load((gu64*)(ws + FL_OFF), RLX_AGENT);
    if (w == 4) {
      u64 pc;
      asm volatile("s_getpc_b64 %0" : "=s"(pc));
      const char* cp = (const char*)(pc & ~(u64)127) + (blockIdx.x >> 3) * 1536 + min(lane, 11) * 128;
      asm volatile("global_load_dword v255, %0, off" :: "v"(cp));
      asm volatile("buffer_load_dword v254, %0, %1, 0 offen sc1" :: "v"((lane << TSH) + 65536), "s"(rw));
    }
    if (w >= 5 && c < 18) {
      const int sec = w - 5;
      const u16* src = wqkv + (32 * c) * (3 * D_) + 72 * j;
      v4u qr[5];
      v2u xr[2];
#pragma unroll
      for (int i = 0; i < 5; ++i) {
        const int ch = min(64 * i + lane, 287), row = (ch * 7282) >> 16;
        qr[i] = *(const v4u*)(src + row * (3 * D_) + sec * D_ + (ch - 9 * row) * 8);
      }
#pragma unroll
      for (int i = 0; i < 2; ++i) xr[i] = *(const v2u*)(src + (16 * i + (lane >> 2)) * (3 * D_) + min(lane & 3, 2) * D_ + 36 * (sec & 1) + 16);
      SB();
      bar();
      CST(25);
      LAS char* scr = L + L_ATT + 40960 + sec * 4736;
#pragma unroll
      for (int i = 0; i < 5; ++i) *(LAS v4u*)(scr + min(64 * i + lane, 287) * 16) = qr[i];
      const int qd = WQ_OFF + 16 * j * REV + (sec >> 1) * 2 * REV + (2 * c + (sec & 1)) * 1024;
#pragma unroll
      for (int h = 0; h < 4; ++h) {
        const LAS char* p = scr + (4 * g + (m >> 2)) * 144 + (h >> 1) * 72 + (h & 1) * 40 + (m & 3) * 8;
        const v2u lo = trread(p), hi = trread(p + 16 * 144);
        stc128(v4u{lo[0], lo[1], hi[0], hi[1]}, rs, voff, qd + h * 4 * REV);
      }
      if (sec < 2) {
#pragma unroll
        for (int i = 0; i < 2; ++i) *(LAS v2u*)(scr + i * 512 + lane * 8) = (lane & 3) == 3 ? v2u{0u, 0u} : xr[i];
        const v2u lo = trread(scr + lane * 8), hi = trread(scr + 512 + lane * 8);
        const int md = WQ_OFF + (16 * j + 8 * sec + 2) * REV + (2 * c + 1) * 1024;
        stc128(v4u{lo[0], lo[1], hi[0], hi[1]}, rs, voff, md);
        stc128(v4u{lo[0], lo[1], hi[0], hi[1]}, rs, voff, md + 4 * REV);
      }
      CST(26);
      arrive<3>(ws, L, 9, 320 + j, c, gen + 1, lane);
      CST(27);
    }
    int wl = w;
    asm volatile("" : "+s"(wl));
    if (wl >= 5 && c >= 18) {
      const int job = min(324 + (c - 18) * 3 + w - 5, j < 4 ? 359 : 341);
      v4u jo[4];
      jload(job, jo, w1, w2, wo, j, lane);
      bar();
      jput(job, jo, L + L_ATT + 40960 + (w - 5) * 4096, rs, j, lane);
      arrive<3>(ws, L, 9, 320 + j, c, gen + 1, lane);
    }
    asm volatile("" : "+s"(wl));
    if (wl < 5) {
      const int cc = (w == 4 && c >= 2) ? c - 2 : c, tt = w == 4 ? 3 : w, t5 = w == 4 ? 0 : w + 1;
      const int r8 = lane >> 3, q8 = lane & 7;
      const u16* src = xin + (size_t)(2 * (64 * (cc >> 1) + 16 * tt + r8) + (cc & 1)) * D_ + 8 * q8;
      if (tid < 16) *(LAS unsigned*)(L + L_W + tid * 4) = 0u;
      bar();
      v4u x[2][9];
#pragma unroll
      for (int i = 0; i < 9; ++i) { x[0][i] = *(const v4u*)(src + 64 * i); x[1][i] = *(const v4u*)(src + 16 * D_ + 64 * i); }
      SB();
      CST(25);
      float s1[2] = {0.f, 0.f}, s2[2] = {0.f, 0.f};
      LAS char* dst = L + L_X + t5 * REV + (q8 >> 2) * 1024 + ((q8 & 1) * 32 + r8) * 16 + ((q8 >> 1) & 1) * 8;
#pragma unroll
      for (int i = 0; i < 9; ++i)
#pragma unroll
        for (int h = 0; h < 2; ++h) {
          *(LAS v2u*)(dst + i * 2048 + h * 128) = v2u{x[h][i][0], x[h][i][1]};
          *(LAS v2u*)(dst + i * 2048 + h * 128 + 256) = v2u{x[h][i][2], x[h][i][3]};
          stat2(x[h][i], s1[h], s2[h]);
          SB();
        }
      if (w == 4) ones_row(L, lane);
#pragma unroll
      for (int h = 0; h < 2; ++h) { s1[h] = red8sum(s1[h]); s2[h] = red8sum(s2[h]); }
      if (q8 < 2) stat_put(L, t5 * 16 + q8 * 8 + r8, q8 ? s1[1] : s1[0], q8 ? s2[1] : s2[0]);
      CST(26);
    }
    if (w == 6) {
      unsigned bv[5];
#pragma unroll
      for (int i = 0; i < 5; ++i) bv[i] = *(const unsigned*)(b1 + 576 * j + min(lane + 64 * i, 287) * 2);
      const unsigned b2v = *(const unsigned*)(b2 + (lane < 32 ? 64 * j + 2 * lane : 512 + 16 * (j & 3) + 2 * (lane & 7)));
#pragma unroll
      for (int i = 0; i < 5; ++i) *(LAS f32x2*)(L + L_B1 + min(lane + 64 * i, 287) * 8) = f32x2{AL * bflo(bv[i]), AL * bfhi(bv[i])};
      if (lane < 40) *(LAS f32x2*)(L + L_B2 + lane * 8) = f32x2{bflo(b2v), bfhi(b2v)};
    }
    STAMP(1);
    CST(29);
    if (w == 7) {
      poll<0>(flagp(ws, 320 + j, min(lane, 17)), gen + 1);
      if (lane == 0) *(LAS u64*)(L + L_W + 48) = gen + 1;
    }
    CST(30);
    bar();
    CST(31);
    sfor<0, 8>([&](auto qc) { rload<qc.value, (qc.value & 3) * 1024>(rw, voff, WQ_OFF + ((4 * j + hl) * 4 + 2 * role) * REV + (qc.value >> 2) * 4096); });
    tag = *(LAS u64*)(L + L_W + 48);
    tag = ((u64)(unsigned)__builtin_amdgcn_readfirstlane((unsigned)(tag >> 32)) << 32) | (unsigned)__builtin_amdgcn_readfirstlane((unsigned)tag);
    qkv(std::integral_constant<int, 0>{});
  }

#pragma unroll 1
  for (int Ly = 0; Ly < 2; ++Ly) {
    CST(2 + 32 * Ly);
    bar();
    CST(3 + 32 * Ly);
    STAMP(3 + 16 * Ly);
    {
      FRESH();
      const LAS char* A = L + L_ATT + hl * ATT_STR + role * 1024;
      const float sc = 0.23570226039551584f * 1.4426950408889634f;
#pragma unroll
      for (int q = 0; q < 2; ++q) {
        const int t = 2 * role + q;
        const LAS char* At = A + q * 512;
        const v2u qm = *(const LAS v2u*)(At + A_Q + 512 + lane * 8);
        const v2u kpm = *(const LAS v2u*)(At + A_K + lane * 8), kcm = *(const LAS v2u*)(At + A_K + 512 + lane * 8);
        const LAS char* Et = At + A_E - (hl & 1) * ATT_STR;
        const LAS char* Ae = Et + m * 32 + (hl & 1) * 4;
        const unsigned xq = *(const LAS unsigned*)(Ae + 512), xkp = *(const LAS unsigned*)(Ae + 8), xkc = *(const LAS unsigned*)(Ae + 512 + 8);
        const v2u vp = trread(At + A_V + lane * 8), vc = trread(At + A_V + 512 + lane * 8);
        const v2u ep = trread(Et + lane * 8), ec = trread(Et + 512 + lane * 8);
        const v4u qf = {qm[0], qm[1], g == 0 ? xq : 0u, 0u};
        const v4u kp = {kpm[0], kpm[1], g == 0 ? xkp : 0u, 0u}, kc = {kcm[0], kcm[1], g == 0 ? xkc : 0u, 0u};
        const f32x4 z = {0.f, 0.f, 0.f, 0.f};
        f32x4 sp = mfma(kp, qf, z), scur = mfma(kc, qf, z);
        const bool noprev = (c < 2) && (t == 0);
        float mx = -3.0e38f;
        bool okp[4], okc[4];
#pragma unroll
        for (int r = 0; r < 4; ++r) {
          const int kk = 4 * g + r;
          okp[r] = (kk >= m + 9) && !noprev;
          okc[r] = (kk <= m) && (kk >= m - 7);
          mx = fmaxf(mx, okp[r] ? sp[r] : -3.0e38f);
          mx = fmaxf(mx, okc[r] ? scur[r] : -3.0e38f);
        }
        mx = red16max(mx);
        float sum = 0.f;
#pragma unroll
        for (int r = 0; r < 4; ++r) {
          sp[r] = okp[r] ? __builtin_amdgcn_exp2f((sp[r] - mx) * sc) : 0.f;
          scur[r] = okc[r] ? __builtin_amdgcn_exp2f((scur[r] - mx) * sc) : 0.f;
          sum += sp[r] + scur[r];
        }
        sum = red16sum(sum);
        const float inv = __builtin_amdgcn_rcpf(sum);
        const v2u pp = pk4(sp * inv), pc = pk4(scur * inv);
        const v4u pf = {pp[0], pp[1], pc[0], pc[1]};
        const f32x4 o = mfma(v4u{vp[0], vp[1], vc[0], vc[1]}, pf, z);
        const f32x4 oe = mfma(v4u{ep[0], ep[1], ec[0], ec[1]}, pf, z);
        const int ob = OB_OFF + Ly * XBSZ + c * XBC + t * REV;
        stc64(pk4(o), rs, voff + 8 * (hl & 1), ob + (2 * j + (hl >> 1)) * 1024);
        if (g == 2) stc32((hl & 1) ? pk2(oe[2], oe[3]) : pk2(oe[0], oe[1]), rs, m * 16 + 4 * hl, ob + (16 + (j >> 2)) * 1024 + (j & 3) * 256);
      }
      CST(4 + 32 * Ly);
      STAMP(4 + 16 * Ly);
      publish(ws, (5 * Ly + 0) * 32 + c, j, tag, tid);
      if (Ly == 0) {
        if (tid == 0) __hip_atomic_store(flagp(ws, 328 + j, c), tag, RLX_AGENT);
        if (w == 1) { poll<1>(flagp(ws, 320 + j, lane & 31), tag); *(volatile LAS unsigned*)(L + L_W + 32) = 1u; }
        else if (w) lds_wait_ge<1>(L, 8, 1u);
      }
      if (w) {
        const int nblk = j < 4 ? 90 : 72;
        v4u wv[13];
#pragma unroll
        for (int i = 0; i < 13; ++i) {
          const int b = min(w - 1 + 7 * i, nblk - 1);
          wv[i] = ldc(rs, voff, b < 72 ? WO_OFF + 4 * j * REV + b * 1024 : WO_OFF + (32 + j) * REV + (b - 72) * 1024);
        }
        SB();
#pragma unroll
        for (int i = 0; i < 13; ++i) *(LAS v4u*)(L + L_X + min(w - 1 + 7 * i, nblk - 1) * 1024 + voff) = wv[i];
      }
      if (Ly && role == 0 && w) fillh<0>(rw, MS_OFF + j * MSJ + ms_wave(w), voff);
      await(ws, 5 * Ly + 0, c, c, tag, w, lane);
    }
    STAMP(5 + 16 * Ly);
    {
      FRESH();
      v4u of[9];
      const int ob = OB_OFF + Ly * XBSZ + c * XBC + hl * REV + role * 9 * 1024;
#pragma unroll
      for (int s = 0; s < 9; ++s) of[s] = ldc(rs, voff + (s & 3) * 1024, ob + (s >> 2) * 4096);
      SB();
      f32x4 acc[5];
      const LAS char* wb = L + L_X + role * 9 * 1024 + voff;
#pragma unroll
      for (int i = 0; i < 5; ++i) acc[i] = f32x4{0.f, 0.f, 0.f, 0.f};
#pragma unroll
      for (int s = 0; s < 9; ++s) {
#pragma unroll
        for (int i = 0; i < 4; ++i) acc[i] = mfma(*(const LAS v4u*)(wb + i * REV + s * 1024), of[s], acc[i]);
        if (s % 3 == 2) SB();
      }
      if (j < 4) {
#pragma unroll
        for (int s = 0; s < 9; ++s) acc[4] = mfma(*(const LAS v4u*)(wb + 4 * REV + s * 1024), of[s], acc[4]);
      }
      LAS char* R = L + L_ATT + hl * 5120 + voff;
      if (role == 1) {
#pragma unroll
        for (int i = 0; i < 5; ++i) *(LAS f32x4*)(R + i * 1024) = acc[i];
      }
      bar();
      if (role == 0) {
        v2u o[5];
#pragma unroll
        for (int i = 0; i < 5; ++i) {
          const v2u rv = *(LAS v2u*)(L + L_RES + (i * 4 + hl) * 512 + lane * 8);
          o[i] = pk4(acc[i] + *(LAS f32x4*)(R + i * 1024) + unpk4(rv[0], rv[1]));
        }
        const int xb = X1_OFF + Ly * XBSZ + c * XBC + hl * REV;
        stc128(v4u{o[0][0], o[0][1], o[1][0], o[1][1]}, rs, voff, xb + (2 * j) * 1024);
        stc128(v4u{o[2][0], o[2][1], o[3][0], o[3][1]}, rs, voff, xb + (2 * j + 1) * 1024);
        if (j < 4) stc64(o[4], rs, voff + 8 * (j & 1), xb + (16 + (j >> 1)) * 1024);
      }
      STAMP(6 + 16 * Ly);
      publish(ws, (5 * Ly + 1) * 32 + c, j, tag, tid);
      if (Ly == 0) {
        if (w == 1) { poll<1>(flagp(ws, 328 + j, lane & 31), tag); *(volatile LAS unsigned*)(L + L_W + 32) = 2u; }
        else lds_wait_ge<1>(L, 8, 2u);
        fillh<0>(rw, MS_OFF + j * MSJ + ms_wave(w), voff);
        if (w >= 5) {
          const int t = (w - 5) * 64 + lane;
#pragma unroll
          for (int i = 0; i < 3; ++i) {
            float v[18], a = 0.f;
#pragma unroll
            for (int s = 0; s < 18; ++s) v[s] = __uint_as_float(__builtin_amdgcn_raw_buffer_load_b32(rs, (t + 192 * i) * 4, CSP_OFF + (j * 18 + s) * 2304, 16));
#pragma unroll
            for (int s = 0; s < 18; ++s) a += v[s];
            *(LAS float*)(L + L_CS1 + (t + 192 * i) * 4) = a;
          }
        }
      }
      if (Ly && w == 0) fillh<0>(rw, MS_OFF + j * MSJ + ms_wave(w), voff);
      if (w >= 4) fillh<1>(rw, MS_OFF + j * MSJ + ms_wave(w), voff);
      await(ws, 5 * Ly + 1, c, c, tag, w, lane);
      if (Ly == 0 && blockIdx.x == 0 && tid == 0) __hip_atomic_store((gu64*)(ws + FL_OFF), tag, RLX_AGENT);
    }
    STAMP(7 + 16 * Ly);
    {
      FRESH();
      if (w < 4) { stage_tile(rs, X1_OFF + Ly * XBSZ + c * XBC + w * REV, L, w, false, lane); fillh<1>(rw, MS_OFF + j * MSJ + ms_wave(w), voff); }
    }
    bar();
    STAMP(8 + 16 * Ly);
    {
      FRESH();
      const int mb = MS_OFF + j * MSJ + ms_wave(w);
      const int pbc = PB_OFF + Ly * PBSZ + c * (64 * 10240);
      const int wq0 = WQ_OFF + ((4 * j + hl) * 4 + 2 * role) * REV;
      const int ui = 2 * role + (hl >> 1), up = hl & 1;
      const bool five = (j < 4) && ((j >> 1) == role) && (hl < 2);
      auto stats = [&](float (&rstd)[4], float (&mr)[4]) {
#pragma unroll
        for (int tt = 0; tt < 4; ++tt) { rstd[tt] = AL * *(LAS float*)(L + L_STR + tt * 64 + m * 4); mr[tt] = AL * *(LAS float*)(L + L_STM + tt * 64 + m * 4); }
      };
      auto gtile = [&](auto pkc, f32x4 (&x)[4], const float (&rstd)[4], const float (&mr)[4], int T, v2u (&o)[4]) {
        const f32x4 cs = *(LAS f32x4*)(L + L_CS1 + T * 64 + g * 16), bb = *(LAS f32x4*)(L + L_B1 + T * 64 + g * 16);
        if constexpr (pkc.value) gelu16p(x, rstd, mr, cs, bb, o); else gelu16(x, rstd, mr, cs, bb, o);
      };
      auto hpair = [&](auto pk0, auto pk1, f32x4 (&x)[4], f32x4 (&y)[4], const float (&rstd)[4], const float (&mr)[4], int T) {
        v2u oa[4], ob[4];
        gtile(pk0, x, rstd, mr, T, oa);
        gtile(pk1, y, rstd, mr, T + 1, ob);
#pragma unroll
        for (int tt = 0; tt < 4; ++tt) *(LAS v4u*)(L + L_H + tt * REV + (T >> 1) * 1024 + voff) = v4u{oa[tt][0], oa[tt][1], ob[tt][0], ob[tt][1]};
        SB();
      };
      auto poff = [&](int t) {
        const int d = 2 * hl + (t >> 1), i = 2 * role + (t & 1);
        return pbc + (((d != j ? d : i) * 8 + j) * 5 + (d != j ? i : 4)) * 2048;
      };
      auto ppack = [&](f32x4 (&x)[4], int p) { const v2u a = pk4(x[2 * p]), b = pk4(x[2 * p + 1]); return v4u{a[0], a[1], b[0], b[1]}; };
      auto pstore2 = [&](f32x4 (&x)[4], f32x4 (&y)[4], int t0, auto slc) {
#pragma unroll
        for (int q = 0; q < 4; ++q) {
          stc128(ppack(q < 2 ? x : y, q & 1), rs, voff + (q & 1) * 1024, poff(t0 + (q >> 1)));
          if (slc.value) { SB(); __builtin_amdgcn_s_sleep(slc.value); SB(); }
        }
        SB();
      };
      constexpr std::integral_constant<int, 0> I0{};
      constexpr std::integral_constant<int, 1> I1{};
      auto gather = [&](f32x4& sa, f32x4& sb, f32x4& ea, f32x4& eb) {
        v4u pv[7];
        const int po = pbc + (j * 8 * 5) * 2048 + up * 1024;
#pragma unroll
        for (int i = 0; i < 7; ++i) { const int src = i + (i >= j ? 1 : 0); pv[i] = ldc(rs, voff, po + (src * 5 + ui) * 2048); }
        SB();
        ea = *(LAS f32x4*)(L + L_B2 + 4 * 64 + g * 16); eb = ea;
        if (five) {
          v4u pe[8];
#pragma unroll
          for (int i = 0; i < 8; ++i) pe[i] = ldc(rs, voff, po + (i * 5 + 4) * 2048);
          SB();
#pragma unroll
          for (int i = 0; i < 8; ++i) acc8(ea, eb, pe[i]);
        }
        sa = *(LAS f32x4*)(L + L_B2 + ui * 64 + g * 16); sb = sa;
#pragma unroll
        for (int i = 0; i < 7; ++i) acc8(sa, sb, pv[i]);
      };
      auto finish = [&](f32x4 a, f32x4 b, int T) {
#pragma unroll
        for (int p = 0; p < 2; ++p) {
          const int tt = 2 * up + p;
          const int fo = tt * REV + (T >> 1) * 1024 + 8 * (T & 1);
          const v2u rv = *(LAS v2u*)(L + L_X + fo + voff);
          const v2u o = pk4((p ? b : a) + unpk4(rv[0], rv[1]));
          if (Ly == 0) stc64(o, rs, voff, X2_OFF + c * XBC + fo);
          else {
            const int tok = 2 * (64 * (c >> 1) + 16 * tt + m) + (c & 1);
            *(v2u*)(out + (size_t)tok * D_ + 16 * T + 4 * g) = o;
          }
        }
      };
      auto tail = [&](f32x4 sa, f32x4 sb, f32x4 ea, f32x4 eb) {
        if (five) finish(ea, eb, 32 + j);
        CST(19 + 32 * Ly);
        bar();
        const v2u oa = *(LAS v2u*)(L + L_OWN + (ui * 4 + 2 * up) * 512 + lane * 8), ob = *(LAS v2u*)(L + L_OWN + (ui * 4 + 2 * up + 1) * 512 + lane * 8);
        finish(sa + unpk4(oa[0], oa[1]), sb + unpk4(ob[0], ob[1]), 4 * j + ui);
      };
      CST(8 + 32 * Ly);
      if (role == 0) {
        __builtin_amdgcn_s_setprio(1);
        {
          f32x4 acc[4][4];
          seg<4, 4, 0, 18, 0, 162, true>({acc[0], acc[1], acc[2], acc[3]}, L + L_X + voff, rw, mb, voff);
          CST(9 + 32 * Ly);
          float rstd[4], mr[4];
          stats(rstd, mr);
          hpair(I0, I0, acc[0], acc[1], rstd, mr, 4 * hl);
          hpair(I0, I0, acc[2], acc[3], rstd, mr, 4 * hl + 2);
        }
        CST(10 + 32 * Ly);
        bar();
        CST(11 + 32 * Ly);
        {
          f32x4 acc[4][4];
          seg<4, 4, 0, 8, 72, 162, true>({acc[0], acc[1], acc[2], acc[3]}, L + L_H + voff, rw, mb, voff);
          CST(12 + 32 * Ly);
          bar();
          CST(13 + 32 * Ly);
          seg<2, 4, 8, 18, 104, 162, false>({acc[0], acc[1]}, L + L_H + voff, rw, mb, voff);
          pstore2(acc[0], acc[1], 0, std::integral_constant<int, TRICKLE>{});
          CST(20 + 32 * Ly);
          seg<2, 4, 8, 18, 124, 162, false>({acc[2], acc[3]}, L + L_H + voff, rw, mb, voff);
          __builtin_amdgcn_s_setprio(0);
          pstore2(acc[2], acc[3], 2, std::integral_constant<int, TRICKLE>{});
          CST(14 + 32 * Ly);
        }
        arrive<4>(ws, L, 0, (5 * Ly + 2) * 32 + c, j, tag, lane);
        CST(15 + 32 * Ly);
        if (hl == 0) { poll8<4>(ws, 5 * Ly + 2, c, c, tag, lane); *(volatile LAS unsigned*)(L + L_W + 8) = Ly + 1; }
        else lds_wait_ge<6>(L, 2, Ly + 1);
        CST(16 + 32 * Ly);
        f32x4 sa, sb, ea, eb;
        gather(sa, sb, ea, eb);
        CST(17 + 32 * Ly);
        lds_wait_ge<3>(L, 4 + hl, Ly + 1);
        CST(18 + 32 * Ly);
        f32x4 own[4];
        seg<1, 4, 0, 18, 144, 162, true>({own}, L + L_H + voff, rw, mb, voff);
#pragma unroll
        for (int tt = 0; tt < 4; ++tt) *(LAS v2u*)(L + L_OWN + (hl * 4 + tt) * 512 + lane * 8) = pk4(own[tt]);
        if (Ly == 0) fillh<0>(rw, wq0, voff);
        tail(sa, sb, ea, eb);
      }
      int role2 = role;
      asm volatile("" : "+s"(role2));
      if (role2 != 0) {
        {
          f32x4 acc[5][4];
          seg<5, 4, 0, 18, 0, 162, true>({acc[0], acc[1], acc[2], acc[3], acc[4]}, L + L_X + voff, rw, mb, voff);
          CST(9 + 32 * Ly);
          bar();
          CST(11 + 32 * Ly);
          float rstd[4], mr[4];
          stats(rstd, mr);
          hpair(I0, std::integral_constant<int, G2PK>{}, acc[0], acc[1], rstd, mr, 16 + 2 * hl);
          CST(10 + 32 * Ly);
          hpair(I1, I1, acc[2], acc[3], rstd, mr, 24 + 2 * hl);
          v2u o[4];
          gtile(I1, acc[4], rstd, mr, 32 + hl, o);
#pragma unroll
          for (int tt = 0; tt < 4; ++tt) *(LAS v2u*)(L + L_H + tt * REV + (16 + (hl >> 1)) * 1024 + voff + 8 * (hl & 1)) = o[tt];
          CST(12 + 32 * Ly);
        }
        bar();
        CST(13 + 32 * Ly);
        {
          f32x4 acc[2][4];
          seg<2, 4, 0, 18, 90, 162, true>({acc[0], acc[1]}, L + L_H + voff, rw, mb, voff);
          CST(20 + 32 * Ly);
          __builtin_amdgcn_s_setprio(1);
          StoreHook hk{{ppack(acc[0], 0), ppack(acc[0], 1), ppack(acc[1], 0), ppack(acc[1], 1)}, rs, voff, poff(0), poff(1)};
          seg<2, 4, 0, 18, 126, 162, true>({acc[0], acc[1]}, L + L_H + voff, rw, mb, voff, hk);
          *(volatile LAS unsigned*)(L + L_W + 16 + hl * 4) = Ly + 1;
          CST(23 + 32 * Ly);
          pstore2(acc[0], acc[1], 2, I0);
          CST(14 + 32 * Ly);
        }
        arrive<4>(ws, L, 1, (5 * Ly + 3) * 32 + c, j, tag, lane);
        CST(15 + 32 * Ly);
        if (hl == 0) { poll8<0>(ws, 5 * Ly + 3, c, c, tag, lane); *(volatile LAS unsigned*)(L + L_W + 12) = Ly + 1; }
        else lds_wait_ge<1>(L, 3, Ly + 1);
        CST(16 + 32 * Ly);
        f32x4 sa, sb, ea, eb;
        gather(sa, sb, ea, eb);
        tail(sa, sb, ea, eb);
        __builtin_amdgcn_s_setprio(0);
      }
      CST(22 + 32 * Ly);
      STAMP(12 + 16 * Ly);
      if (Ly == 0) {
        const int cprev = c >= 2 ? c - 2 : c;
        publish(ws, 4 * 32 + c, j, tag, tid);
        if (role) fillh<0>(rw, wq0, voff);
        fillh<1>(rw, wq0, voff);
        await(ws, 4, c, cprev, tag, w, lane);
        STAMP(17);
        if (w < 5) stage_tile(rs, X2_OFF + (w == 4 ? cprev * XBC + 3 * REV : c * XBC + w * REV), L, w == 4 ? 0 : w + 1, w == 4, lane);
        bar();
        qkv(std::integral_constant<int, 1>{});
      }
    }
  }
}

extern "C" void kernel_launch(void* const* d_in, const int* in_sizes, int n_in, void* d_out, int out_size, void* d_ws,
                              size_t ws_size, hipStream_t stream) {
  const u16* x = (const u16*)d_in[0];
  const u16* wqkv = (const u16*)d_in[1];
  const u16* wo = (const u16*)d_in[2];
  const u16* w1 = (const u16*)d_in[4];
  const u16* b1 = (const u16*)d_in[5];
  const u16* w2 = (const u16*)d_in[6];
  const u16* b2 = (const u16*)d_in[7];
  (void)hipFuncSetAttribute((const void*)k_main, hipFuncAttributeMaxDynamicSharedMemorySize, LDS_BYTES);
  k_main<<<256, 512, LDS_BYTES, stream>>>((char*)d_ws, x, wqkv, wo, w1, b1, w2, b2, (u16*)d_out);
}
```

```cpp
#include <hip/hip_runtime.h>
#include <hip/hip_bf16.h>
#include <type_traits>

#define GAS __attribute__((address_space(1)))
#define LAS __attribute__((address_space(3)))
typedef unsigned short u16;
typedef unsigned v4u __attribute__((ext_vector_type(4)));
typedef unsigned v2u __attribute__((ext_vector_type(2)));
typedef float f32x4 __attribute__((ext_vector_type(4)));
typedef float f32x2 __attribute__((ext_vector_type(2)));
typedef short bf16x8 __attribute__((ext_vector_type(8)));
typedef __bf16 bf16x2 __attribute__((ext_vector_type(2)));
typedef GAS unsigned gu32;
typedef unsigned long long u64;
typedef GAS u64 gu64;
typedef __amdgpu_buffer_rsrc_t rsrc_t;
#define RLX_AGENT __ATOMIC_RELAXED, __HIP_MEMORY_SCOPE_AGENT

#define TRICKLE 4
#define G2PK 0
#define TSH 20
#define GSPREAD 4

constexpr int S_ = 2048, D_ = 576, F_ = 4608;
constexpr int REV = 18 * 1024;
constexpr int XBC = 4 * REV;
constexpr int XBSZ = 32 * XBC;
constexpr int FL_OFF = 0;
constexpr int WQ_OFF = 131072;
constexpr int WO_OFF = WQ_OFF + 128 * REV;
constexpr int MS_OFF = WO_OFF + 36 * REV;
constexpr int MSJ = 72 * REV;
constexpr int CSP_OFF = MS_OFF + 8 * MSJ;
constexpr int X2_OFF = CSP_OFF + 8 * 18 * 2304 + 4096 - (8 * 18 * 2304) % 4096;
constexpr int OB_OFF = X2_OFF + XBSZ;
constexpr int X1_OFF = OB_OFF + XBSZ;
constexpr int PB_OFF = X1_OFF + XBSZ;
constexpr int PBSZ = 32 * 8 * 8 * 10240;
constexpr int ST_OFF = PB_OFF + PBSZ;
constexpr int ST2_OFF = ST_OFF + 256 * 64 * 8;
constexpr int WS_BYTES = ST2_OFF + 2 * 256 * 8 * 64 * 8;
constexpr int L_X = 0, L_H = 73728, L_ATT = 92160, ATT_STR = 10240, A_Q = 0, A_K = 2560, A_V = 5120, A_E = 7680;
constexpr int L_RES = 137216, L_OWN = 147456, L_CS1 = L_OWN + 8192, L_B1 = L_CS1 + 2304, L_STR = L_B1 + 2304,
              L_STM = L_STR + 320, L_B2 = L_STM + 320, L_W = L_B2 + 320, L_END = L_W + 64;
static_assert(L_END <= 163840, "lds");
constexpr int LDS_BYTES = 163840;

__device__ __forceinline__ float bf2f(u16 b) { return __uint_as_float(((unsigned)b) << 16); }
__device__ __forceinline__ float bflo(unsigned u) { return __uint_as_float(u << 16); }
__device__ __forceinline__ float bfhi(unsigned u) { return __uint_as_float(u & 0xffff0000u); }
__device__ __forceinline__ unsigned pk2(float a, float b) {
  f32x2 v = {a, b};
  return __builtin_bit_cast(unsigned, __builtin_convertvector(v, bf16x2));
}
__device__ __forceinline__ v2u pk4(f32x4 v) { return v2u{pk2(v[0], v[1]), pk2(v[2], v[3])}; }
__device__ __forceinline__ f32x4 unpk4(unsigned a, unsigned b) { return f32x4{bflo(a), bfhi(a), bflo(b), bfhi(b)}; }
__device__ __forceinline__ f32x4 mfma(v4u a, v4u b, f32x4 c) {
  return __builtin_amdgcn_mfma_f32_16x16x32_bf16(__builtin_bit_cast(bf16x8, a), __builtin_bit_cast(bf16x8, b), c, 0, 0, 0);
}
__device__ __forceinline__ v4u ldw(rsrc_t r, int voff, int soff) { return __builtin_amdgcn_raw_buffer_load_b128(r, voff, soff, 0); }
__device__ __forceinline__ v4u ldc(rsrc_t r, int voff, int soff) { return __builtin_amdgcn_raw_buffer_load_b128(r, voff, soff, 16); }
__device__ __forceinline__ void stc128(v4u v, rsrc_t r, int voff, int soff) {
  __builtin_amdgcn_raw_buffer_store_b128(v, r, voff, soff, 16);
  asm volatile("s_nop 1" :: "v"(v));
}
__device__ __forceinline__ void stc64(v2u v, rsrc_t r, int voff, int soff) { __builtin_amdgcn_raw_buffer_store_b64(v, r, voff, soff, 16); }
__device__ __forceinline__ void stc32(unsigned v, rsrc_t r, int voff, int soff) { __builtin_amdgcn_raw_buffer_store_b32(v, r, voff, soff, 16); }
#define SB() __builtin_amdgcn_sched_barrier(0)
__device__ __forceinline__ void bar() {
  asm volatile("s_waitcnt lgkmcnt(0)" ::: "memory");
  __builtin_amdgcn_s_barrier();
  asm volatile("" ::: "memory");
}
__device__ __forceinline__ float red16max(float x) {
  auto r = __builtin_amdgcn_permlane16_swap(__float_as_uint(x), __float_as_uint(x), false, false);
  x = fmaxf(__uint_as_float(r[0]), __uint_as_float(r[1]));
  auto q = __builtin_amdgcn_permlane32_swap(__float_as_uint(x), __float_as_uint(x), false, false);
  return fmaxf(__uint_as_float(q[0]), __uint_as_float(q[1]));
}
__device__ __forceinline__ float red16sum(float x) {
  auto r = __builtin_amdgcn_permlane16_swap(__float_as_uint(x), __float_as_uint(x), false, false);
  x = __uint_as_float(r[0]) + __uint_as_float(r[1]);
  auto q = __builtin_amdgcn_permlane32_swap(__float_as_uint(x), __float_as_uint(x), false, false);
  return __uint_as_float(q[0]) + __uint_as_float(q[1]);
}
__device__ __forceinline__ int ms_wave(int w) { return 162 * 1024 * w; }
__device__ __forceinline__ int kperm(int s, int g, int e) { return 32 * s + 4 * g + (e & 3) + ((e >> 2) << 4); }

#define STAMP(i) do {} while (0)
#define CST(i) do {} while (0)

__device__ __forceinline__ gu64* flagp(char* ws, int line, int i) { return (gu64*)(ws + FL_OFF + 256 + line * 256 + i * 8); }
__device__ __forceinline__ void publish(char* ws, int line, int i, u64 tag, int tid) {
  asm volatile("s_waitcnt vmcnt(0)" ::: "memory");
  bar();
  if (tid == 0) __hip_atomic_store(flagp(ws, line, i), tag, RLX_AGENT);
}
template <int SL> __device__ __forceinline__ void poll(gu64* p, u64 tag) {
  for (unsigned spins = 0; spins < (1u << 22); ++spins) {
    const u64 v = __hip_atomic_load(p, RLX_AGENT);
    if (__all(v == tag)) break;
    if (SL) __builtin_amdgcn_s_sleep(SL);
  }
  __builtin_amdgcn_fence(__ATOMIC_ACQUIRE, "wavefront");
}
template <int SL> __device__ __forceinline__ void poll8(char* ws, int ph, int c, int c2, u64 tag, int lane) {
  poll<SL>(flagp(ws, ph * 32 + ((lane & 8) ? c2 : c), lane & 7), tag);
}
__device__ __forceinline__ void await(char* ws, int ph, int c, int c2, u64 tag, int w, int lane) {
  if (w == 0) poll8<0>(ws, ph, c, c2, tag, lane);
  bar();
}

__device__ __forceinline__ void stat2(v4u x, float& s1, float& s2) {
  const bf16x2 ones = __builtin_bit_cast(bf16x2, 0x3f803f80u);
#pragma unroll
  for (int i = 0; i < 4; ++i) {
    const unsigned u = x[i];
    const bf16x2 v = __builtin_bit_cast(bf16x2, u);
    s1 = __builtin_amdgcn_fdot2_f32_bf16(v, ones, s1, false);
    s2 = __builtin_amdgcn_fdot2_f32_bf16(v, v, s2, false);
  }
}
__device__ __forceinline__ void stat_put(LAS char* L, int idx, float s1, float s2) {
  const float mean = s1 * (1.0f / D_);
  const float var = fmaxf(s2 * (1.0f / D_) - mean * mean, 0.f);
  const float rstd = __builtin_amdgcn_rsqf(var + 1e-5f);
  *(LAS float*)(L + L_STR + idx * 4) = rstd;
  *(LAS float*)(L + L_STM + idx * 4) = mean * rstd;
}
__device__ __forceinline__ void ones_row(LAS char* L, int lane) {
  if ((lane & 15) == 0) {
#pragma unroll
    for (int s = 0; s < 18; ++s) *(LAS v4u*)(L + L_X + s * 1024 + lane * 16) = v4u{0x3f803f80u, 0x3f803f80u, 0x3f803f80u, 0x3f803f80u};
  }
}
template <int CTRL> __device__ __forceinline__ float dppadd(float x) {
  return x + __uint_as_float(__builtin_amdgcn_update_dpp(0u, __float_as_uint(x), CTRL, 0xf, 0xf, true));
}
__device__ __forceinline__ float red8sum(float x) { return dppadd<0x141>(dppadd<0x4E>(dppadd<0xB1>(x))); }
__device__ __forceinline__ void stage_tile(rsrc_t rs, int soff, LAS char* L, int t5, bool halo, int lane) {
  v4u x[18];
#pragma unroll
  for (int s = 0; s < 18; ++s) x[s] = ldc(rs, lane * 16 + (s & 3) * 1024, soff + (s >> 2) * 4096);
  SB();
  float s1 = 0.f, s2 = 0.f;
#pragma unroll
  for (int s = 0; s < 18; ++s) {
    *(LAS v4u*)(L + L_X + t5 * REV + s * 1024 + lane * 16) = x[s];
    stat2(x[s], s1, s2);
    SB();
  }
  if (halo) ones_row(L, lane);
  s1 = red16sum(s1); s2 = red16sum(s2);
  if (lane < 16) stat_put(L, t5 * 16 + lane, s1, s2);
}

#define RING 28
constexpr int R_ = RING;
constexpr int RB = 256 - 4 * R_;
template <int SLOT, int OFF> __device__ __forceinline__ void rload(v4u rs, int voff, int so) {
  asm volatile("buffer_load_dwordx4 v[%c3:%c4], %0, %1, %2 offen offset:%c5 sc1"
               :: "v"(voff), "s"(rs), "s"(so), "i"(RB + 4 * SLOT), "i"(RB + 4 * SLOT + 3), "i"(OFF));
}
template <int N> __device__ __forceinline__ void rwait() { asm volatile("s_waitcnt vmcnt(%c0)" :: "i"(N)); }
template <int SLOT> __device__ __forceinline__ void rmfma(f32x4& acc, v4u x) {
  asm volatile("v_mfma_f32_16x16x32_bf16 %0, v[%c2:%c3], %1, %0" : "+v"(acc) : "v"(x), "i"(RB + 4 * SLOT), "i"(RB + 4 * SLOT + 3));
}
template <int SLOT> __device__ __forceinline__ void rmfma0(f32x4& acc, v4u x) {
  asm volatile("v_mfma_f32_16x16x32_bf16 %0, v[%c2:%c3], %1, 0" : "=&v"(acc) : "v"(x), "i"(RB + 4 * SLOT), "i"(RB + 4 * SLOT + 3));
}
template <int I, int N, class Fn> __device__ __forceinline__ void sfor(Fn&& f) {
  if constexpr (I < N) { f(std::integral_constant<int, I>{}); sfor<I + 1, N>(f); }
}
constexpr int cmin(int a, int b) { return a < b ? a : b; }
template <int P, int TOTAL> __device__ __forceinline__ void refill(v4u r, int mb, int voff) {
  constexpr int Q = P + R_;
  if constexpr (Q < TOTAL) rload<P % R_, (Q & 3) * 1024>(r, voff, mb + (Q >> 2) * 4096);
}
struct NoHook { template <int S> static constexpr int extra() { return 0; } template <int S> __device__ __forceinline__ void at() {} };
template <int NT, int NK, int S0, int S1, int P0, int TOTAL, bool INIT, int I0 = R_, int NKL = NK, class Hook = NoHook>
__device__ __forceinline__ void segx(f32x4* const (&acc)[NT], const LAS char* const (&xb)[NK], v4u r, int mb, int voff, Hook& hook) {
  constexpr int XD = NT >= 4 ? 1 : (NT == 1 ? 4 : 2);
  v4u xf[XD][NK];
#pragma unroll
  for (int d = 0; d < XD; ++d)
#pragma unroll
    for (int tt = 0; tt < NK; ++tt)
      if (S0 + d < S1) xf[d][tt] = *(const LAS v4u*)(xb[tt] + (S0 + d) * 1024);
  SB();
  asm volatile("s_nop 1");
  sfor<S0, S1>([&](auto sc) {
    constexpr int s = sc.value, p = P0 + (s - S0) * NT, d = (s - S0) % XD;
    constexpr int RS = (R_ - I0 + 3) / 4;
    constexpr int lag = RS > 1 ? RS : 1;
    constexpr int k = s - S0;
    constexpr int issued = k < RS ? I0 + 4 * k : cmin(TOTAL, R_ + (k < lag ? P0 : p - lag * NT));
    rwait<issued - (p + NT) + Hook::template extra<s>()>();
    if constexpr (k < RS) sfor<I0 + 4 * k, cmin(R_, I0 + 4 * k + 4)>([&](auto qc) { rload<qc.value, (qc.value & 3) * 1024>(r, voff, mb + (qc.value >> 2) * 4096); });
    hook.template at<s>();
    sfor<0, NK>([&](auto kc) {
      constexpr int tt = kc.value;
      sfor<0, NT>([&](auto tc) {
        constexpr int t = tc.value;
        if constexpr (t == NT - 1 && tt >= NKL) {}
        else if constexpr (INIT && s == S0) rmfma0<(p + t) % R_>(acc[t][tt], xf[d][tt]);
        else rmfma<(p + t) % R_>(acc[t][tt], xf[d][tt]);
        constexpr int idx = tt * NT + t;
        if constexpr (k >= lag && idx % NK == 0 && idx / NK < NT) refill<p - lag * NT + idx / NK, TOTAL>(r, mb, voff);
      });
      SB();
      if constexpr (s + XD < S1) { xf[d][tt] = *(const LAS v4u*)(xb[tt] + (s + XD) * 1024); SB(); }
    });
  });
  static_assert(I0 == R_ || P0 + (S1 - S0 - (R_ - I0 + 3) / 4) * NT + R_ >= TOTAL, "ramp: all refills are out inside the loop");
  sfor<0, NT>([&](auto tc) { refill<P0 + (S1 - 1 - S0) * NT + tc.value, TOTAL>(r, mb, voff); });
  asm volatile("s_nop 7\n\ts_nop 1");
  SB();
}
template <int NT, int NK, int S0, int S1, int P0, int TOTAL, bool INIT, class Hook = NoHook>
__device__ __forceinline__ void seg(f32x4* const (&acc)[NT], const LAS char* xb, v4u r, int mb, int voff, Hook hook = Hook{}) {
  const LAS char* x2 = xb + 2 * REV;
  asm volatile("" : "+v"(x2));
  segx<NT, 4, S0, S1, P0, TOTAL, INIT, R_, 4, Hook>(acc, {xb, xb + REV, x2, x2 + REV}, r, mb, voff, hook);
}
template <int LO> __device__ __forceinline__ void fillh(v4u r, int n, int voff) {
  sfor<LO ? R_ / 2 : 0, LO ? R_ : R_ / 2>([&](auto sc) { rload<sc.value, (sc.value & 3) * 1024>(r, voff, n + (sc.value >> 2) * 4096); });
  SB();
}
__device__ __forceinline__ v2u trread(const LAS char* p) {
  typedef short s4 __attribute__((ext_vector_type(4)));
  return __builtin_bit_cast(v2u, __builtin_amdgcn_ds_read_tr16_b64_v4i16((LAS s4*)p));
}
constexpr float AL = 0.46866869f, KK = 2.3022082f / AL;
__device__ __forceinline__ float sfma(float a, float b, float c) { float d; asm("v_fma_f32 %0, %1, %2, %3" : "=v"(d) : "v"(a), "v"(b), "v"(c)); return d; }
__device__ __forceinline__ float sfnma(float a, float b, float c) { float d; asm("v_fma_f32 %0, -%1, %2, %3" : "=v"(d) : "v"(a), "v"(b), "v"(c)); return d; }
__device__ __forceinline__ float sfmas(float a, float s) { float d; asm("v_fma_f32 %0, %1, %2, %2" : "=v"(d) : "v"(a), "s"(s)); return d; }
__device__ __forceinline__ float ssqs(float a, float s) { float d; asm("v_fma_f32 %0, %1, %1, %2" : "=v"(d) : "v"(a), "s"(s)); return d; }
__device__ __forceinline__ float smul(float a, float b) { float d; asm("v_mul_f32 %0, %1, %2" : "=v"(d) : "v"(a), "v"(b)); return d; }
#define EACH16 _Pragma("unroll") for (int tt = 0; tt < 4; ++tt) _Pragma("unroll") for (int r = 0; r < 4; ++r)
__device__ __forceinline__ void gelu16(f32x4 (&a)[4], const float (&rstd)[4], const float (&mr)[4], f32x4 cs, f32x4 bb, v2u (&o)[4]) {
  float e[4][4], u[4][4];
  EACH16 e[tt][r] = sfnma(cs[r], mr[tt], bb[r]);
  SB();
  EACH16 u[tt][r] = sfma(a[tt][r], rstd[tt], e[tt][r]);
  SB();
  EACH16 e[tt][r] = ssqs(u[tt][r], KK);
  SB();
  EACH16 e[tt][r] = smul(e[tt][r], u[tt][r]);
  SB();
  EACH16 e[tt][r] = __builtin_amdgcn_exp2f(-e[tt][r]);
  SB();
  EACH16 e[tt][r] = sfmas(e[tt][r], AL);
  SB();
  EACH16 e[tt][r] = __builtin_amdgcn_rcpf(e[tt][r]);
  SB();
  EACH16 u[tt][r] = smul(u[tt][r], e[tt][r]);
  SB();
#pragma unroll
  for (int tt = 0; tt < 4; ++tt) o[tt] = v2u{pk2(u[tt][0], u[tt][1]), pk2(u[tt][2], u[tt][3])};
  SB();
}
__device__ __forceinline__ void gelu16p(f32x4 (&a)[4], const float (&rstd)[4], const float (&mr)[4], f32x4 cs, f32x4 bb, v2u (&o)[4]) {
  f32x4 e[4];
#pragma unroll
  for (int tt = 0; tt < 4; ++tt) e[tt] = bb - cs * mr[tt];
  SB();
#pragma unroll
  for (int tt = 0; tt < 4; ++tt) a[tt] = a[tt] * rstd[tt] + e[tt];
  SB();
#pragma unroll
  for (int tt = 0; tt < 4; ++tt) e[tt] = a[tt] * a[tt] + KK;
  SB();
#pragma unroll
  for (int tt = 0; tt < 4; ++tt) e[tt] = e[tt] * a[tt];
  SB();
  EACH16 e[tt][r] = __builtin_amdgcn_exp2f(-e[tt][r]);
  SB();
#pragma unroll
  for (int tt = 0; tt < 4; ++tt) e[tt] = e[tt] * AL + AL;
  SB();
  EACH16 e[tt][r] = __builtin_amdgcn_rcpf(e[tt][r]);
  SB();
#pragma unroll
  for (int tt = 0; tt < 4; ++tt) a[tt] = a[tt] * e[tt];
  SB();
#pragma unroll
  for (int tt = 0; tt < 4; ++tt) o[tt] = pk4(a[tt]);
  SB();
}
struct StoreHook {
  v4u d[4]; rsrc_t rs; int voff, po0, po1;
  template <int S> static constexpr int extra() { return S <= 6 ? 0 : S <= 9 ? 1 : S <= 12 ? 2 : S <= 15 ? 3 : 4; }
  template <int S> __device__ __forceinline__ void at() {
    if constexpr (S == 6 || S == 9 || S == 12 || S == 15) {
      constexpr int q = (S - 6) / 3;
      SB(); stc128(d[q], rs, voff + (q & 1) * 1024, q < 2 ? po0 : po1); SB();
    }
  }
};
template <int SL> __device__ __forceinline__ void lds_wait_ge(LAS char* L, int word, unsigned want) {
  volatile LAS unsigned* p = (volatile LAS unsigned*)(L + L_W + word * 4);
  for (unsigned spins = 0; spins < (1u << 22); ++spins) {
    if (__builtin_amdgcn_readfirstlane(*p) >= want) break;
    __builtin_amdgcn_s_sleep(SL);
  }
  asm volatile("" ::: "memory");
}
template <int N> __device__ __forceinline__ void arrive(char* ws, LAS char* L, int word, int line, int i, u64 tag, int lane) {
  asm volatile("s_waitcnt vmcnt(0)" ::: "memory");
  if (lane == 0) {
    const unsigned old = __hip_atomic_fetch_add((LAS unsigned*)(L + L_W + word * 4), 1u, __ATOMIC_RELAXED, __HIP_MEMORY_SCOPE_WORKGROUP);
    if (old % N == N - 1) __hip_atomic_store(flagp(ws, line, i), tag, RLX_AGENT);
  }
}
__device__ __forceinline__ float sadd(float a, float b) { float d; asm("v_add_f32 %0, %1, %2" : "=v"(d) : "v"(a), "v"(b)); return d; }
__device__ __forceinline__ void acc8(f32x4& a, f32x4& b, v4u p) {
  a[0] = sadd(a[0], bflo(p[0])); a[1] = sadd(a[1], bfhi(p[0])); a[2] = sadd(a[2], bflo(p[1])); a[3] = sadd(a[3], bfhi(p[1]));
  b[0] = sadd(b[0], bflo(p[2])); b[1] = sadd(b[1], bfhi(p[2])); b[2] = sadd(b[2], bflo(p[3])); b[3] = sadd(b[3], bfhi(p[3]));
}

#define FRESH() int lane = lane0, c = c0, j = j0, w = w0; asm volatile("" : "+v"(lane)); asm volatile("" : "+s"(c), "+s"(j), "+s"(w)); \
  const int g = lane >> 4, m = lane & 15, voff = lane * 16, role = w >> 2, hl = w & 3; (void)g; (void)m; (void)voff; (void)role; (void)hl;

__device__ __forceinline__ void jload(int job, v4u (&r)[4], const u16* w1, const u16* w2, const u16* wo, int j, int lane) {
      const int kind = job < 162 ? 0 : (job < 324 ? 1 : 2);
      const int q = job - 162 * kind, s9 = q / 9, s = kind < 2 ? s9 : (q < 18 ? q : q - 18), B = kind < 2 ? q - 9 * s9 : (q < 18 ? j : 8);
      const u16* src = kind == 0 ? w1 + (32 * s * F_ + 576 * j) : (kind == 1 ? w2 + (576 * j + 32 * s) * D_ : wo);
      const int ld = kind == 0 ? F_ : D_;
#pragma unroll
      for (int i = 0; i < 4; ++i) {
        const int rho = 8 * i + (lane >> 3);
        const int orow = s < 16 ? 36 * s + rho + (i >= 2 ? 4 : 0) : 36 * (8 * (s - 16) + 2 * ((rho >> 2) & 3) + (i >> 1)) + 16 + (rho & 3);
        r[i] = *(const v4u*)(src + (kind < 2 ? rho : orow) * ld + 64 * B + (lane & 7) * 8);
      }
}
__device__ __forceinline__ void jput(int job, v4u (&r)[4], LAS char* scr, rsrc_t rs, int j, int lane) {
      const int g = lane >> 4, m = lane & 15, voff = lane * 16;
      const int kind = job < 162 ? 0 : (job < 324 ? 1 : 2);
      const bool one = kind == 0;
      const int q = job - 162 * kind, s9 = q / 9, s = kind < 2 ? s9 : (q < 18 ? q : q - 18), B = kind < 2 ? q - 9 * s9 : (q < 18 ? j : 8);
      const int d = B < 8 ? B : j;
      const int e0 = 162 * (d >> 1) + (s < 8 ? 72 + 4 * s + 2 * (d & 1) : 88 + 20 * (d & 1) + 2 * s), e1 = 162 * (4 + (d >> 1)) + 90 + 36 * (d & 1) + 2 * s;
      const bool spread = one ? B == 8 : B == j;
      int base = one ? (B < 4 ? 162 * B + 4 * s : (B < 8 ? 162 * (4 + 2 * (B & 1)) + 5 * s + (B >= 6 ? 2 : 0) : 652 + 5 * s)) : (spread ? 144 + s : e0);
      int c1 = spread ? 162 : 1, c2 = spread ? 324 : (one ? (B < 4 ? 2 : 162) : e1 - e0);
      base = kind < 2 ? (MS_OFF + j * MSJ) / 1024 + base : WO_OFF / 1024 + 72 * B + s;
      c1 = kind < 2 ? c1 : 18; c2 = kind < 2 ? c2 : 36;
      v2u lo[4], hi[4];
      const LAS char* p = scr + (4 * g + (m >> 2)) * 128 + (m & 3) * 8;
      *(LAS v4u*)(scr + voff) = r[0]; *(LAS v4u*)(scr + 1024 + voff) = r[1];
#pragma unroll
      for (int t = 0; t < 4; ++t) lo[t] = trread(p + t * 32);
      SB();
      *(LAS v4u*)(scr + voff) = r[2]; *(LAS v4u*)(scr + 1024 + voff) = r[3];
#pragma unroll
      for (int t = 0; t < 4; ++t) hi[t] = trread(p + t * 32);
      float cs4[4];
#pragma unroll
      for (int t = 0; t < 4; ++t) {
        const v4u f = {lo[t][0], lo[t][1], hi[t][0], hi[t][1]};
        stc128(f, rs, voff, (base + (t & 1) * c1 + (t >> 1) * c2) * 1024);
        float a = 0.f, dummy = 0.f;
        stat2(f, a, dummy);
        cs4[t] = red16sum(a);
      }
      const float csum = g < 2 ? (g == 0 ? cs4[0] : cs4[1]) : (g == 2 ? cs4[2] : cs4[3]);
      if (one) stc32(__float_as_uint(csum), rs, lane * 4, CSP_OFF + (j * 18 + s) * 2304 + B * 256);
}

struct JobHook {
  v4u jr[3][4]; const u16 *w1, *w2; int job0, j, lane;
  template <int S> static constexpr int extra() { return S <= 9 ? 0 : S <= 10 ? 4 : S <= 11 ? 8 : 12; }
  template <int S> __device__ __forceinline__ void at() {
    if constexpr (S >= 9 && S <= 11) { SB(); jload(min(job0 + 128 * (S - 9), 323), jr[S - 9], w1, w2, w1, j, lane); SB(); }
  }
};

__global__ __launch_bounds__(512, 2) __attribute__((amdgpu_num_vgpr(RB / 2)))
void k_main(char* __restrict__ ws, const u16* __restrict__ xin, const u16* __restrict__ wqkv, const u16* __restrict__ wo,
            const u16* __restrict__ w1, const u16* __restrict__ b1, const u16* __restrict__ w2, const u16* __restrict__ b2,
            u16* __restrict__ out) {
  extern __shared__ __attribute__((aligned(16))) char smem[];
  LAS char* L = (LAS char*)smem;
  const int tid = threadIdx.x, lane0 = tid & 63, w0 = __builtin_amdgcn_readfirstlane(tid >> 6);
  int c0, j0;
  {
    constexpr int G = GSPREAD, per = 8 / G;
    const int b = blockIdx.x, x = b & 7, r = b >> 3;
    j0 = (x % G) * per + (r % per);
    c0 = (x / G) * (4 * G) + r / per;
  }
  const rsrc_t rs = __builtin_amdgcn_make_buffer_rsrc((void*)ws, 0, WS_BYTES, 0x00020000);
  const v4u rw = {(unsigned)(size_t)ws, (unsigned)((size_t)ws >> 32) & 0xffffu, (unsigned)WS_BYTES, 0x00020000u};
  asm volatile("" ::: "v255");
  STAMP(0);
  CST(24);
  u64 tag = 0;
  auto qkv = [&](auto lc) {
    constexpr int Ly = lc.value;
    STAMP(2 + 16 * Ly);
    FRESH();
    if (role == 0) {
#pragma unroll
      for (int i = 0; i < 5; ++i) {
        const int T = i < 4 ? 4 * j + i : 32 + (j & 3);
        v2u v = *(LAS v2u*)(L + L_X + (hl + 1) * REV + (T >> 1) * 1024 + voff + 8 * (T & 1));
        *(LAS v2u*)(L + L_RES + (i * 4 + hl) * 512 + lane * 8) = v;
      }
    }
    const int qb = WQ_OFF + ((4 * j + hl) * 4 + 2 * role) * REV;
    constexpr int I0 = Ly == 0 ? 8 : R_;
    auto fix = [&](f32x4 a, const float (&cs)[4], float rstd, float mr) {
      float u[4];
#pragma unroll
      for (int r = 0; r < 4; ++r) u[r] = smul(a[r], rstd);
#pragma unroll
      for (int r = 0; r < 4; ++r) u[r] = sfnma(cs[r], mr, u[r]);
      return v2u{pk2(u[0], u[1]), pk2(u[2], u[3])};
    };
    auto bcast = [&](f32x4 a, float (&cs)[4]) {
#pragma unroll
      for (int r = 0; r < 4; ++r) cs[r] = __uint_as_float(__builtin_amdgcn_update_dpp(0u, __float_as_uint(a[r]), 0x150, 0xf, 0xf, false));
    };
    CST(0 + 32 * Ly);
    if (role == 0) {
      f32x4 a0[5], a1[5];
      const LAS char* xa = L + L_X + voff, *xc = xa + 3 * REV;
      asm volatile("" : "+v"(xc));
      NoHook hk;
      segx<2, 5, 0, 18, 0, 36, true, I0, 5>({a0, a1}, {xa, xa + REV, xa + 2 * REV, xc, xc + REV}, rw, qb, voff, hk);
      CST(1 + 32 * Ly);
      LAS char* A = L + L_ATT + hl * ATT_STR + lane * 8;
      float rstd[5], mr[5], cs0[4], cs1[4];
#pragma unroll
      for (int t = 0; t < 5; ++t) { rstd[t] = *(LAS float*)(L + L_STR + t * 64 + m * 4); mr[t] = *(LAS float*)(L + L_STM + t * 64 + m * 4); }
      bcast(a0[0], cs0); bcast(a1[0], cs1);
      SB();
#pragma unroll
      for (int t = 0; t < 5; ++t) {
        if (t) *(LAS v2u*)(A + A_Q + t * 512) = fix(a0[t], cs0, rstd[t], mr[t]);
        *(LAS v2u*)(A + A_K + t * 512) = fix(a1[t], cs1, rstd[t], mr[t]);
      }
    }
    int role2 = role;
    asm volatile("" : "+s"(role2));
    if (role2) {
      f32x4 a0[5], a1[5];
      const int ta = (hl & 1) ? 3 : 1, tb = 4 - ta;
      const LAS char* x0 = L + L_X + voff, *xa = x0 + ta * REV, *xc = x0 + tb * REV;
      std::conditional_t<Ly == 0, JobHook, NoHook> hk;
      if constexpr (Ly == 0) { hk.w1 = w1; hk.w2 = w2; hk.job0 = c * 4 + hl; hk.j = j; hk.lane = lane; }
      __builtin_amdgcn_s_setprio(1);
      segx<2, 5, 0, 18, 0, 36, true, I0, 3>({a0, a1}, {x0, xa, xa + REV, xc, xc + REV}, rw, qb, voff, hk);
      __builtin_amdgcn_s_setprio(0);
      CST(1 + 32 * Ly);
      LAS char* A = L + L_ATT + hl * ATT_STR + A_V + m * 32 + g * 8, *E = L + L_ATT + (hl & 2) * ATT_STR + A_E + m * 32 + g * 8;
      float rstd[5], mr[5], cs0[4], cs1[4];
      int to[5] = {0, ta, ta + 1, tb, tb + 1};
#pragma unroll
      for (int t = 0; t < 5; ++t) { rstd[t] = *(LAS float*)(L + L_STR + to[t] * 64 + m * 4); mr[t] = *(LAS float*)(L + L_STM + to[t] * 64 + m * 4); }
      bcast(a0[0], cs0); bcast(a1[0], cs1);
      SB();
#pragma unroll
      for (int t = 0; t < 5; ++t) {
        *(LAS v2u*)(A + to[t] * 512) = fix(a0[t], cs0, rstd[t], mr[t]);
        if (t < 3) *(LAS v2u*)(E + to[t] * 512) = fix(a1[t], cs1, rstd[t], mr[t]);
      }
      if (Ly) fillh<0>(rw, MS_OFF + j * MSJ + ms_wave(w), voff);
      if constexpr (Ly == 0) {
#pragma unroll
        for (int i = 0; i < 3; ++i) jput(min(c * 4 + hl + 128 * i, 323), hk.jr[i], L + L_OWN + hl * 2048, rs, j, lane);
      }
    }
  };
  {
    FRESH();
    u64 gen = 0;
    if (w >= 5) gen = __hip_atomic_load((gu64*)(ws + FL_OFF), RLX_AGENT);
    if (w == 4) {
      u64 pc;
      asm volatile("s_getpc_b64 %0" : "=s"(pc));
      const char* cp = (const char*)(pc & ~(u64)127) + (blockIdx.x >> 3) * 1536 + min(lane, 11) * 128;
      asm volatile("global_load_dword v255, %0, off" :: "v"(cp));
      asm volatile("buffer_load_dword v254, %0, %1, 0 offen sc1" :: "v"((lane << TSH) + 65536), "s"(rw));
    }
    if (w >= 5 && c < 18) {
      const int sec = w - 5;
      const u16* src = wqkv + (32 * c) * (3 * D_) + 72 * j;
      v4u qr[5];
      v2u xr[2];
#pragma unroll
      for (int i = 0; i < 5; ++i) {
        const int ch = min(64 * i + lane, 287), row = (ch * 7282) >> 16;
        qr[i] = *(const v4u*)(src + row * (3 * D_) + sec * D_ + (ch - 9 * row) * 8);
      }
#pragma unroll
      for (int i = 0; i < 2; ++i) xr[i] = *(const v2u*)(src + (16 * i + (lane >> 2)) * (3 * D_) + min(lane & 3, 2) * D_ + 36 * (sec & 1) + 16);
      SB();
      bar();
      CST(25);
      LAS char* scr = L + L_ATT + 40960 + sec * 4736;
#pragma unroll
      for (int i = 0; i < 5; ++i) *(LAS v4u*)(scr + min(64 * i + lane, 287) * 16) = qr[i];
      const int qd = WQ_OFF + 16 * j * REV + (sec >> 1) * 2 * REV + (2 * c + (sec & 1)) * 1024;
#pragma unroll
      for (int h = 0; h < 4; ++h) {
        const LAS char* p = scr + (4 * g + (m >> 2)) * 144 + (h >> 1) * 72 + (h & 1) * 40 + (m & 3) * 8;
        const v2u lo = trread(p), hi = trread(p + 16 * 144);
        stc128(v4u{lo[0], lo[1], hi[0], hi[1]}, rs, voff, qd + h * 4 * REV);
      }
      if (sec < 2) {
#pragma unroll
        for (int i = 0; i < 2; ++i) *(LAS v2u*)(scr + i * 512 + lane * 8) = (lane & 3) == 3 ? v2u{0u, 0u} : xr[i];
        const v2u lo = trread(scr + lane * 8), hi = trread(scr + 512 + lane * 8);
        const int md = WQ_OFF + (16 * j + 8 * sec + 2) * REV + (2 * c + 1) * 1024;
        stc128(v4u{lo[0], lo[1], hi[0], hi[1]}, rs, voff, md);
        stc128(v4u{lo[0], lo[1], hi[0], hi[1]}, rs, voff, md + 4 * REV);
      }
      CST(26);
      arrive<3>(ws, L, 9, 320 + j, c, gen + 1, lane);
      CST(27);
    }
    int wl = w;
    asm volatile("" : "+s"(wl));
    if (wl >= 5 && c >= 18) {
      const int job = min(324 + (c - 18) * 3 + w - 5, j < 4 ? 359 : 341);
      v4u jo[4];
      jload(job, jo, w1, w2, wo, j, lane);
      bar();
      jput(job, jo, L + L_ATT + 40960 + (w - 5) * 4096, rs, j, lane);
      arrive<3>(ws, L, 9, 320 + j, c, gen + 1, lane);
    }
    asm volatile("" : "+s"(wl));
    if (wl < 5) {
      const int cc = (w == 4 && c >= 2) ? c - 2 : c, tt = w == 4 ? 3 : w, t5 = w == 4 ? 0 : w + 1;
      const int r8 = lane >> 3, q8 = lane & 7;
      const u16* src = xin + (size_t)(2 * (64 * (cc >> 1) + 16 * tt + r8) + (cc & 1)) * D_ + 8 * q8;
      if (tid < 16) *(LAS unsigned*)(L + L_W + tid * 4) = 0u;
      bar();
      v4u x[2][9];
#pragma unroll
      for (int i = 0; i < 9; ++i) { x[0][i] = *(const v4u*)(src + 64 * i); x[1][i] = *(const v4u*)(src + 16 * D_ + 64 * i); }
      SB();
      CST(25);
      float s1[2] = {0.f, 0.f}, s2[2] = {0.f, 0.f};
      LAS char* dst = L + L_X + t5 * REV + (q8 >> 2) * 1024 + ((q8 & 1) * 32 + r8) * 16 + ((q8 >> 1) & 1) * 8;
#pragma unroll
      for (int i = 0; i < 9; ++i)
#pragma unroll
        for (int h = 0; h < 2; ++h) {
          *(LAS v2u*)(dst + i * 2048 + h * 128) = v2u{x[h][i][0], x[h][i][1]};
          *(LAS v2u*)(dst + i * 2048 + h * 128 + 256) = v2u{x[h][i][2], x[h][i][3]};
          stat2(x[h][i], s1[h], s2[h]);
          SB();
        }
      if (w == 4) ones_row(L, lane);
#pragma unroll
      for (int h = 0; h < 2; ++h) { s1[h] = red8sum(s1[h]); s2[h] = red8sum(s2[h]); }
      if (q8 < 2) stat_put(L, t5 * 16 + q8 * 8 + r8, q8 ? s1[1] : s1[0], q8 ? s2[1] : s2[0]);
      CST(26);
    }
    if (w == 6) {
      unsigned bv[5];
#pragma unroll
      for (int i = 0; i < 5; ++i) bv[i] = *(const unsigned*)(b1 + 576 * j + min(lane + 64 * i, 287) * 2);
      const unsigned b2v = *(const unsigned*)(b2 + (lane < 32 ? 64 * j + 2 * lane : 512 + 16 * (j & 3) + 2 * (lane & 7)));
#pragma unroll
      for (int i = 0; i < 5; ++i) *(LAS f32x2*)(L + L_B1 + min(lane + 64 * i, 287) * 8) = f32x2{AL * bflo(bv[i]), AL * bfhi(bv[i])};
      if (lane < 40) *(LAS f32x2*)(L + L_B2 + lane * 8) = f32x2{bflo(b2v), bfhi(b2v)};
    }
    STAMP(1);
    CST(29);
    if (w == 7) {
      poll<0>(flagp(ws, 320 + j, min(lane, 17)), gen + 1);
      if (lane == 0) *(LAS u64*)(L + L_W + 48) = gen + 1;
    }
    CST(30);
    bar();
    CST(31);
    sfor<0, 8>([&](auto qc) { rload<qc.value, (qc.value & 3) * 1024>(rw, voff, WQ_OFF + ((4 * j + hl) * 4 + 2 * role) * REV + (qc.value >> 2) * 4096); });
    tag = *(LAS u64*)(L + L_W + 48);
    tag = ((u64)(unsigned)__builtin_amdgcn_readfirstlane((unsigned)(tag >> 32)) << 32) | (unsigned)__builtin_amdgcn_readfirstlane((unsigned)tag);
    qkv(std::integral_constant<int, 0>{});
  }

#pragma unroll 1
  for (int Ly = 0; Ly < 2; ++Ly) {
    CST(2 + 32 * Ly);
    bar();
    CST(3 + 32 * Ly);
    STAMP(3 + 16 * Ly);
    {
      FRESH();
      const LAS char* A = L + L_ATT + hl * ATT_STR + role * 1024;
      const float sc = 0.23570226039551584f * 1.4426950408889634f;
#pragma unroll
      for (int q = 0; q < 2; ++q) {
        const int t = 2 * role + q;
        const LAS char* At = A + q * 512;
        const v2u qm = *(const LAS v2u*)(At + A_Q + 512 + lane * 8);
        const v2u kpm = *(const LAS v2u*)(At + A_K + lane * 8), kcm = *(const LAS v2u*)(At + A_K + 512 + lane * 8);
        const LAS char* Et = At + A_E - (hl & 1) * ATT_STR;
        const LAS char* Ae = Et + m * 32 + (hl & 1) * 4;
        const unsigned xq = *(const LAS unsigned*)(Ae + 512), xkp = *(const LAS unsigned*)(Ae + 8), xkc = *(const LAS unsigned*)(Ae + 512 + 8);
        const v2u vp = trread(At + A_V + lane * 8), vc = trread(At + A_V + 512 + lane * 8);
        const v2u ep = trread(Et + lane * 8), ec = trread(Et + 512 + lane * 8);
        const v4u qf = {qm[0], qm[1], g == 0 ? xq : 0u, 0u};
        const v4u kp = {kpm[0], kpm[1], g == 0 ? xkp : 0u, 0u}, kc = {kcm[0], kcm[1], g == 0 ? xkc : 0u, 0u};
        const f32x4 z = {0.f, 0.f, 0.f, 0.f};
        f32x4 sp = mfma(kp, qf, z), scur = mfma(kc, qf, z);
        const bool noprev = (c < 2) && (t == 0);
        float mx = -3.0e38f;
        bool okp[4], okc[4];
#pragma unroll
        for (int r = 0; r < 4; ++r) {
          const int kk = 4 * g + r;
          okp[r] = (kk >= m + 9) && !noprev;
          okc[r] = (kk <= m) && (kk >= m - 7);
          mx = fmaxf(mx, okp[r] ? sp[r] : -3.0e38f);
          mx = fmaxf(mx, okc[r] ? scur[r] : -3.0e38f);
        }
        mx = red16max(mx);
        float sum = 0.f;
#pragma unroll
        for (int r = 0; r < 4; ++r) {
          sp[r] = okp[r] ? __builtin_amdgcn_exp2f((sp[r] - mx) * sc) : 0.f;
          scur[r] = okc[r] ? __builtin_amdgcn_exp2f((scur[r] - mx) * sc) : 0.f;
          sum += sp[r] + scur[r];
        }
        sum = red16sum(sum);
        const float inv = __builtin_amdgcn_rcpf(sum);
        const v2u pp = pk4(sp * inv), pc = pk4(scur * inv);
        const v4u pf = {pp[0], pp[1], pc[0], pc[1]};
        const f32x4 o = mfma(v4u{vp[0], vp[1], vc[0], vc[1]}, pf, z);
        const f32x4 oe = mfma(v4u{ep[0], ep[1], ec[0], ec[1]}, pf, z);
        const int ob = OB_OFF + c * XBC + t * REV;
        stc64(pk4(o), rs, voff + 8 * (hl & 1), ob + (2 * j + (hl >> 1)) * 1024);
        if (g == 2) stc32((hl & 1) ? pk2(oe[2], oe[3]) : pk2(oe[0], oe[1]), rs, m * 16 + 4 * hl, ob + (16 + (j >> 2)) * 1024 + (j & 3) * 256);
      }
      CST(4 + 32 * Ly);
      STAMP(4 + 16 * Ly);
      publish(ws, (5 * Ly + 0) * 32 + c, j, tag, tid);
      if (Ly == 0) {
        if (tid == 0) __hip_atomic_store(flagp(ws, 328 + j, c), tag, RLX_AGENT);
        if (w == 1) { poll<1>(flagp(ws, 320 + j, lane & 31), tag); *(volatile LAS unsigned*)(L + L_W + 32) = 1u; }
        else if (w) lds_wait_ge<1>(L, 8, 1u);
      }
      if (w) {
        const int nblk = j < 4 ? 90 : 72;
        v4u wv[13];
#pragma unroll
        for (int i = 0; i < 13; ++i) {
          const int b = min(w - 1 + 7 * i, nblk - 1);
          wv[i] = ldc(rs, voff, b < 72 ? WO_OFF + 4 * j * REV + b * 1024 : WO_OFF + (32 + j) * REV + (b - 72) * 1024);
        }
        SB();
#pragma unroll
        for (int i = 0; i < 13; ++i) *(LAS v4u*)(L + L_X + min(w - 1 + 7 * i, nblk - 1) * 1024 + voff) = wv[i];
      }
      if (Ly && role == 0 && w) fillh<0>(rw, MS_OFF + j * MSJ + ms_wave(w), voff);
      await(ws, 5 * Ly + 0, c, c, tag, w, lane);
    }
    STAMP(5 + 16 * Ly);
    {
      FRESH();
      v4u of[9];
      const int ob = OB_OFF + c * XBC + hl * REV + role * 9 * 1024;
#pragma unroll
      for (int s = 0; s < 9; ++s) of[s] = ldc(rs, voff + (s & 3) * 1024, ob + (s >> 2) * 4096);
      SB();
      f32x4 acc[5];
      const LAS char* wb = L + L_X + role * 9 * 1024 + voff;
#pragma unroll
      for (int i = 0; i < 5; ++i) acc[i] = f32x4{0.f, 0.f, 0.f, 0.f};
#pragma unroll
      for (int s = 0; s < 9; ++s) {
#pragma unroll
        for (int i = 0; i < 4; ++i) acc[i] = mfma(*(const LAS v4u*)(wb + i * REV + s * 1024), of[s], acc[i]);
        if (s % 3 == 2) SB();
      }
      if (j < 4) {
#pragma unroll
        for (int s = 0; s < 9; ++s) acc[4] = mfma(*(const LAS v4u*)(wb + 4 * REV + s * 1024), of[s], acc[4]);
      }
      LAS char* R = L + L_ATT + hl * 5120 + voff;
      if (role == 1) {
#pragma unroll
        for (int i = 0; i < 5; ++i) *(LAS f32x4*)(R + i * 1024) = acc[i];
      }
      bar();
      if (role == 0) {
        v2u o[5];
#pragma unroll
        for (int i = 0; i < 5; ++i) {
          const v2u rv = *(LAS v2u*)(L + L_RES + (i * 4 + hl) * 512 + lane * 8);
          o[i] = pk4(acc[i] + *(LAS f32x4*)(R + i * 1024) + unpk4(rv[0], rv[1]));
        }
        const int xb = X1_OFF + c * XBC + hl * REV;
        stc128(v4u{o[0][0], o[0][1], o[1][0], o[1][1]}, rs, voff, xb + (2 * j) * 1024);
        stc128(v4u{o[2][0], o[2][1], o[3][0], o[3][1]}, rs, voff, xb + (2 * j + 1) * 1024);
        if (j < 4) stc64(o[4], rs, voff + 8 * (j & 1), xb + (16 + (j >> 1)) * 1024);
      }
      STAMP(6 + 16 * Ly);
      publish(ws, (5 * Ly + 1) * 32 + c, j, tag, tid);
      if (Ly == 0) {
        if (w == 1) { poll<1>(flagp(ws, 328 + j, lane & 31), tag); *(volatile LAS unsigned*)(L + L_W + 32) = 2u; }
        else lds_wait_ge<1>(L, 8, 2u);
        fillh<0>(rw, MS_OFF + j * MSJ + ms_wave(w), voff);
        if (w >= 5) {
          const int t = (w - 5) * 64 + lane;
#pragma unroll
          for (int i = 0; i < 3; ++i) {
            float v[18], a = 0.f;
#pragma unroll
            for (int s = 0; s < 18; ++s) v[s] = __uint_as_float(__builtin_amdgcn_raw_buffer_load_b32(rs, (t + 192 * i) * 4, CSP_OFF + (j * 18 + s) * 2304, 16));
#pragma unroll
            for (int s = 0; s < 18; ++s) a += v[s];
            *(LAS float*)(L + L_CS1 + (t + 192 * i) * 4) = a;
          }
        }
      }
      if (Ly && w == 0) fillh<0>(rw, MS_OFF + j * MSJ + ms_wave(w), voff);
      if (w >= 4) fillh<1>(rw, MS_OFF + j * MSJ + ms_wave(w), voff);
      await(ws, 5 * Ly + 1, c, c, tag, w, lane);
      if (Ly == 0 && blockIdx.x == 0 && tid == 0) __hip_atomic_store((gu64*)(ws + FL_OFF), tag, RLX_AGENT);
    }
    STAMP(7 + 16 * Ly);
    {
      FRESH();
      if (w < 4) { stage_tile(rs, X1_OFF + c * XBC + w * REV, L, w, false, lane); fillh<1>(rw, MS_OFF + j * MSJ + ms_wave(w), voff); }
    }
    bar();
    STAMP(8 + 16 * Ly);
    {
      FRESH();
      const int mb = MS_OFF + j * MSJ + ms_wave(w);
      const int pbc = PB_OFF + c * (64 * 10240);
      const int wq0 = WQ_OFF + ((4 * j + hl) * 4 + 2 * role) * REV;
      const int ui = 2 * role + (hl >> 1), up = hl & 1;
      const bool five = (j < 4) && ((j >> 1) == role) && (hl < 2);
      auto stats = [&](float (&rstd)[4], float (&mr)[4]) {
#pragma unroll
        for (int tt = 0; tt < 4; ++tt) { rstd[tt] = AL * *(LAS float*)(L + L_STR + tt * 64 + m * 4); mr[tt] = AL * *(LAS float*)(L + L_STM + tt * 64 + m * 4); }
      };
      auto gtile = [&](auto pkc, f32x4 (&x)[4], const float (&rstd)[4], const float (&mr)[4], int T, v2u (&o)[4]) {
        const f32x4 cs = *(LAS f32x4*)(L + L_CS1 + T * 64 + g * 16), bb = *(LAS f32x4*)(L + L_B1 + T * 64 + g * 16);
        if constexpr (pkc.value) gelu16p(x, rstd, mr, cs, bb, o); else gelu16(x, rstd, mr, cs, bb, o);
      };
      auto hpair = [&](auto pk0, auto pk1, f32x4 (&x)[4], f32x4 (&y)[4], const float (&rstd)[4], const float (&mr)[4], int T) {
        v2u oa[4], ob[4];
        gtile(pk0, x, rstd, mr, T, oa);
        gtile(pk1, y, rstd, mr, T + 1, ob);
#pragma unroll
        for (int tt = 0; tt < 4; ++tt) *(LAS v4u*)(L + L_H + tt * REV + (T >> 1) * 1024 + voff) = v4u{oa[tt][0], oa[tt][1], ob[tt][0], ob[tt][1]};
        SB();
      };
      auto poff = [&](int t) {
        const int d = 2 * hl + (t >> 1), i = 2 * role + (t & 1);
        return pbc + (((d != j ? d : i) * 8 + j) * 5 + (d != j ? i : 4)) * 2048;
      };
      auto ppack = [&](f32x4 (&x)[4], int p) { const v2u a = pk4(x[2 * p]), b = pk4(x[2 * p + 1]); return v4u{a[0], a[1], b[0], b[1]}; };
      auto pstore2 = [&](f32x4 (&x)[4], f32x4 (&y)[4], int t0, auto slc) {
#pragma unroll
        for (int q = 0; q < 4; ++q) {
          stc128(ppack(q < 2 ? x : y, q & 1), rs, voff + (q & 1) * 1024, poff(t0 + (q >> 1)));
          if (slc.value) { SB(); __builtin_amdgcn_s_sleep(slc.value); SB(); }
        }
        SB();
      };
      constexpr std::integral_constant<int, 0> I0{};
      constexpr std::integral_constant<int, 1> I1{};
      auto gather = [&](f32x4& sa, f32x4& sb, f32x4& ea, f32x4& eb) {
        v4u pv[7];
        const int po = pbc + (j * 8 * 5) * 2048 + up * 1024;
#pragma unroll
        for (int i = 0; i < 7; ++i) { const int src = i + (i >= j ? 1 : 0); pv[i] = ldc(rs, voff, po + (src * 5 + ui) * 2048); }
        SB();
        ea = *(LAS f32x4*)(L + L_B2 + 4 * 64 + g * 16); eb = ea;
        if (five) {
          v4u pe[8];
#pragma unroll
          for (int i = 0; i < 8; ++i) pe[i] = ldc(rs, voff, po + (i * 5 + 4) * 2048);
          SB();
#pragma unroll
          for (int i = 0; i < 8; ++i) acc8(ea, eb, pe[i]);
        }
        sa = *(LAS f32x4*)(L + L_B2 + ui * 64 + g * 16); sb = sa;
#pragma unroll
        for (int i = 0; i < 7; ++i) acc8(sa, sb, pv[i]);
      };
      auto finish = [&](f32x4 a, f32x4 b, int T) {
#pragma unroll
        for (int p = 0; p < 2; ++p) {
          const int tt = 2 * up + p;
          const int fo = tt * REV + (T >> 1) * 1024 + 8 * (T & 1);
          const v2u rv = *(LAS v2u*)(L + L_X + fo + voff);
          const v2u o = pk4((p ? b : a) + unpk4(rv[0], rv[1]));
          if (Ly == 0) stc64(o, rs, voff, X2_OFF + c * XBC + fo);
          else {
            const int tok = 2 * (64 * (c >> 1) + 16 * tt + m) + (c & 1);
            *(v2u*)(out + (size_t)tok * D_ + 16 * T + 4 * g) = o;
          }
        }
      };
      auto tail = [&](f32x4 sa, f32x4 sb, f32x4 ea, f32x4 eb) {
        if (five) finish(ea, eb, 32 + j);
        CST(19 + 32 * Ly);
        bar();
        const v2u oa = *(LAS v2u*)(L + L_OWN + (ui * 4 + 2 * up) * 512 + lane * 8), ob = *(LAS v2u*)(L + L_OWN + (ui * 4 + 2 * up + 1) * 512 + lane * 8);
        finish(sa + unpk4(oa[0], oa[1]), sb + unpk4(ob[0], ob[1]), 4 * j + ui);
      };
      CST(8 + 32 * Ly);
      if (role == 0) {
        __builtin_amdgcn_s_setprio(1);
        {
          f32x4 acc[4][4];
          seg<4, 4, 0, 18, 0, 162, true>({acc[0], acc[1], acc[2], acc[3]}, L + L_X + voff, rw, mb, voff);
          CST(9 + 32 * Ly);
          float rstd[4], mr[4];
          stats(rstd, mr);
          hpair(I0, I0, acc[0], acc[1], rstd, mr, 4 * hl);
          hpair(I0, I0, acc[2], acc[3], rstd, mr, 4 * hl + 2);
        }
        CST(10 + 32 * Ly);
        bar();
        CST(11 + 32 * Ly);
        {
          f32x4 acc[4][4];
          seg<4, 4, 0, 8, 72, 162, true>({acc[0], acc[1], acc[2], acc[3]}, L + L_H + voff, rw, mb, voff);
          CST(12 + 32 * Ly);
          bar();
          CST(13 + 32 * Ly);
          seg<2, 4, 8, 18, 104, 162, false>({acc[0], acc[1]}, L + L_H + voff, rw, mb, voff);
          pstore2(acc[0], acc[1], 0, std::integral_constant<int, TRICKLE>{});
          CST(20 + 32 * Ly);
          seg<2, 4, 8, 18, 124, 162, false>({acc[2], acc[3]}, L + L_H + voff, rw, mb, voff);
          __builtin_amdgcn_s_setprio(0);
          pstore2(acc[2], acc[3], 2, std::integral_constant<int, TRICKLE>{});
          CST(14 + 32 * Ly);
        }
        arrive<4>(ws, L, 0, (5 * Ly + 2) * 32 + c, j, tag, lane);
        CST(15 + 32 * Ly);
        if (hl == 0) { poll8<4>(ws, 5 * Ly + 2, c, c, tag, lane); *(volatile LAS unsigned*)(L + L_W + 8) = Ly + 1; }
        else lds_wait_ge<6>(L, 2, Ly + 1);
        CST(16 + 32 * Ly);
        f32x4 sa, sb, ea, eb;
        gather(sa, sb, ea, eb);
        CST(17 + 32 * Ly);
        lds_wait_ge<3>(L, 4 + hl, Ly + 1);
        CST(18 + 32 * Ly);
        f32x4 own[4];
        seg<1, 4, 0, 18, 144, 162, true>({own}, L + L_H + voff, rw, mb, voff);
#pragma unroll
        for (int tt = 0; tt < 4; ++tt) *(LAS v2u*)(L + L_OWN + (hl * 4 + tt) * 512 + lane * 8) = pk4(own[tt]);
        if (Ly == 0) fillh<0>(rw, wq0, voff);
        tail(sa, sb, ea, eb);
      }
      int role2 = role;
      asm volatile("" : "+s"(role2));
      if (role2 != 0) {
        {
          f32x4 acc[5][4];
          seg<5, 4, 0, 18, 0, 162, true>({acc[0], acc[1], acc[2], acc[3], acc[4]}, L + L_X + voff, rw, mb, voff);
          CST(9 + 32 * Ly);
          bar();
          CST(11 + 32 * Ly);
          float rstd[4], mr[4];
          stats(rstd, mr);
          hpair(I0, std::integral_constant<int, G2PK>{}, acc[0], acc[1], rstd, mr, 16 + 2 * hl);
          CST(10 + 32 * Ly);
          hpair(I1, I1, acc[2], acc[3], rstd, mr, 24 + 2 * hl);
          v2u o[4];
          gtile(I1, acc[4], rstd, mr, 32 + hl, o);
#pragma unroll
          for (int tt = 0; tt < 4; ++tt) *(LAS v2u*)(L + L_H + tt * REV + (16 + (hl >> 1)) * 1024 + voff + 8 * (hl & 1)) = o[tt];
          CST(12 + 32 * Ly);
        }
        bar();
        CST(13 + 32 * Ly);
        {
          f32x4 acc[2][4];
          seg<2, 4, 0, 18, 90, 162, true>({acc[0], acc[1]}, L + L_H + voff, rw, mb, voff);
          CST(20 + 32 * Ly);
          __builtin_amdgcn_s_setprio(1);
          StoreHook hk{{ppack(acc[0], 0), ppack(acc[0], 1), ppack(acc[1], 0), ppack(acc[1], 1)}, rs, voff, poff(0), poff(1)};
          seg<2, 4, 0, 18, 126, 162, true>({acc[0], acc[1]}, L + L_H + voff, rw, mb, voff, hk);
          *(volatile LAS unsigned*)(L + L_W + 16 + hl * 4) = Ly + 1;
          CST(23 + 32 * Ly);
          pstore2(acc[0], acc[1], 2, I0);
          CST(14 + 32 * Ly);
        }
        arrive<4>(ws, L, 1, (5 * Ly + 3) * 32 + c, j, tag, lane);
        CST(15 + 32 * Ly);
        if (hl == 0) { poll8<0>(ws, 5 * Ly + 3, c, c, tag, lane); *(volatile LAS unsigned*)(L + L_W + 12) = Ly + 1; }
        else lds_wait_ge<1>(L, 3, Ly + 1);
        CST(16 + 32 * Ly);
        f32x4 sa, sb, ea, eb;
        gather(sa, sb, ea, eb);
        tail(sa, sb, ea, eb);
        __builtin_amdgcn_s_setprio(0);
      }
      CST(22 + 32 * Ly);
      STAMP(12 + 16 * Ly);
      if (Ly == 0) {
        const int cprev = c >= 2 ? c - 2 : c;
        publish(ws, 4 * 32 + c, j, tag, tid);
        if (role) fillh<0>(rw, wq0, voff);
        fillh<1>(rw, wq0, voff);
        await(ws, 4, c, cprev, tag, w, lane);
        STAMP(17);
        if (w < 5) stage_tile(rs, X2_OFF + (w == 4 ? cprev * XBC + 3 * REV : c * XBC + w * REV), L, w == 4 ? 0 : w + 1, w == 4, lane);
        bar();
        qkv(std::integral_constant<int, 1>{});
      }
    }
  }
}

extern "C" void kernel_launch(void* const* d_in, const int* in_sizes, int n_in, void* d_out, int out_size, void* d_ws,
                              size_t ws_size, hipStream_t stream) {
  const u16* x = (const u16*)d_in[0];
  const u16* wqkv = (const u16*)d_in[1];
  const u16* wo = (const u16*)d_in[2];
  const u16* w1 = (const u16*)d_in[4];
  const u16* b1 = (const u16*)d_in[5];
  const u16* w2 = (const u16*)d_in[6];
  const u16* b2 = (const u16*)d_in[7];
  (void)hipFuncSetAttribute((const void*)k_main, hipFuncAttributeMaxDynamicSharedMemorySize, LDS_BYTES);
  k_main<<<256, 512, LDS_BYTES, stream>>>((char*)d_ws, x, wqkv, wo, w1, b1, w2, b2, (u16*)d_out);
}
```

```cpp
#include <hip/hip_runtime.h>
#include <hip/hip_bf16.h>
#include <type_traits>

#define GAS __attribute__((address_space(1)))
#define LAS __attribute__((address_space(3)))
typedef unsigned short u16;
typedef unsigned v4u __attribute__((ext_vector_type(4)));
typedef unsigned v2u __attribute__((ext_vector_type(2)));
typedef float f32x4 __attribute__((ext_vector_type(4)));
typedef float f32x2 __attribute__((ext_vector_type(2)));
typedef short bf16x8 __attribute__((ext_vector_type(8)));
typedef __bf16 bf16x2 __attribute__((ext_vector_type(2)));
typedef GAS unsigned gu32;
typedef unsigned long long u64;
typedef GAS u64 gu64;
typedef __amdgpu_buffer_rsrc_t rsrc_t;
#define RLX_AGENT __ATOMIC_RELAXED, __HIP_MEMORY_SCOPE_AGENT

#define TRICKLE 4
#define G2PK 0
#define TSH 20
#define SFILL1 0
#define TK 15
#define W2GAP 2
#define FV 2
#define GSPREAD 2

constexpr int S_ = 2048, D_ = 576, F_ = 4608;
constexpr int REV = 18 * 1024;
constexpr int XBC = 4 * REV;
constexpr int XBSZ = 32 * XBC;
constexpr int FL_OFF = 0;
constexpr int WQ_OFF = 131072;
constexpr int WO_OFF = WQ_OFF + 128 * REV;
constexpr int MS_OFF = WO_OFF + 36 * REV;
constexpr int MSJ = 72 * REV;
constexpr int CSP_OFF = MS_OFF + 8 * MSJ;
constexpr int X2_OFF = CSP_OFF + 8 * 18 * 2304 + 4096 - (8 * 18 * 2304) % 4096;
constexpr int OB_OFF = X2_OFF + XBSZ;
constexpr int X1_OFF = OB_OFF + XBSZ;
constexpr int PB_OFF = X1_OFF + XBSZ;
constexpr int PBSZ = 32 * 8 * 8 * 9216;
constexpr int ST_OFF = PB_OFF + PBSZ;
constexpr int ST2_OFF = ST_OFF + 256 * 64 * 8;
constexpr int WS_BYTES = ST2_OFF + 2 * 256 * 8 * 64 * 8;
constexpr int L_X = 0, L_H = 73728, L_ATT = 92160, ATT_STR = 10240, A_Q = 0, A_K = 2560, A_V = 5120, A_E = 7680;
constexpr int L_RES = 137216, L_OWN = 147456, L_CS1 = L_OWN + 8192, L_B1 = L_CS1 + 2304, L_SP = L_B1 + 2304,
              L_SH = L_SP + 1024, L_B2 = L_SH + 768, L_W = L_B2 + 384, L_END = L_W + 64;
static_assert(L_END <= 163840, "lds");
constexpr int LDS_BYTES = 163840;

__device__ __forceinline__ float bf2f(u16 b) { return __uint_as_float(((unsigned)b) << 16); }
__device__ __forceinline__ float bflo(unsigned u) { return __uint_as_float(u << 16); }
__device__ __forceinline__ float bfhi(unsigned u) { return __uint_as_float(u & 0xffff0000u); }
__device__ __forceinline__ unsigned pk2(float a, float b) {
  f32x2 v = {a, b};
  return __builtin_bit_cast(unsigned, __builtin_convertvector(v, bf16x2));
}
__device__ __forceinline__ v2u pk4(f32x4 v) { return v2u{pk2(v[0], v[1]), pk2(v[2], v[3])}; }
__device__ __forceinline__ f32x4 unpk4(unsigned a, unsigned b) { return f32x4{bflo(a), bfhi(a), bflo(b), bfhi(b)}; }
__device__ __forceinline__ f32x4 mfma(v4u a, v4u b, f32x4 c) {
  return __builtin_amdgcn_mfma_f32_16x16x32_bf16(__builtin_bit_cast(bf16x8, a), __builtin_bit_cast(bf16x8, b), c, 0, 0, 0);
}
__device__ __forceinline__ v4u ldw(rsrc_t r, int voff, int soff) { return __builtin_amdgcn_raw_buffer_load_b128(r, voff, soff, 0); }
__device__ __forceinline__ v4u ldc(rsrc_t r, int voff, int soff) { return __builtin_amdgcn_raw_buffer_load_b128(r, voff, soff, 16); }
__device__ __forceinline__ void stc128(v4u v, rsrc_t r, int voff, int soff) {
  __builtin_amdgcn_raw_buffer_store_b128(v, r, voff, soff, 16);
  asm volatile("s_nop 1" :: "v"(v));
}
__device__ __forceinline__ v2u ldc64(rsrc_t r, int voff, int soff) { return __builtin_amdgcn_raw_buffer_load_b64(r, voff, soff, 16); }
__device__ __forceinline__ void stc64(v2u v, rsrc_t r, int voff, int soff) { __builtin_amdgcn_raw_buffer_store_b64(v, r, voff, soff, 16); }
__device__ __forceinline__ void stc32(unsigned v, rsrc_t r, int voff, int soff) { __builtin_amdgcn_raw_buffer_store_b32(v, r, voff, soff, 16); }
#define SB() __builtin_amdgcn_sched_barrier(0)
__device__ __forceinline__ void bar() {
  asm volatile("s_waitcnt lgkmcnt(0)" ::: "memory");
  __builtin_amdgcn_s_barrier();
  asm volatile("" ::: "memory");
}
__device__ __forceinline__ float red16max(float x) {
  auto r = __builtin_amdgcn_permlane16_swap(__float_as_uint(x), __float_as_uint(x), false, false);
  x = fmaxf(__uint_as_float(r[0]), __uint_as_float(r[1]));
  auto q = __builtin_amdgcn_permlane32_swap(__float_as_uint(x), __float_as_uint(x), false, false);
  return fmaxf(__uint_as_float(q[0]), __uint_as_float(q[1]));
}
__device__ __forceinline__ float red16sum(float x) {
  auto r = __builtin_amdgcn_permlane16_swap(__float_as_uint(x), __float_as_uint(x), false, false);
  x = __uint_as_float(r[0]) + __uint_as_float(r[1]);
  auto q = __builtin_amdgcn_permlane32_swap(__float_as_uint(x), __float_as_uint(x), false, false);
  return __uint_as_float(q[0]) + __uint_as_float(q[1]);
}
__device__ __forceinline__ int ms_wave(int w) { return 162 * 1024 * w; }
__device__ __forceinline__ int kperm(int s, int g, int e) { return 32 * s + 4 * g + (e & 3) + ((e >> 2) << 4); }

#define STAMP(i) do {} while (0)
#define CST(i) do {} while (0)

__device__ __forceinline__ gu64* flagp(char* ws, int line, int i) { return (gu64*)(ws + FL_OFF + 256 + line * 256 + i * 8); }
__device__ __forceinline__ void publish(char* ws, int line, int i, u64 tag, int tid) {
  asm volatile("s_waitcnt vmcnt(0)" ::: "memory");
  bar();
  if (tid == 0) __hip_atomic_store(flagp(ws, line, i), tag, RLX_AGENT);
}
template <int SL> __device__ __forceinline__ void poll(gu64* p, u64 tag) {
  for (unsigned spins = 0; spins < (1u << 22); ++spins) {
    const u64 v = __hip_atomic_load(p, RLX_AGENT);
    if (__all(v == tag)) break;
    if (SL) __builtin_amdgcn_s_sleep(SL);
  }
  __builtin_amdgcn_fence(__ATOMIC_ACQUIRE, "wavefront");
}
template <int SL> __device__ __forceinline__ void poll8(char* ws, int ph, int c, int c2, u64 tag, int lane) {
  poll<SL>(flagp(ws, ph * 32 + ((lane & 8) ? c2 : c), lane & 7), tag);
}
__device__ __forceinline__ void await(char* ws, int ph, int c, int c2, u64 tag, int w, int lane) {
  if (w == 0) poll8<0>(ws, ph, c, c2, tag, lane);
  bar();
}

__device__ __forceinline__ float sfma(float a, float b, float c) { float d; asm("v_fma_f32 %0, %1, %2, %3" : "=v"(d) : "v"(a), "v"(b), "v"(c)); return d; }
__device__ __forceinline__ float sfnma(float a, float b, float c) { float d; asm("v_fma_f32 %0, -%1, %2, %3" : "=v"(d) : "v"(a), "v"(b), "v"(c)); return d; }
__device__ __forceinline__ float sfmas(float a, float s) { float d; asm("v_fma_f32 %0, %1, %2, %2" : "=v"(d) : "v"(a), "s"(s)); return d; }
__device__ __forceinline__ float ssqs(float a, float s) { float d; asm("v_fma_f32 %0, %1, %1, %2" : "=v"(d) : "v"(a), "s"(s)); return d; }
__device__ __forceinline__ float smul(float a, float b) { float d; asm("v_mul_f32 %0, %1, %2" : "=v"(d) : "v"(a), "v"(b)); return d; }
__device__ __forceinline__ float sadd(float a, float b) { float d; asm("v_add_f32 %0, %1, %2" : "=v"(d) : "v"(a), "v"(b)); return d; }
__device__ __forceinline__ void stat2(v4u x, float& s1, float& s2) {
  const bf16x2 ones = __builtin_bit_cast(bf16x2, 0x3f803f80u);
#pragma unroll
  for (int i = 0; i < 4; ++i) {
    const unsigned u = x[i];
    const bf16x2 v = __builtin_bit_cast(bf16x2, u);
    s1 = __builtin_amdgcn_fdot2_f32_bf16(v, ones, s1, false);
    s2 = __builtin_amdgcn_fdot2_f32_bf16(v, v, s2, false);
  }
}
__device__ __forceinline__ void stat_fin(float s1, float s2, float scale, float& rstd, float& mr) {
  const float inv = 1.0f / D_, mean = smul(s1, inv);
  const float var = fmaxf(sfnma(mean, mean, smul(s2, inv)), 0.f);
  float r = __builtin_amdgcn_rsqf(sadd(var, 1e-5f));
  asm volatile("s_nop 0" : "+v"(r));
  rstd = smul(r, scale);
  mr = smul(mean, rstd);
}
__device__ __forceinline__ void stat_own(const LAS char* L, int tt, int m, float scale, float& rstd, float& mr) {
  const f32x4 p = *(const LAS f32x4*)(L + L_SP + tt * 256 + m * 16);
  stat_fin(sadd(p[0], p[2]), sadd(p[1], p[3]), scale, rstd, mr);
}
__device__ __forceinline__ void stat_halo(const LAS char* L, int m, float& rstd, float& mr) {
  const f32x4 p = *(const LAS f32x4*)(L + L_SH + m * 48), q = *(const LAS f32x4*)(L + L_SH + m * 48 + 16), r = *(const LAS f32x4*)(L + L_SH + m * 48 + 32);
  stat_fin(sadd(sadd(sadd(p[0], p[2]), sadd(q[0], q[2])), sadd(r[0], r[2])), sadd(sadd(sadd(p[1], p[3]), sadd(q[1], q[3])), sadd(r[1], r[3])), 1.0f, rstd, mr);
}
__device__ __forceinline__ void ones_row(LAS char* L, int lane) {
  if ((lane & 15) == 0) {
#pragma unroll
    for (int s = 0; s < 18; ++s) *(LAS v4u*)(L + L_X + s * 1024 + lane * 16) = v4u{0x3f803f80u, 0x3f803f80u, 0x3f803f80u, 0x3f803f80u};
  }
}
template <int CTRL> __device__ __forceinline__ float dppadd(float x) {
  return x + __uint_as_float(__builtin_amdgcn_update_dpp(0u, __float_as_uint(x), CTRL, 0xf, 0xf, true));
}
__device__ __forceinline__ float red8sum(float x) { return dppadd<0x141>(dppadd<0x4E>(dppadd<0xB1>(x))); }
template <bool HALO> __device__ __forceinline__ void stage(rsrc_t rs, int xo, int ho, LAS char* L, int w, int lane) {
  const int tt = w & 3, kh = w >> 2, voff = lane * 16, hw = min(w, 5);
  v4u x[9], h[3];
#pragma unroll
  for (int s = 0; s < 9; ++s) x[s] = ldc(rs, voff + (s & 3) * 1024, xo + tt * REV + kh * 9216 + (s >> 2) * 4096);
  if (HALO) {
#pragma unroll
    for (int s = 0; s < 3; ++s) h[s] = ldc(rs, voff + s * 1024, ho + hw * 3072);
  }
  SB();
  float s1[2] = {0.f, 0.f}, s2[2] = {0.f, 0.f};
#pragma unroll
  for (int s = 0; s < 9; ++s) {
    *(LAS v4u*)(L + L_X + (tt + (HALO ? 1 : 0)) * REV + kh * 9216 + s * 1024 + voff) = x[s];
    stat2(x[s], s1[s & 1], s2[s & 1]);
    SB();
  }
  float a = red16sum(s1[0] + s1[1]), b = red16sum(s2[0] + s2[1]);
  if (lane < 16) *(LAS f32x2*)(L + L_SP + tt * 256 + lane * 16 + kh * 8) = f32x2{a, b};
  if (HALO) {
    a = 0.f; b = 0.f;
    const v4u ones = {0x3f803f80u, 0x3f803f80u, 0x3f803f80u, 0x3f803f80u};
#pragma unroll
    for (int s = 0; s < 3; ++s) {
      *(LAS v4u*)(L + L_X + hw * 3072 + s * 1024 + voff) = (lane & 15) ? h[s] : ones;
      stat2(h[s], a, b);
    }
    a = red16sum(a); b = red16sum(b);
    if (lane < 16) *(LAS f32x2*)(L + L_SH + lane * 48 + hw * 8) = f32x2{a, b};
  }
}

#define RING 28
constexpr int R_ = RING;
constexpr int RB = 256 - 4 * R_;
template <int SLOT, int OFF> __device__ __forceinline__ void rload(v4u rs, int voff, int so) {
  asm volatile("buffer_load_dwordx4 v[%c3:%c4], %0, %1, %2 offen offset:%c5 sc1"
               :: "v"(voff), "s"(rs), "s"(so), "i"(RB + 4 * SLOT), "i"(RB + 4 * SLOT + 3), "i"(OFF));
}
template <int SLOT, int OFF> __device__ __forceinline__ void rput(unsigned lds) {
  asm volatile("ds_write_b128 %0, v[%c1:%c2] offset:%c3" :: "v"(lds), "i"(RB + 4 * SLOT), "i"(RB + 4 * SLOT + 3), "i"(OFF) : "memory");
}
template <int N> __device__ __forceinline__ void rwait() { asm volatile("s_waitcnt vmcnt(%c0)" :: "i"(N)); }
template <int SLOT> __device__ __forceinline__ void rmfma(f32x4& acc, v4u x) {
  asm volatile("v_mfma_f32_16x16x32_bf16 %0, v[%c2:%c3], %1, %0" : "+v"(acc) : "v"(x), "i"(RB + 4 * SLOT), "i"(RB + 4 * SLOT + 3));
}
template <int SLOT> __device__ __forceinline__ void rmfma0(f32x4& acc, v4u x) {
  asm volatile("v_mfma_f32_16x16x32_bf16 %0, v[%c2:%c3], %1, 0" : "=&v"(acc) : "v"(x), "i"(RB + 4 * SLOT), "i"(RB + 4 * SLOT + 3));
}
template <int I, int N, class Fn> __device__ __forceinline__ void sfor(Fn&& f) {
  if constexpr (I < N) { f(std::integral_constant<int, I>{}); sfor<I + 1, N>(f); }
}
constexpr int cmin(int a, int b) { return a < b ? a : b; }
template <int P, int TOTAL> __device__ __forceinline__ void refill(v4u r, int mb, int voff) {
  constexpr int Q = P + R_;
  if constexpr (Q < TOTAL) rload<P % R_, (Q & 3) * 1024>(r, voff, mb + (Q >> 2) * 4096);
}
struct NoHook { template <int S> static constexpr int extra() { return 0; } template <int S> __device__ __forceinline__ void at() {} };
template <int NT, int NK, int S0, int S1, int P0, int TOTAL, bool INIT, int I0 = R_, int NKL = NK, class Hook = NoHook>
__device__ __forceinline__ void segx(f32x4* const (&acc)[NT], const LAS char* const (&xb)[NK], v4u r, int mb, int voff, Hook& hook) {
  constexpr int XD = NT >= 4 ? 1 : (NT == 1 ? 4 : 2);
  v4u xf[XD][NK];
#pragma unroll
  for (int d = 0; d < XD; ++d)
#pragma unroll
    for (int tt = 0; tt < NK; ++tt)
      if (S0 + d < S1) xf[d][tt] = *(const LAS v4u*)(xb[tt] + (S0 + d) * 1024);
  SB();
  asm volatile("s_nop 1");
  sfor<S0, S1>([&](auto sc) {
    constexpr int s = sc.value, p = P0 + (s - S0) * NT, d = (s - S0) % XD;
    constexpr int RS = (R_ - I0 + 3) / 4;
    constexpr int lag = RS > 1 ? RS : 1;
    constexpr int k = s - S0;
    constexpr int issued = k < RS ? I0 + 4 * k : cmin(TOTAL, R_ + (k < lag ? P0 : p - lag * NT));
    rwait<issued - (p + NT) + Hook::template extra<s>()>();
    if constexpr (k < RS) sfor<I0 + 4 * k, cmin(R_, I0 + 4 * k + 4)>([&](auto qc) { rload<qc.value, (qc.value & 3) * 1024>(r, voff, mb + (qc.value >> 2) * 4096); });
    hook.template at<s>();
    sfor<0, NK>([&](auto kc) {
      constexpr int tt = kc.value;
      sfor<0, NT>([&](auto tc) {
        constexpr int t = tc.value;
        if constexpr (t == NT - 1 && tt >= NKL) {}
        else if constexpr (INIT && s == S0) rmfma0<(p + t) % R_>(acc[t][tt], xf[d][tt]);
        else rmfma<(p + t) % R_>(acc[t][tt], xf[d][tt]);
        constexpr int idx = tt * NT + t;
        if constexpr (k >= lag && idx % NK == 0 && idx / NK < NT) refill<p - lag * NT + idx / NK, TOTAL>(r, mb, voff);
      });
      SB();
      if constexpr (s + XD < S1) { xf[d][tt] = *(const LAS v4u*)(xb[tt] + (s + XD) * 1024); SB(); }
    });
  });
  static_assert(I0 == R_ || P0 + (S1 - S0 - (R_ - I0 + 3) / 4) * NT + R_ >= TOTAL, "ramp: all refills are out inside the loop");
  sfor<0, NT>([&](auto tc) { refill<P0 + (S1 - 1 - S0) * NT + tc.value, TOTAL>(r, mb, voff); });
  asm volatile("s_nop 7\n\ts_nop 1");
  SB();
}
template <int NT, int NK, int S0, int S1, int P0, int TOTAL, bool INIT, class Hook = NoHook>
__device__ __forceinline__ void seg(f32x4* const (&acc)[NT], const LAS char* xb, v4u r, int mb, int voff, Hook hook = Hook{}) {
  const LAS char* x2 = xb + 2 * REV;
  asm volatile("" : "+v"(x2));
  segx<NT, 4, S0, S1, P0, TOTAL, INIT, R_, 4, Hook>(acc, {xb, xb + REV, x2, x2 + REV}, r, mb, voff, hook);
}
template <int LO> __device__ __forceinline__ void fillh(v4u r, int n, int voff) {
  sfor<LO ? R_ / 2 : 0, LO ? R_ : R_ / 2>([&](auto sc) { rload<sc.value, (sc.value & 3) * 1024>(r, voff, n + (sc.value >> 2) * 4096); });
  SB();
}
__device__ __forceinline__ v2u trread(const LAS char* p) {
  typedef short s4 __attribute__((ext_vector_type(4)));
  return __builtin_bit_cast(v2u, __builtin_amdgcn_ds_read_tr16_b64_v4i16((LAS s4*)p));
}
constexpr float AL = 0.46866869f, KK = 2.3022082f / AL;
#define EACH16 _Pragma("unroll") for (int tt = 0; tt < 4; ++tt) _Pragma("unroll") for (int r = 0; r < 4; ++r)
__device__ __forceinline__ void gelu16(f32x4 (&a)[4], const float (&rstd)[4], const float (&mr)[4], f32x4 cs, f32x4 bb, v2u (&o)[4]) {
  float e[4][4], u[4][4];
  EACH16 e[tt][r] = sfnma(cs[r], mr[tt], bb[r]);
  SB();
  EACH16 u[tt][r] = sfma(a[tt][r], rstd[tt], e[tt][r]);
  SB();
  EACH16 e[tt][r] = ssqs(u[tt][r], KK);
  SB();
  EACH16 e[tt][r] = smul(e[tt][r], u[tt][r]);
  SB();
  EACH16 e[tt][r] = __builtin_amdgcn_exp2f(-e[tt][r]);
  SB();
  EACH16 e[tt][r] = sfmas(e[tt][r], AL);
  SB();
  EACH16 e[tt][r] = __builtin_amdgcn_rcpf(e[tt][r]);
  SB();
  EACH16 u[tt][r] = smul(u[tt][r], e[tt][r]);
  SB();
#pragma unroll
  for (int tt = 0; tt < 4; ++tt) o[tt] = v2u{pk2(u[tt][0], u[tt][1]), pk2(u[tt][2], u[tt][3])};
  SB();
}
__device__ __forceinline__ void gelu16p(f32x4 (&a)[4], const float (&rstd)[4], const float (&mr)[4], f32x4 cs, f32x4 bb, v2u (&o)[4]) {
  f32x4 e[4];
#pragma unroll
  for (int tt = 0; tt < 4; ++tt) e[tt] = bb - cs * mr[tt];
  SB();
#pragma unroll
  for (int tt = 0; tt < 4; ++tt) a[tt] = a[tt] * rstd[tt] + e[tt];
  SB();
#pragma unroll
  for (int tt = 0; tt < 4; ++tt) e[tt] = a[tt] * a[tt] + KK;
  SB();
#pragma unroll
  for (int tt = 0; tt < 4; ++tt) e[tt] = e[tt] * a[tt];
  SB();
  EACH16 e[tt][r] = __builtin_amdgcn_exp2f(-e[tt][r]);
  SB();
#pragma unroll
  for (int tt = 0; tt < 4; ++tt) e[tt] = e[tt] * AL + AL;
  SB();
  EACH16 e[tt][r] = __builtin_amdgcn_rcpf(e[tt][r]);
  SB();
#pragma unroll
  for (int tt = 0; tt < 4; ++tt) a[tt] = a[tt] * e[tt];
  SB();
#pragma unroll
  for (int tt = 0; tt < 4; ++tt) o[tt] = pk4(a[tt]);
  SB();
}
struct StoreHook {
  v4u d[4]; rsrc_t rs; int voff, po0, po1;
  template <int S> static constexpr int extra() { return S <= 6 ? 0 : S <= 9 ? 1 : S <= 12 ? 2 : S <= 15 ? 3 : 4; }
  template <int S> __device__ __forceinline__ void at() {
    if constexpr (S == 6 || S == 9 || S == 12 || S == 15) {
      constexpr int q = (S - 6) / 3;
      SB(); stc128(d[q], rs, voff, po0 + q * po1); SB();
    }
  }
};
template <int SL> __device__ __forceinline__ void lds_wait_ge(LAS char* L, int word, unsigned want) {
  volatile LAS unsigned* p = (volatile LAS unsigned*)(L + L_W + word * 4);
  for (unsigned spins = 0; spins < (1u << 22); ++spins) {
    if (__builtin_amdgcn_readfirstlane(*p) >= want) break;
    __builtin_amdgcn_s_sleep(SL);
  }
  asm volatile("" ::: "memory");
}
template <int N> __device__ __forceinline__ void arrive(char* ws, LAS char* L, int word, int line, int i, u64 tag, int lane) {
  asm volatile("s_waitcnt vmcnt(0)" ::: "memory");
  if (lane == 0) {
    const unsigned old = __hip_atomic_fetch_add((LAS unsigned*)(L + L_W + word * 4), 1u, __ATOMIC_RELAXED, __HIP_MEMORY_SCOPE_WORKGROUP);
    if (old % N == N - 1) __hip_atomic_store(flagp(ws, line, i), tag, RLX_AGENT);
  }
}
__device__ __forceinline__ void acc8(f32x4& a, f32x4& b, v4u p) {
  a[0] = sadd(a[0], bflo(p[0])); a[1] = sadd(a[1], bfhi(p[0])); a[2] = sadd(a[2], bflo(p[1])); a[3] = sadd(a[3], bfhi(p[1]));
  b[0] = sadd(b[0], bflo(p[2])); b[1] = sadd(b[1], bfhi(p[2])); b[2] = sadd(b[2], bflo(p[3])); b[3] = sadd(b[3], bfhi(p[3]));
}

#define FRESH() int lane = lane0, c = c0, j = j0, w = w0; asm volatile("" : "+v"(lane)); asm volatile("" : "+s"(c), "+s"(j), "+s"(w)); \
  const int g = lane >> 4, m = lane & 15, voff = lane * 16, role = w >> 2, hl = w & 3; (void)g; (void)m; (void)voff; (void)role; (void)hl;

__device__ __forceinline__ void jload(int job, v4u (&r)[4], const u16* w1, const u16* w2, const u16* wo, int j, int lane) {
      const int kind = job < 162 ? 0 : (job < 324 ? 1 : 2);
      const int q = job - 162 * kind, s9 = q / 9, s = kind < 2 ? s9 : (q < 18 ? q : q - 18), B = kind < 2 ? q - 9 * s9 : (q < 18 ? j : 8);
      const u16* src = kind == 0 ? w1 + (32 * s * F_ + 576 * j) : (kind == 1 ? w2 + (576 * j + 32 * s) * D_ : wo);
      const int ld = kind == 0 ? F_ : D_;
#pragma unroll
      for (int i = 0; i < 4; ++i) {
        const int rho = 8 * i + (lane >> 3);
        const int orow = s < 16 ? 36 * s + rho + (i >= 2 ? 4 : 0) : 36 * (8 * (s - 16) + 2 * ((rho >> 2) & 3) + (i >> 1)) + 16 + (rho & 3);
        r[i] = *(const v4u*)(src + (kind < 2 ? rho : orow) * ld + 64 * B + (lane & 7) * 8);
      }
}
__device__ __forceinline__ void jput(int job, v4u (&r)[4], LAS char* scr, rsrc_t rs, int j, int lane) {
      const int g = lane >> 4, m = lane & 15, voff = lane * 16;
      const int kind = job < 162 ? 0 : (job < 324 ? 1 : 2);
      const bool one = kind == 0;
      const int q = job - 162 * kind, s9 = q / 9, s = kind < 2 ? s9 : (q < 18 ? q : q - 18), B = kind < 2 ? q - 9 * s9 : (q < 18 ? j : 8);
      const int d = B < 8 ? B : j;
      const int e0 = 162 * (d >> 1) + (s < 8 ? 72 + 4 * s + 2 * (d & 1) : 88 + 20 * (d & 1) + 2 * s), e1 = 162 * (4 + (d >> 1)) + 90 + 36 * (d & 1) + 2 * s;
      const bool spread = one ? B == 8 : B == j;
      int base = one ? (B < 4 ? 162 * B + 4 * s : (B < 8 ? 162 * (4 + 2 * (B & 1)) + 5 * s + (B >= 6 ? 2 : 0) : 652 + 5 * s)) : (spread ? 144 + s : e0);
      int c1 = spread ? 162 : 1, c2 = spread ? 324 : (one ? (B < 4 ? 2 : 162) : e1 - e0);
      base = kind < 2 ? (MS_OFF + j * MSJ) / 1024 + base : WO_OFF / 1024 + 72 * B + s;
      c1 = kind < 2 ? c1 : 18; c2 = kind < 2 ? c2 : 36;
      v2u lo[4], hi[4];
      const LAS char* p = scr + (4 * g + (m >> 2)) * 128 + (m & 3) * 8;
      *(LAS v4u*)(scr + voff) = r[0]; *(LAS v4u*)(scr + 1024 + voff) = r[1];
#pragma unroll
      for (int t = 0; t < 4; ++t) lo[t] = trread(p + t * 32);
      SB();
      *(LAS v4u*)(scr + voff) = r[2]; *(LAS v4u*)(scr + 1024 + voff) = r[3];
#pragma unroll
      for (int t = 0; t < 4; ++t) hi[t] = trread(p + t * 32);
      float cs4[4];
#pragma unroll
      for (int t = 0; t < 4; ++t) {
        const v4u f = {lo[t][0], lo[t][1], hi[t][0], hi[t][1]};
        stc128(f, rs, voff, (base + (t & 1) * c1 + (t >> 1) * c2) * 1024);
        float a = 0.f, dummy = 0.f;
        stat2(f, a, dummy);
        cs4[t] = red16sum(a);
      }
      const float csum = g < 2 ? (g == 0 ? cs4[0] : cs4[1]) : (g == 2 ? cs4[2] : cs4[3]);
      if (one) stc32(__float_as_uint(csum), rs, lane * 4, CSP_OFF + (j * 18 + s) * 2304 + B * 256);
}

__device__ __forceinline__ void jtouch(const u16* base, int ld, int qa, int qb, int lane) {
  const int q = lane < 32 ? qa : qb, s = (q * 7282) >> 16;
  const u16* p = base + (32 * s + (lane & 31)) * ld + 64 * (q - 9 * s);
  asm volatile("global_load_dword v%c1, %0, off" :: "v"(p), "i"(RB - 1));
}
__device__ __forceinline__ void wo_req(v4u rw, int j, int hl, int voff) {
  const int wa = WO_OFF + (4 * j + hl) * REV, wb = WO_OFF + (32 + 2 * (j >> 2)) * REV + hl * 9216;
  sfor<0, 27>([&](auto ic) { constexpr int i = ic.value, q = i < 18 ? i : i - 18; rload<i, (q & 3) * 1024>(rw, voff, (i < 18 ? wa : wb) + (q >> 2) * 4096); });
  SB();
}
struct TouchHook {
  const u16* base; int qa, lane;
  template <int S> static constexpr int extra() { return S <= TK ? 0 : 1; }
  template <int S> __device__ __forceinline__ void at() { if constexpr (S == TK) { SB(); jtouch(base, D_, qa, min(qa + 128, 161), lane); SB(); } }
};
struct WordHook {
  LAS char* L; unsigned want;
  template <int S> static constexpr int extra() { return 0; }
  template <int S> __device__ __forceinline__ void at() { if constexpr (S == 10) { SB(); lds_wait_ge<1>(L, 10, want); SB(); } }
};
__device__ __forceinline__ void jobs2(int q0, int qa, const u16* w1, const u16* w2, LAS char* scr, rsrc_t rs, int j, int lane) {
  v4u jr[2][4];
  jload(q0 + qa, jr[0], w1, w2, w1, j, lane);
  if (qa + 128 < 162) jload(q0 + qa + 128, jr[1], w1, w2, w1, j, lane);
  jput(q0 + qa, jr[0], scr, rs, j, lane);
  if (qa + 128 < 162) jput(q0 + qa + 128, jr[1], scr, rs, j, lane);
}

__global__ __launch_bounds__(512, 2) __attribute__((amdgpu_num_vgpr(RB / 2 - 1)))
void k_main(char* __restrict__ ws, const u16* __restrict__ xin, const u16* __restrict__ wqkv, const u16* __restrict__ wo,
            const u16* __restrict__ w1, const u16* __restrict__ b1, const u16* __restrict__ w2, const u16* __restrict__ b2,
            u16* __restrict__ out) {
  extern __shared__ __attribute__((aligned(16))) char smem[];
  LAS char* L = (LAS char*)smem;
  const int tid = threadIdx.x, lane0 = tid & 63, w0 = __builtin_amdgcn_readfirstlane(tid >> 6);
  int c0, j0;
  {
    constexpr int G = GSPREAD, per = 8 / G;
    const int b = blockIdx.x, x = b & 7, r = b >> 3;
    j0 = (x % G) * per + (r % per);
    c0 = (x / G) * (4 * G) + r / per;
  }
  const rsrc_t rs = __builtin_amdgcn_make_buffer_rsrc((void*)ws, 0, WS_BYTES, 0x00020000);
  const v4u rw = {(unsigned)(size_t)ws, (unsigned)((size_t)ws >> 32) & 0xffffu, (unsigned)WS_BYTES, 0x00020000u};
  asm volatile("" ::: "v255");
  STAMP(0);
  CST(24);
  u64 tag = 0;
  auto qkv = [&](auto lc) {
    constexpr int Ly = lc.value;
    STAMP(2 + 16 * Ly);
    FRESH();
    {
      const LAS char* xr = L + L_X + (hl + 1) * REV + voff;
      const v4u ra = *(const LAS v4u*)(xr + (2 * j + role) * 1024), rb = *(const LAS v4u*)(xr + (16 + (j >> 2)) * 1024);
      *(LAS v4u*)(L + L_RES + w * 1024 + voff) = ra;
      if (w == (j & 3)) *(LAS v4u*)(L + L_RES + 8192 + voff) = rb;
    }
    const int qb = WQ_OFF + ((4 * j + hl) * 4 + 2 * role) * REV;
    constexpr int I0 = Ly == 0 ? 8 : R_;
    auto fix = [&](f32x4 a, const float (&cs)[4], float rstd, float mr) {
      float u[4];
#pragma unroll
      for (int r = 0; r < 4; ++r) u[r] = smul(a[r], rstd);
#pragma unroll
      for (int r = 0; r < 4; ++r) u[r] = sfnma(cs[r], mr, u[r]);
      return v2u{pk2(u[0], u[1]), pk2(u[2], u[3])};
    };
    auto bcast = [&](f32x4 a, float (&cs)[4]) {
#pragma unroll
      for (int r = 0; r < 4; ++r) cs[r] = __uint_as_float(__builtin_amdgcn_update_dpp(0u, __float_as_uint(a[r]), 0x150, 0xf, 0xf, false));
    };
    CST(0 + 32 * Ly);
    if (role == 0) {
      f32x4 a0[5], a1[5];
      const LAS char* xa = L + L_X + voff, *xc = xa + 3 * REV;
      asm volatile("" : "+v"(xc));
      std::conditional_t<Ly == 0, TouchHook, NoHook> hk;
      if constexpr (Ly == 0) { hk.base = w2 + 576 * j * D_; hk.qa = c * 4 + hl; hk.lane = lane; }
      float rstd[5], mr[5], cs0[4], cs1[4];
      stat_halo(L, m, rstd[0], mr[0]);
#pragma unroll
      for (int t = 1; t < 5; ++t) stat_own(L, t - 1, m, 1.0f, rstd[t], mr[t]);
      segx<2, 5, 0, 18, 0, 36, true, I0, 5>({a0, a1}, {xa, xa + REV, xa + 2 * REV, xc, xc + REV}, rw, qb, voff, hk);
      CST(1 + 32 * Ly);
      LAS char* A = L + L_ATT + hl * ATT_STR + lane * 8;
      bcast(a0[0], cs0); bcast(a1[0], cs1);
      SB();
#pragma unroll
      for (int t = 0; t < 5; ++t) {
        if (t) *(LAS v2u*)(A + A_Q + t * 512) = fix(a0[t], cs0, rstd[t], mr[t]);
        *(LAS v2u*)(A + A_K + t * 512) = fix(a1[t], cs1, rstd[t], mr[t]);
      }
    }
    int role2 = role;
    asm volatile("" : "+s"(role2));
    if (role2) {
      f32x4 a0[5], a1[5];
      const int ta = (hl & 1) ? 3 : 1, tb = 4 - ta;
      const LAS char* x0 = L + L_X + voff, *xa = x0 + ta * REV, *xc = x0 + tb * REV;
      NoHook hk;
      __builtin_amdgcn_s_setprio(1);
      segx<2, 5, 0, 18, 0, 36, true, I0, 3>({a0, a1}, {x0, xa, xa + REV, xc, xc + REV}, rw, qb, voff, hk);
      __builtin_amdgcn_s_setprio(0);
      CST(1 + 32 * Ly);
      v4u jr[2][4];
      if constexpr (Ly == 0) {
        jload(c * 4 + hl, jr[0], w1, w2, w1, j, lane);
        if (c * 4 + hl + 128 < 162) jload(c * 4 + hl + 128, jr[1], w1, w2, w1, j, lane);
        SB();
      }
      LAS char* A = L + L_ATT + hl * ATT_STR + A_V + m * 32 + g * 8, *E = L + L_ATT + (hl & 2) * ATT_STR + A_E + m * 32 + g * 8;
      float rstd[5], mr[5], cs0[4], cs1[4];
      int to[5] = {0, ta, ta + 1, tb, tb + 1};
      stat_halo(L, m, rstd[0], mr[0]);
#pragma unroll
      for (int t = 1; t < 5; ++t) stat_own(L, to[t] - 1, m, 1.0f, rstd[t], mr[t]);
      bcast(a0[0], cs0); bcast(a1[0], cs1);
      SB();
#pragma unroll
      for (int t = 0; t < 5; ++t) {
        *(LAS v2u*)(A + to[t] * 512) = fix(a0[t], cs0, rstd[t], mr[t]);
        if (t < 3) *(LAS v2u*)(E + to[t] * 512) = fix(a1[t], cs1, rstd[t], mr[t]);
      }
      if constexpr (Ly == 0) {
        jput(c * 4 + hl, jr[0], L + L_OWN + hl * 2048, rs, j, lane);
        if (c * 4 + hl + 128 < 162) jput(c * 4 + hl + 128, jr[1], L + L_OWN + hl * 2048, rs, j, lane);
      } else wo_req(rw, j, hl, voff);
    }
  };
  {
    FRESH();
    u64 gen = 0;
    if (w >= 5) gen = __hip_atomic_load((gu64*)(ws + FL_OFF), RLX_AGENT);
    if (w == 4) {
      u64 pc;
      asm volatile("s_getpc_b64 %0" : "=s"(pc));
      const char* cp = (const char*)(pc & ~(u64)127) + (blockIdx.x >> 3) * 2176 + min(lane, 16) * 128;
      asm volatile("global_load_dword v%c1, %0, off" :: "v"(cp), "i"(RB - 1));
    }
    if (w >= 5 && c < 18) {
      const int sec = w - 5;
      const u16* src = wqkv + (32 * c) * (3 * D_) + 72 * j;
      v4u qr[5];
      v2u xr[2];
#pragma unroll
      for (int i = 0; i < 5; ++i) {
        const int ch = min(64 * i + lane, 287), row = (ch * 7282) >> 16;
        qr[i] = *(const v4u*)(src + row * (3 * D_) + sec * D_ + (ch - 9 * row) * 8);
      }
#pragma unroll
      for (int i = 0; i < 2; ++i) xr[i] = *(const v2u*)(src + (16 * i + (lane >> 2)) * (3 * D_) + min(lane & 3, 2) * D_ + 36 * (sec & 1) + 16);
      SB();
      bar();

      LAS char* scr = L + L_ATT + 40960 + sec * 4736;
#pragma unroll
      for (int i = 0; i < 5; ++i) *(LAS v4u*)(scr + min(64 * i + lane, 287) * 16) = qr[i];
      const int qd = WQ_OFF + 16 * j * REV + (sec >> 1) * 2 * REV + (2 * c + (sec & 1)) * 1024;
#pragma unroll
      for (int h = 0; h < 4; ++h) {
        const LAS char* p = scr + (4 * g + (m >> 2)) * 144 + (h >> 1) * 72 + (h & 1) * 40 + (m & 3) * 8;
        const v2u lo = trread(p), hi = trread(p + 16 * 144);
        stc128(v4u{lo[0], lo[1], hi[0], hi[1]}, rs, voff, qd + h * 4 * REV);
      }
      if (sec < 2) {
#pragma unroll
        for (int i = 0; i < 2; ++i) *(LAS v2u*)(scr + i * 512 + lane * 8) = (lane & 3) == 3 ? v2u{0u, 0u} : xr[i];
        const v2u lo = trread(scr + lane * 8), hi = trread(scr + 512 + lane * 8);
        const int md = WQ_OFF + (16 * j + 8 * sec + 2) * REV + (2 * c + 1) * 1024;
        stc128(v4u{lo[0], lo[1], hi[0], hi[1]}, rs, voff, md);
        stc128(v4u{lo[0], lo[1], hi[0], hi[1]}, rs, voff, md + 4 * REV);
      }

      arrive<3>(ws, L, 9, 320 + j, c, gen + 1, lane);

    }
    int wl = w;
    asm volatile("" : "+s"(wl));
    if (wl >= 5 && c >= 18) {
      const int job = min(324 + (c - 18) * 3 + w - 5, 359);
      v4u jo[4];
      jload(job, jo, w1, w2, wo, j, lane);
      bar();
      jput(job, jo, L + L_ATT + 40960 + (w - 5) * 4096, rs, j, lane);
      arrive<3>(ws, L, 9, 320 + j, c, gen + 1, lane);
    }
    asm volatile("" : "+s"(wl));
    if (wl < 5) {
      const int cc = (w == 4 && c >= 2) ? c - 2 : c, tt = w == 4 ? 3 : w, t5 = w == 4 ? 0 : w + 1;
      const int r8 = lane >> 3, q8 = lane & 7;
      const u16* src = xin + (size_t)(2 * (64 * (cc >> 1) + 16 * tt + r8) + (cc & 1)) * D_ + 8 * q8;
      if (tid < 16) *(LAS unsigned*)(L + L_W + tid * 4) = 0u;
      bar();
      v4u x[2][9];
#pragma unroll
      for (int i = 0; i < 9; ++i) { x[0][i] = *(const v4u*)(src + 64 * i); x[1][i] = *(const v4u*)(src + 16 * D_ + 64 * i); }
      SB();

      float s1[2] = {0.f, 0.f}, s2[2] = {0.f, 0.f};
      LAS char* dst = L + L_X + t5 * REV + (q8 >> 2) * 1024 + ((q8 & 1) * 32 + r8) * 16 + ((q8 >> 1) & 1) * 8;
#pragma unroll
      for (int i = 0; i < 9; ++i)
#pragma unroll
        for (int h = 0; h < 2; ++h) {
          *(LAS v2u*)(dst + i * 2048 + h * 128) = v2u{x[h][i][0], x[h][i][1]};
          *(LAS v2u*)(dst + i * 2048 + h * 128 + 256) = v2u{x[h][i][2], x[h][i][3]};
          stat2(x[h][i], s1[h], s2[h]);
          SB();
        }
      if (w == 4) ones_row(L, lane);
#pragma unroll
      for (int h = 0; h < 2; ++h) { s1[h] = red8sum(s1[h]); s2[h] = red8sum(s2[h]); }
      if (q8 < 2) {
        const f32x4 p = {q8 ? s1[1] : s1[0], q8 ? s2[1] : s2[0], 0.f, 0.f}, z = {0.f, 0.f, 0.f, 0.f};
        if (w < 4) *(LAS f32x4*)(L + L_SP + w * 256 + (q8 * 8 + r8) * 16) = p;
        else { LAS char* d = L + L_SH + (q8 * 8 + r8) * 48; *(LAS f32x4*)d = p; *(LAS f32x4*)(d + 16) = z; *(LAS f32x4*)(d + 32) = z; }
      }

    }
    if (w == 6) {
      unsigned bv[5];
#pragma unroll
      for (int i = 0; i < 5; ++i) bv[i] = *(const unsigned*)(b1 + 576 * j + min(lane + 64 * i, 287) * 2);
      const unsigned b2v = *(const unsigned*)(b2 + (lane < 32 ? 64 * j + 2 * lane : 512 + 32 * (j >> 2) + 2 * (lane & 15)));
#pragma unroll
      for (int i = 0; i < 5; ++i) *(LAS f32x2*)(L + L_B1 + min(lane + 64 * i, 287) * 8) = f32x2{AL * bflo(bv[i]), AL * bfhi(bv[i])};
      if (lane < 48) *(LAS f32x2*)(L + L_B2 + lane * 8) = f32x2{bflo(b2v), bfhi(b2v)};
    }
    if (c >= 18 && w < 3) {
      const int q = (c - 18) * 12 + w * 4;
      jtouch(w1 + 576 * j, F_, min(q, 161), min(q + 1, 161), lane);
      jtouch(w1 + 576 * j, F_, min(q + 2, 161), min(q + 3, 161), lane);
    }
    STAMP(1);

    if (w == 7) {
      poll<0>(flagp(ws, 320 + j, min(lane, 17)), gen + 1);
      if (lane == 0) *(LAS u64*)(L + L_W + 48) = gen + 1;
    }

    bar();

    sfor<0, 8>([&](auto qc) { rload<qc.value, (qc.value & 3) * 1024>(rw, voff, WQ_OFF + ((4 * j + hl) * 4 + 2 * role) * REV + (qc.value >> 2) * 4096); });
    tag = *(LAS u64*)(L + L_W + 48);
    tag = ((u64)(unsigned)__builtin_amdgcn_readfirstlane((unsigned)(tag >> 32)) << 32) | (unsigned)__builtin_amdgcn_readfirstlane((unsigned)tag);
    qkv(std::integral_constant<int, 0>{});
  }

#pragma unroll 1
  for (int Ly = 0; Ly < 2; ++Ly) {
    CST(2 + 32 * Ly);
    bar();
    CST(3 + 32 * Ly);
    STAMP(3 + 16 * Ly);
    {
      FRESH();
      const LAS char* A = L + L_ATT + 2 * role * ATT_STR + hl * 512 + lane * 8;
      const LAS char* Ee = L + L_ATT + 2 * role * ATT_STR + hl * 512 + A_E + m * 32;
      v2u qm[2], km[2][2], vm[2][2], em[2], xk[2];
#pragma unroll
      for (int q = 0; q < 2; ++q) {
        qm[q] = *(const LAS v2u*)(A + q * ATT_STR + A_Q + 512);
        xk[q] = *(const LAS v2u*)(Ee + q * 512 + 8);
#pragma unroll
        for (int i = 0; i < 2; ++i) km[q][i] = *(const LAS v2u*)(A + q * ATT_STR + A_K + i * 512);
      }
      const v2u xq = *(const LAS v2u*)(Ee + 512);
#pragma unroll
      for (int q = 0; q < 2; ++q) {
        em[q] = trread(A + A_E + q * 512);
#pragma unroll
        for (int i = 0; i < 2; ++i) vm[q][i] = trread(A + q * ATT_STR + A_V + i * 512);
      }
      const float sc = 0.23570226039551584f * 1.4426950408889634f, NEG = -3.0e38f;
      f32x4 bp, bc;
#pragma unroll
      for (int r = 0; r < 4; ++r) {
        const int dk = m - (4 * g + r);
        bp[r] = (dk <= -9 && !(c < 2 && hl == 0)) ? 0.f : NEG;
        bc[r] = (unsigned)dk <= 7u ? 0.f : NEG;
      }
      SB();
      const f32x4 z = {0.f, 0.f, 0.f, 0.f};
      f32x4 sp[2], sq[2];
#pragma unroll
      for (int q = 0; q < 2; ++q) {
        const v4u qf = {qm[q][0], qm[q][1], g == 0 ? xq[q] : 0u, 0u};
        sp[q] = mfma(v4u{km[q][0][0], km[q][0][1], xk[0][q], 0u}, qf, z);
        sq[q] = mfma(v4u{km[q][1][0], km[q][1][1], xk[1][q], 0u}, qf, z);
      }
      float mx[2], sum[2];
#pragma unroll
      for (int q = 0; q < 2; ++q) {
        sp[q] = sp[q] * sc + bp; sq[q] = sq[q] * sc + bc;
        mx[q] = fmaxf(fmaxf(fmaxf(sp[q][0], sp[q][1]), fmaxf(sp[q][2], sp[q][3])), fmaxf(fmaxf(sq[q][0], sq[q][1]), fmaxf(sq[q][2], sq[q][3])));
      }
#pragma unroll
      for (int q = 0; q < 2; ++q) mx[q] = red16max(mx[q]);
#pragma unroll
      for (int q = 0; q < 2; ++q) {
        sp[q] = sp[q] - mx[q]; sq[q] = sq[q] - mx[q];
#pragma unroll
        for (int r = 0; r < 4; ++r) { sp[q][r] = __builtin_amdgcn_exp2f(sp[q][r]); sq[q][r] = __builtin_amdgcn_exp2f(sq[q][r]); }
        const f32x4 t4 = sp[q] + sq[q];
        sum[q] = (t4[0] + t4[1]) + (t4[2] + t4[3]);
      }
#pragma unroll
      for (int q = 0; q < 2; ++q) sum[q] = __builtin_amdgcn_rcpf(red16sum(sum[q]));
      f32x4 o[2], oe[2];
#pragma unroll
      for (int q = 0; q < 2; ++q) {
        const v2u pp = pk4(sp[q] * sum[q]), pc = pk4(sq[q] * sum[q]);
        const v4u pf = {pp[0], pp[1], pc[0], pc[1]};
        o[q] = mfma(v4u{vm[q][0][0], vm[q][0][1], vm[q][1][0], vm[q][1][1]}, pf, z);
        oe[q] = mfma(v4u{em[0][0], em[0][1], em[1][0], em[1][1]}, pf, z);
      }
      const int ob = OB_OFF + c * XBC + hl * REV;
      const v2u oa = pk4(o[0]), oc = pk4(o[1]);
      stc128(v4u{oa[0], oa[1], oc[0], oc[1]}, rs, voff, ob + (2 * j + role) * 1024);
      if (g == 2) stc64(v2u{pk2(oe[0][0], oe[0][1]), pk2(oe[1][2], oe[1][3])}, rs, m * 8, ob + 16384 + (2 * j + role) * 128);
      SB();
      CST(4 + 32 * Ly);
      STAMP(4 + 16 * Ly);
      publish(ws, (5 * Ly + 0) * 32 + c, j, tag, tid);
      CST(5 + 32 * Ly);
      if (Ly == 0 && tid == 0) __hip_atomic_store(flagp(ws, 328 + j, c), tag, RLX_AGENT);
      if (role) {
        if (Ly == 0) { poll<1>(flagp(ws, 320 + j, lane & 31), tag); wo_req(rw, j, hl, voff); rwait<0>(); }
        const unsigned la = (unsigned)(size_t)(L + L_X + hl * REV + voff), lb = (unsigned)(size_t)(L + L_X + 4 * REV + hl * 9216 + voff);
        sfor<0, 27>([&](auto ic) {
          constexpr int i = ic.value;
          rput<i, (i < 18 ? i : i - 18) * 1024>(i < 18 ? la : lb);
        });
      }
      if (SFILL1 && role) asm volatile("s_waitcnt lgkmcnt(0)" ::: "memory");
      if (Ly && (role == 0 || SFILL1)) fillh<0>(rw, MS_OFF + j * MSJ + ms_wave(w), voff);
    }
    {
      FRESH();
      const LAS char* wb = L + L_X + role * 9216 + voff;
      const LAS char* wb2 = wb + 3 * REV;
      asm volatile("" : "+v"(wb2));
      const bool ext = hl == (j & 3);
      v4u wf[2][4];
      auto wld = [&](int s) {
#pragma unroll
        for (int i = 0; i < 4; ++i) wf[s % 2][i] = *(const LAS v4u*)((i < 3 ? wb + i * REV : wb2) + s * 1024);
      };
      f32x4 acc[6];
      {
        const v4u ra = *(LAS v4u*)(L + L_RES + w * 1024 + voff), rb = *(LAS v4u*)(L + L_RES + 8192 + voff);
        acc[0] = unpk4(role ? 0u : ra[0], role ? 0u : ra[1]); acc[1] = unpk4(role ? 0u : ra[2], role ? 0u : ra[3]);
        acc[2] = unpk4(role ? ra[0] : 0u, role ? ra[1] : 0u); acc[3] = unpk4(role ? ra[2] : 0u, role ? ra[3] : 0u);
        acc[4] = unpk4(role ? 0u : rb[0], role ? 0u : rb[1]); acc[5] = unpk4(role ? 0u : rb[2], role ? 0u : rb[3]);
      }
      SB();
      CST(6 + 32 * Ly);
      await(ws, 5 * Ly + 0, c, c, tag, w, lane);
      CST(7 + 32 * Ly);
      STAMP(5 + 16 * Ly);
      v4u of[9];
      const int ob = OB_OFF + c * XBC + hl * REV + role * 9216;
#pragma unroll
      for (int s = 0; s < 7; ++s) of[s] = ldc(rs, voff + (s & 3) * 1024, ob + (s >> 2) * 4096);
      {
        const int va = role ? g * 256 + m * 8 : voff, vb = va + (role ? 128 : 8);
#pragma unroll
        for (int s = 7; s < 9; ++s) {
          const v2u lo = ldc64(rs, va + (s - 7) * 1024, ob + 7168), hi = ldc64(rs, vb + (s - 7) * 1024, ob + 7168);
          of[s] = v4u{lo[0], lo[1], hi[0], hi[1]};
        }
      }
      SB();
      wld(0); wld(1);
      SB();
#pragma unroll
      for (int s = 0; s < 9; ++s) {
#pragma unroll
        for (int i = 0; i < 4; ++i) acc[i] = mfma(wf[s % 2][i], of[s], acc[i]);
        SB();
        if (s + 2 < 9) { wld(s + 2); SB(); }
      }
      LAS char* R = L + L_X + 6 * REV + hl * 6144 + voff;
      *(LAS f32x4*)(R + role * 2048) = role ? acc[0] : acc[2];
      *(LAS f32x4*)(R + role * 2048 + 1024) = role ? acc[1] : acc[3];
      if (ext) {
#pragma unroll
        for (int s = 0; s < 9; ++s) {
          acc[4] = mfma(*(const LAS v4u*)(wb2 + REV + s * 1024), of[s], acc[4]);
          acc[5] = mfma(*(const LAS v4u*)(wb2 + 2 * REV + s * 1024), of[s], acc[5]);
        }
        if (role) { *(LAS f32x4*)(R + 4096) = acc[4]; *(LAS f32x4*)(R + 5120) = acc[5]; }
      }
      CST(25 + 32 * Ly);
      if (Ly == 0 && W2GAP == 1) publish(ws, 336 + j, c, tag, tid); else bar();
      {
        const f32x4 pa = *(LAS f32x4*)(R + 2048 - role * 2048), pb = *(LAS f32x4*)(R + 3072 - role * 2048);
        const v2u oa = pk4((role ? acc[2] : acc[0]) + pa), ob2 = pk4((role ? acc[3] : acc[1]) + pb);
        const int xb = X1_OFF + c * XBC + hl * REV;
        stc128(v4u{oa[0], oa[1], ob2[0], ob2[1]}, rs, voff, xb + (2 * j + role) * 1024);
        if (ext && role == 0) {
          const v2u ea = pk4(acc[4] + *(LAS f32x4*)(R + 4096)), eb = pk4(acc[5] + *(LAS f32x4*)(R + 5120));
          stc128(v4u{ea[0], ea[1], eb[0], eb[1]}, rs, voff, xb + (16 + (j >> 2)) * 1024);
        }
      }
      STAMP(6 + 16 * Ly);
      CST(26 + 32 * Ly);
      publish(ws, (5 * Ly + 1) * 32 + c, j, tag, tid);
      CST(27 + 32 * Ly);
      if (W2GAP == 2 && Ly == 0 && role) jobs2(162, c * 4 + hl, w1, w2, L + L_OWN + hl * 2048, rs, j, lane);
      if (Ly == 0) {
        if (w == 1) { poll<1>(flagp(ws, 328 + j, lane & 31), tag); *(volatile LAS unsigned*)(L + L_W + 32) = 2u; }
        else lds_wait_ge<1>(L, 8, 2u);
        fillh<0>(rw, MS_OFF + j * MSJ + ms_wave(w), voff);
      }
      if (role == 0) fillh<1>(rw, MS_OFF + j * MSJ + ms_wave(w), voff);
      else if (Ly && !SFILL1) fillh<0>(rw, MS_OFF + j * MSJ + ms_wave(w), voff);
      CST(28 + 32 * Ly);
      await(ws, 5 * Ly + 1, c, c, tag, w, lane);
      CST(29 + 32 * Ly);
      if (Ly == 0 && blockIdx.x == 0 && tid == 0) __hip_atomic_store((gu64*)(ws + FL_OFF), tag, RLX_AGENT);
    }
    STAMP(7 + 16 * Ly);
    {
      FRESH();
      stage<false>(rs, X1_OFF + c * XBC, 0, L, w, lane);
      CST(30 + 32 * Ly);
    }
    if (W2GAP == 2 && Ly == 0) publish(ws, 336 + j0, c0, tag, tid); else bar();
    STAMP(8 + 16 * Ly);
    {
      FRESH();
      const int mb = MS_OFF + j * MSJ + ms_wave(w);
      const int pbc = PB_OFF + c * (64 * 9216);
      const int wq0 = WQ_OFF + ((4 * j + hl) * 4 + 2 * role) * REV;
      auto stats = [&](float (&rstd)[4], float (&mr)[4]) {
#pragma unroll
        for (int tt = 0; tt < 4; ++tt) stat_own(L, tt, m, AL, rstd[tt], mr[tt]);
      };
      auto gtile = [&](auto pkc, f32x4 (&x)[4], const float (&rstd)[4], const float (&mr)[4], int T, v2u (&o)[4]) {
        const f32x4 cs = *(LAS f32x4*)(L + L_CS1 + T * 64 + g * 16), bb = *(LAS f32x4*)(L + L_B1 + T * 64 + g * 16);
        if constexpr (pkc.value) gelu16p(x, rstd, mr, cs, bb, o); else gelu16(x, rstd, mr, cs, bb, o);
      };
      auto hpair = [&](auto pk0, auto pk1, f32x4 (&x)[4], f32x4 (&y)[4], const float (&rstd)[4], const float (&mr)[4], int T) {
        v2u oa[4], ob[4];
        gtile(pk0, x, rstd, mr, T, oa);
        gtile(pk1, y, rstd, mr, T + 1, ob);
#pragma unroll
        for (int tt = 0; tt < 4; ++tt) *(LAS v4u*)(L + L_H + tt * REV + (T >> 1) * 1024 + voff) = v4u{oa[tt][0], oa[tt][1], ob[tt][0], ob[tt][1]};
        SB();
      };
      auto poff = [&](int ch) { const int d = 2 * hl + ch; return pbc + (d != j ? (d * 8 + j) * 9216 + role * 4096 : (role * 32 + j) * 9216 + 8192); };
      auto pstr = [&](int ch) { return 2 * hl + ch != j ? 1024 : 8 * 9216; };
      auto ppack = [&](f32x4 x, f32x4 y) { const v2u a = pk4(x), b = pk4(y); return v4u{a[0], a[1], b[0], b[1]}; };
      auto pstore2 = [&](f32x4 (&x)[4], f32x4 (&y)[4], int ch, auto slc) {
#pragma unroll
        for (int q = 0; q < 4; ++q) {
          stc128(ppack(x[q], y[q]), rs, voff, poff(ch) + q * pstr(ch));
          if (slc.value) { SB(); __builtin_amdgcn_s_sleep(slc.value); SB(); }
        }
        SB();
      };
      constexpr std::integral_constant<int, 0> I0{};
      constexpr std::integral_constant<int, 1> I1{};
      auto base = [&](f32x4& a, f32x4& b, int ks, int tt, int u) {
        a = *(LAS f32x4*)(L + L_B2 + (u >> 2) * 128 + g * 16); b = *(LAS f32x4*)(L + L_B2 + (u >> 2) * 128 + 64 + g * 16);
        acc8(a, b, *(const LAS v4u*)(L + L_X + tt * REV + ks * 1024 + voff));
        if (u < 8) acc8(a, b, *(const LAS v4u*)(L + L_OWN + u * 1024 + voff));
      };
      auto finish = [&](auto nc, f32x4 a, f32x4 b, int ks, int tt, int u, auto&& mid) {
        constexpr int N = nc.value;
        v4u pv[N];
        const int po = pbc + j * 8 * 9216 + u * 1024;
#pragma unroll
        for (int i = 0; i < N; ++i) pv[i] = ldc(rs, voff, po + (i + ((N == 7 && i >= j) ? 1 : 0)) * 9216);
        SB();
        mid(a, b);
#pragma unroll
        for (int i = 0; i < N; ++i) acc8(a, b, pv[i]);
        const v4u o = ppack(a, b);
        if (Ly == 0) stc128(o, rs, voff, X2_OFF + c * XBC + tt * REV + ks * 1024);
        else {
          u16* p = out + (size_t)(2 * (64 * (c >> 1) + 16 * tt + m) + (c & 1)) * D_ + 32 * ks + 4 * g;
          *(v2u*)p = v2u{o[0], o[1]}; *(v2u*)(p + 16) = v2u{o[2], o[3]};
        }
      };
      auto nomid = [](f32x4&, f32x4&) {};
      CST(8 + 32 * Ly);
      if (role == 0) {
        __builtin_amdgcn_s_setprio(1);
        {
          f32x4 acc[4][4];
          seg<4, 4, 0, 18, 0, 162, true>({acc[0], acc[1], acc[2], acc[3]}, L + L_X + voff, rw, mb, voff, WordHook{L, 4u});
          CST(9 + 32 * Ly);
          float rstd[4], mr[4];
          stats(rstd, mr);
          hpair(I0, I0, acc[0], acc[1], rstd, mr, 4 * hl);
          hpair(I0, I0, acc[2], acc[3], rstd, mr, 4 * hl + 2);
        }
        CST(10 + 32 * Ly);
        bar();
        CST(11 + 32 * Ly);
        {
          f32x4 acc[4][4];
          seg<4, 4, 0, 8, 72, 162, true>({acc[0], acc[1], acc[2], acc[3]}, L + L_H + voff, rw, mb, voff);
          CST(12 + 32 * Ly);
          bar();
          CST(13 + 32 * Ly);
          seg<2, 4, 8, 18, 104, 162, false>({acc[0], acc[1]}, L + L_H + voff, rw, mb, voff);
          pstore2(acc[0], acc[1], 0, std::integral_constant<int, TRICKLE>{});
          CST(20 + 32 * Ly);
          seg<2, 4, 8, 18, 124, 162, false>({acc[2], acc[3]}, L + L_H + voff, rw, mb, voff);
          __builtin_amdgcn_s_setprio(0);
          pstore2(acc[2], acc[3], 1, std::integral_constant<int, TRICKLE>{});
          CST(14 + 32 * Ly);
        }
        arrive<4>(ws, L, 0, (5 * Ly + 2) * 32 + c, j, tag, lane);
        CST(15 + 32 * Ly);
        lds_wait_ge<3>(L, 4 + hl, Ly + 1);
        CST(18 + 32 * Ly);
        f32x4 own[4];
        seg<1, 4, 0, 18, 144, 162, true>({own}, L + L_H + voff, rw, mb, voff);
#pragma unroll
        for (int tt = 0; tt < 4; ++tt) *(LAS v2u*)(L + L_OWN + ((hl >> 1) * 4 + tt) * 1024 + voff + 8 * (hl & 1)) = pk4(own[tt]);
        asm volatile("s_waitcnt lgkmcnt(0)" ::: "memory");
        if (lane == 0) __hip_atomic_fetch_add((LAS unsigned*)(L + L_W + 44), 1u, __ATOMIC_RELAXED, __HIP_MEMORY_SCOPE_WORKGROUP);
        CST(17 + 32 * Ly);
        lds_wait_ge<1>(L, 2, Ly + 1);
        CST(16 + 32 * Ly);
        f32x4 sa, sb;
        finish(std::integral_constant<int, 7>{}, sa, sb, 2 * j, hl, hl,
               [&](f32x4& a, f32x4& b) { lds_wait_ge<1>(L, 11, 4 * (Ly + 1)); base(a, b, 2 * j, hl, hl); });
        if (hl == 3) {
          base(sa, sb, 16 + (j >> 2), j & 3, 8);
          if (j >= 4) lds_wait_ge<1>(L, 3, Ly + 1);
          finish(std::integral_constant<int, 8>{}, sa, sb, 16 + (j >> 2), j & 3, 8, nomid);
        }
      }
      int role2 = role;
      asm volatile("" : "+s"(role2));
      if (role2 != 0) {
        {
          fillh<1>(rw, mb, voff);
          if (Ly == 0) {
            if (hl == 0) poll<2>(flagp(ws, 336 + j, lane & 31), tag);
            else {
              const int o4 = ((hl - 1) * 48 + min(lane, 47)) * 16;
              v4u v[18];
#pragma unroll
              for (int s = 0; s < 18; ++s) v[s] = ldc(rs, o4, CSP_OFF + (j * 18 + s) * 2304);
              SB();
              f32x4 a = __builtin_bit_cast(f32x4, v[0]);
#pragma unroll
              for (int s = 1; s < 18; ++s)
#pragma unroll
                for (int r = 0; r < 4; ++r) { const unsigned u = v[s][r]; a[r] = sadd(a[r], __uint_as_float(u)); }
              *(LAS f32x4*)(L + L_CS1 + o4) = a;
            }
            asm volatile("s_waitcnt lgkmcnt(0)" ::: "memory");
            if (lane == 0) __hip_atomic_fetch_add((LAS unsigned*)(L + L_W + 40), 1u, __ATOMIC_RELAXED, __HIP_MEMORY_SCOPE_WORKGROUP);
            lds_wait_ge<2>(L, 10, 4u);
          }
          float rstd[4], mr[4];
          stats(rstd, mr);
          f32x4 acc[5][4];
          seg<5, 4, 0, 18, 0, 162, true>({acc[0], acc[1], acc[2], acc[3], acc[4]}, L + L_X + voff, rw, mb, voff);
          CST(9 + 32 * Ly);
          bar();
          CST(11 + 32 * Ly);
          hpair(I0, std::integral_constant<int, G2PK>{}, acc[0], acc[1], rstd, mr, 16 + 2 * hl);
          CST(10 + 32 * Ly);
          hpair(I1, I1, acc[2], acc[3], rstd, mr, 24 + 2 * hl);
          v2u o[4];
          gtile(I1, acc[4], rstd, mr, 32 + hl, o);
#pragma unroll
          for (int tt = 0; tt < 4; ++tt) *(LAS v2u*)(L + L_H + tt * REV + (16 + (hl >> 1)) * 1024 + voff + 8 * (hl & 1)) = o[tt];
          CST(12 + 32 * Ly);
        }
        bar();
        CST(13 + 32 * Ly);
        {
          f32x4 acc[2][4];
          seg<2, 4, 0, 18, 90, 162, true>({acc[0], acc[1]}, L + L_H + voff, rw, mb, voff);
          CST(20 + 32 * Ly);
          __builtin_amdgcn_s_setprio(1);
          StoreHook hk{{ppack(acc[0][0], acc[1][0]), ppack(acc[0][1], acc[1][1]), ppack(acc[0][2], acc[1][2]), ppack(acc[0][3], acc[1][3])}, rs, voff, poff(0), pstr(0)};
          seg<2, 4, 0, 18, 126, 162, true>({acc[0], acc[1]}, L + L_H + voff, rw, mb, voff, hk);
          *(volatile LAS unsigned*)(L + L_W + 16 + hl * 4) = Ly + 1;
          CST(23 + 32 * Ly);
          pstore2(acc[0], acc[1], 1, I0);
          CST(14 + 32 * Ly);
        }
        u64 fv = 0;
        if (hl == 1) fv = __hip_atomic_load(flagp(ws, (5 * Ly + 2) * 32 + c, lane & 7), RLX_AGENT);
        arrive<4>(ws, L, 1, (5 * Ly + 3) * 32 + c, j, tag, lane);
        if (hl == 1) {
          if (!__all(fv == tag)) poll8<0>(ws, 5 * Ly + 2, c, c, tag, lane);
          *(volatile LAS unsigned*)(L + L_W + 8) = Ly + 1;
        }
        CST(15 + 32 * Ly);
        if (Ly == 0) fillh<0>(rw, wq0, voff);
        lds_wait_ge<1>(L, 11, 4 * (Ly + 1));
        f32x4 sa, sb;
        base(sa, sb, 2 * j + 1, hl, 4 + hl);
        SB();
        CST(17 + 32 * Ly);
        if (hl == 0) { poll8<0>(ws, 5 * Ly + 3, c, c, tag, lane); *(volatile LAS unsigned*)(L + L_W + 12) = Ly + 1; }
        else lds_wait_ge<1>(L, 3, Ly + 1);
        CST(16 + 32 * Ly);
        finish(std::integral_constant<int, 7>{}, sa, sb, 2 * j + 1, hl, 4 + hl, nomid);
        __builtin_amdgcn_s_setprio(0);
      }
      CST(22 + 32 * Ly);
      { const int tid = threadIdx.x ^ 256; STAMP(12 + 16 * Ly); }
      if (Ly == 0) {
        const int cprev = c >= 2 ? c - 2 : c;
        arrive<8>(ws, L, 14, 4 * 32 + c, j, tag, lane);
        CST(19);
        if (role == 0) { if (FV & 1) fillh<0>(rw, wq0, voff); lds_wait_ge<2>(L, 14, 8); if (!(FV & 1)) fillh<0>(rw, wq0, voff); }
        if (role || !(FV & 2)) fillh<1>(rw, wq0, voff);
        CST(21);
        await(ws, 4, c, cprev, tag, w, lane);
        CST(31);
        STAMP(17);
        stage<true>(rs, X2_OFF + c * XBC, X2_OFF + cprev * XBC + 3 * REV, L, w, lane);
        if (role == 0 && (FV & 2)) fillh<1>(rw, wq0, voff);
        bar();
        qkv(std::integral_constant<int, 1>{});
      }
    }
  }
}

extern "C" void kernel_launch(void* const* d_in, const int* in_sizes, int n_in, void* d_out, int out_size, void* d_ws,
                              size_t ws_size, hipStream_t stream) {
  const u16* x = (const u16*)d_in[0];
  const u16* wqkv = (const u16*)d_in[1];
  const u16* wo = (const u16*)d_in[2];
  const u16* w1 = (const u16*)d_in[4];
  const u16* b1 = (const u16*)d_in[5];
  const u16* w2 = (const u16*)d_in[6];
  const u16* b2 = (const u16*)d_in[7];
  (void)hipFuncSetAttribute((const void*)k_main, hipFuncAttributeMaxDynamicSharedMemorySize, LDS_BYTES);
  k_main<<<256, 512, LDS_BYTES, stream>>>((char*)d_ws, x, wqkv, wo, w1, b1, w2, b2, (u16*)d_out);
}
```

```cpp
#include <hip/hip_runtime.h>
#include <hip/hip_bf16.h>
#include <type_traits>

#define GAS __attribute__((address_space(1)))
#define LAS __attribute__((address_space(3)))
typedef unsigned short u16;
typedef unsigned v4u __attribute__((ext_vector_type(4)));
typedef unsigned v2u __attribute__((ext_vector_type(2)));
typedef float f32x4 __attribute__((ext_vector_type(4)));
typedef float f32x2 __attribute__((ext_vector_type(2)));
typedef short bf16x8 __attribute__((ext_vector_type(8)));
typedef __bf16 bf16x2 __attribute__((ext_vector_type(2)));
typedef GAS unsigned gu32;
typedef unsigned long long u64;
typedef GAS u64 gu64;
typedef __amdgpu_buffer_rsrc_t rsrc_t;
#define RLX_AGENT __ATOMIC_RELAXED, __HIP_MEMORY_SCOPE_AGENT

#define TRICKLE 4
#define G2PK 0
#define GOS 8
#define JR1 1
#define PV 3
#define PVS 1
#define GSPREAD 2

constexpr int S_ = 2048, D_ = 576, F_ = 4608;
constexpr int REV = 18 * 1024;
constexpr int XBC = 4 * REV;
constexpr int XBSZ = 32 * XBC;
constexpr int FL_OFF = 0;
constexpr int WQ_OFF = 131072;
constexpr int WO_OFF = WQ_OFF + 128 * REV;
constexpr int W1_OFF = WO_OFF + 36 * REV;
constexpr int W2_OFF = W1_OFF + 288 * REV;
constexpr int CSP_OFF = W2_OFF + 288 * REV;
constexpr int X2_OFF = CSP_OFF + 8 * 18 * 2304 + 4096 - (8 * 18 * 2304) % 4096;
constexpr int OB_OFF = X2_OFF + XBSZ;
constexpr int X1_OFF = OB_OFF + XBSZ;
constexpr int PB_OFF = X1_OFF + XBSZ;
constexpr int PBSZ = 32 * 8 * 8 * 9216;
constexpr int ST_OFF = PB_OFF + PBSZ;
constexpr int ST2_OFF = ST_OFF + 256 * 64 * 8;
constexpr int WS_BYTES = ST2_OFF + 2 * 256 * 8 * 64 * 8;
constexpr int L_X = 0, L_H = 73728, L_ATT = 92160, ATT_STR = 10240, A_Q = 0, A_K = 2560, A_V = 5120, A_E = 7680;
constexpr int L_RES = 137216, L_OWN = 147456, L_CS1 = L_OWN + 8192, L_B1 = L_CS1 + 2304, L_SP = L_B1 + 2304,
              L_SH = L_SP + 2048, L_B2 = L_SH + 768, L_W = L_B2 + 384, L_END = L_W + 128;
static_assert(L_END <= 163840, "lds");
constexpr int LDS_BYTES = 163840;
constexpr int CODE_SHARE = 1920;

__device__ __forceinline__ float bflo(unsigned u) { return __uint_as_float(u << 16); }
__device__ __forceinline__ float bfhi(unsigned u) { return __uint_as_float(u & 0xffff0000u); }
__device__ __forceinline__ unsigned pk2(float a, float b) {
  f32x2 v = {a, b};
  return __builtin_bit_cast(unsigned, __builtin_convertvector(v, bf16x2));
}
__device__ __forceinline__ v2u pk4(f32x4 v) { return v2u{pk2(v[0], v[1]), pk2(v[2], v[3])}; }
__device__ __forceinline__ f32x4 unpk4(unsigned a, unsigned b) { return f32x4{bflo(a), bfhi(a), bflo(b), bfhi(b)}; }
__device__ __forceinline__ f32x4 mfma(v4u a, v4u b, f32x4 c) {
  return __builtin_amdgcn_mfma_f32_16x16x32_bf16(__builtin_bit_cast(bf16x8, a), __builtin_bit_cast(bf16x8, b), c, 0, 0, 0);
}
__device__ __forceinline__ v4u ldc(rsrc_t r, int voff, int soff) { return __builtin_amdgcn_raw_buffer_load_b128(r, voff, soff, 16); }
__device__ __forceinline__ void stc128(v4u v, rsrc_t r, int voff, int soff) {
  __builtin_amdgcn_raw_buffer_store_b128(v, r, voff, soff, 16);
  asm volatile("s_nop 1" :: "v"(v));
}
__device__ __forceinline__ v2u ldc64(rsrc_t r, int voff, int soff) { return __builtin_amdgcn_raw_buffer_load_b64(r, voff, soff, 16); }
__device__ __forceinline__ void stc64(v2u v, rsrc_t r, int voff, int soff) { __builtin_amdgcn_raw_buffer_store_b64(v, r, voff, soff, 16); }
__device__ __forceinline__ void stc32(unsigned v, rsrc_t r, int voff, int soff) { __builtin_amdgcn_raw_buffer_store_b32(v, r, voff, soff, 16); }
#define SB() __builtin_amdgcn_sched_barrier(0)
__device__ __forceinline__ void bar() {
  asm volatile("s_waitcnt lgkmcnt(0)" ::: "memory");
  __builtin_amdgcn_s_barrier();
  asm volatile("" ::: "memory");
}
__device__ __forceinline__ float red16max(float x) {
  auto r = __builtin_amdgcn_permlane16_swap(__float_as_uint(x), __float_as_uint(x), false, false);
  x = fmaxf(__uint_as_float(r[0]), __uint_as_float(r[1]));
  auto q = __builtin_amdgcn_permlane32_swap(__float_as_uint(x), __float_as_uint(x), false, false);
  return fmaxf(__uint_as_float(q[0]), __uint_as_float(q[1]));
}
__device__ __forceinline__ float red16sum(float x) {
  auto r = __builtin_amdgcn_permlane16_swap(__float_as_uint(x), __float_as_uint(x), false, false);
  x = __uint_as_float(r[0]) + __uint_as_float(r[1]);
  auto q = __builtin_amdgcn_permlane32_swap(__float_as_uint(x), __float_as_uint(x), false, false);
  return __uint_as_float(q[0]) + __uint_as_float(q[1]);
}

#define PST(i) do {} while (0)
#define PSTOUT() do {} while (0)
#define STAMP(i) do {} while (0)
#define CST(i) do {} while (0)

__device__ __forceinline__ gu64* flagp(char* ws, int line, int i) { return (gu64*)(ws + FL_OFF + 256 + line * 256 + i * 8); }
__device__ __forceinline__ void publish(char* ws, int line, int i, u64 tag, int tid) {
  asm volatile("s_waitcnt vmcnt(0)" ::: "memory");
  bar();
  if (tid == 0) __hip_atomic_store(flagp(ws, line, i), tag, RLX_AGENT);
}
template <int SL> __device__ __forceinline__ void poll(gu64* p, u64 tag) {
#pragma unroll 1
  for (unsigned spins = 0; spins < (1u << 22); ++spins) {
    const u64 v = __hip_atomic_load(p, RLX_AGENT);
    if (__all(v == tag)) break;
    if (SL) __builtin_amdgcn_s_sleep(SL);
  }
  __builtin_amdgcn_fence(__ATOMIC_ACQUIRE, "wavefront");
}
template <int SL> __device__ __forceinline__ void poll8(char* ws, int ph, int c, int c2, u64 tag, int lane) {
  poll<SL>(flagp(ws, ph * 32 + ((lane & 8) ? c2 : c), lane & 7), tag);
}
__device__ __forceinline__ void await(char* ws, int ph, int c, int c2, u64 tag, int w, int lane) {
  if (w == 0) poll8<0>(ws, ph, c, c2, tag, lane);
  bar();
}

__device__ __forceinline__ float sfma(float a, float b, float c) { float d; asm("v_fma_f32 %0, %1, %2, %3" : "=v"(d) : "v"(a), "v"(b), "v"(c)); return d; }
__device__ __forceinline__ float sfnma(float a, float b, float c) { float d; asm("v_fma_f32 %0, -%1, %2, %3" : "=v"(d) : "v"(a), "v"(b), "v"(c)); return d; }
__device__ __forceinline__ float sfmas(float a, float s) { float d; asm("v_fma_f32 %0, %1, %2, %2" : "=v"(d) : "v"(a), "s"(s)); return d; }
__device__ __forceinline__ float ssqs(float a, float s) { float d; asm("v_fma_f32 %0, %1, %1, %2" : "=v"(d) : "v"(a), "s"(s)); return d; }
__device__ __forceinline__ float smul(float a, float b) { float d; asm("v_mul_f32 %0, %1, %2" : "=v"(d) : "v"(a), "v"(b)); return d; }
__device__ __forceinline__ float sadd(float a, float b) { float d; asm("v_add_f32 %0, %1, %2" : "=v"(d) : "v"(a), "v"(b)); return d; }
__device__ __forceinline__ void stat2(v4u x, float& s1, float& s2) {
  const bf16x2 ones = __builtin_bit_cast(bf16x2, 0x3f803f80u);
#pragma unroll
  for (int i = 0; i < 4; ++i) {
    const unsigned u = x[i];
    const bf16x2 v = __builtin_bit_cast(bf16x2, u);
    s1 = __builtin_amdgcn_fdot2_f32_bf16(v, ones, s1, false);
    s2 = __builtin_amdgcn_fdot2_f32_bf16(v, v, s2, false);
  }
}
__device__ __forceinline__ void stat_fin(float s1, float s2, float scale, float& rstd, float& mr) {
  const float inv = 1.0f / D_, mean = smul(s1, inv);
  const float var = fmaxf(sfnma(mean, mean, smul(s2, inv)), 0.f);
  float r = __builtin_amdgcn_rsqf(sadd(var, 1e-5f));
  asm volatile("s_nop 0" : "+v"(r));
  rstd = smul(r, scale);
  mr = smul(mean, rstd);
}
__device__ __forceinline__ void stat_own(const LAS char* L, int tt, int m, float scale, float& rstd, float& mr) {
  const f32x4 p = *(const LAS f32x4*)(L + L_SP + tt * 256 + m * 16);
  stat_fin(sadd(p[0], p[2]), sadd(p[1], p[3]), scale, rstd, mr);
}
__device__ __forceinline__ void stat_own4(const LAS char* L, int tt, int m, float scale, float& rstd, float& mr) {
  const f32x4 p = *(const LAS f32x4*)(L + L_SP + tt * 256 + m * 16), q = *(const LAS f32x4*)(L + L_SP + 1024 + tt * 256 + m * 16);
  stat_fin(sadd(sadd(p[0], p[2]), sadd(q[0], q[2])), sadd(sadd(p[1], p[3]), sadd(q[1], q[3])), scale, rstd, mr);
}
__device__ __forceinline__ void stat_halo(const LAS char* L, int m, float& rstd, float& mr) {
  const f32x4 p = *(const LAS f32x4*)(L + L_SH + m * 48), q = *(const LAS f32x4*)(L + L_SH + m * 48 + 16), r = *(const LAS f32x4*)(L + L_SH + m * 48 + 32);
  stat_fin(sadd(sadd(sadd(p[0], p[2]), sadd(q[0], q[2])), sadd(r[0], r[2])), sadd(sadd(sadd(p[1], p[3]), sadd(q[1], q[3])), sadd(r[1], r[3])), 1.0f, rstd, mr);
}
__device__ __forceinline__ void ones_row(LAS char* L, int lane) {
  if ((lane & 15) == 0) {
#pragma unroll
    for (int s = 0; s < 18; ++s) *(LAS v4u*)(L + L_X + s * 1024 + lane * 16) = v4u{0x3f803f80u, 0x3f803f80u, 0x3f803f80u, 0x3f803f80u};
  }
}
template <int CTRL> __device__ __forceinline__ float dppadd(float x) {
  return x + __uint_as_float(__builtin_amdgcn_update_dpp(0u, __float_as_uint(x), CTRL, 0xf, 0xf, true));
}
__device__ __forceinline__ float red8sum(float x) { return dppadd<0x141>(dppadd<0x4E>(dppadd<0xB1>(x))); }
template <bool HALO> __device__ __forceinline__ void stage(rsrc_t rs, int xo, int ho, LAS char* L, int w, int lane) {
  const int tt = w & 3, kh = w >> 2, voff = lane * 16, hw = min(w, 5);
  v4u x[9], h[3];
#pragma unroll
  for (int s = 0; s < 9; ++s) x[s] = ldc(rs, voff + (s & 3) * 1024, xo + tt * REV + kh * 9216 + (s >> 2) * 4096);
  if (HALO) {
#pragma unroll
    for (int s = 0; s < 3; ++s) h[s] = ldc(rs, voff + s * 1024, ho + hw * 3072);
  }
  SB();
  float s1[2] = {0.f, 0.f}, s2[2] = {0.f, 0.f};
#pragma unroll
  for (int s = 0; s < 9; ++s) {
    *(LAS v4u*)(L + L_X + (tt + (HALO ? 1 : 0)) * REV + kh * 9216 + s * 1024 + voff) = x[s];
    stat2(x[s], s1[s & 1], s2[s & 1]);
    SB();
  }
  float a = red16sum(s1[0] + s1[1]), b = red16sum(s2[0] + s2[1]);
  if (lane < 16) *(LAS f32x2*)(L + L_SP + tt * 256 + lane * 16 + kh * 8) = f32x2{a, b};
  if (HALO) {
    a = 0.f; b = 0.f;
    const v4u ones = {0x3f803f80u, 0x3f803f80u, 0x3f803f80u, 0x3f803f80u};
#pragma unroll
    for (int s = 0; s < 3; ++s) {
      *(LAS v4u*)(L + L_X + hw * 3072 + s * 1024 + voff) = (lane & 15) ? h[s] : ones;
      stat2(h[s], a, b);
    }
    a = red16sum(a); b = red16sum(b);
    if (lane < 16) *(LAS f32x2*)(L + L_SH + lane * 48 + hw * 8) = f32x2{a, b};
  }
}

__device__ __forceinline__ void stage8(rsrc_t rs, int xo, LAS char* L, int j, int w, int lane) {
  const int tt = w & 3, kh = w >> 2, voff = lane * 16;
  v4u x[8];
#pragma unroll
  for (int i = 0; i < 4; ++i) {
    const int q = 2 + 8 * kh + 2 * i, ks = q < 16 ? (q + 2 * j) & 15 : q;
    x[2 * i] = ldc(rs, voff, xo + tt * REV + ks * 1024);
    x[2 * i + 1] = ldc(rs, voff + 1024, xo + tt * REV + ks * 1024);
  }
  SB();
  float s1[2] = {0.f, 0.f}, s2[2] = {0.f, 0.f};
#pragma unroll
  for (int s = 0; s < 8; ++s) {
    *(LAS v4u*)(L + L_X + tt * REV + (2 + 8 * kh + s) * 1024 + voff) = x[s];
    stat2(x[s], s1[s & 1], s2[s & 1]);
    SB();
  }
  const float a = red16sum(s1[0] + s1[1]), b = red16sum(s2[0] + s2[1]);
  if (lane < 16) *(LAS f32x2*)(L + L_SP + tt * 256 + lane * 16 + kh * 8) = f32x2{a, b};
  asm volatile("s_waitcnt lgkmcnt(0)" ::: "memory");
  if (lane == 0) __hip_atomic_fetch_add((LAS unsigned*)(L + L_W + 64 + kh * 4), 1u, __ATOMIC_RELAXED, __HIP_MEMORY_SCOPE_WORKGROUP);
}

#define RING 28
constexpr int R_ = RING;
constexpr int RB = 256 - 4 * R_;
template <int SLOT, int OFF> __device__ __forceinline__ void rload(v4u rs, int voff, int so) {
  asm volatile("buffer_load_dwordx4 v[%c3:%c4], %0, %1, %2 offen offset:%c5 sc1"
               :: "v"(voff), "s"(rs), "s"(so), "i"(RB + 4 * SLOT), "i"(RB + 4 * SLOT + 3), "i"(OFF));
}
template <int SLOT, int OFF> __device__ __forceinline__ void rput(unsigned lds) {
  asm volatile("ds_write_b128 %0, v[%c1:%c2] offset:%c3" :: "v"(lds), "i"(RB + 4 * SLOT), "i"(RB + 4 * SLOT + 3), "i"(OFF) : "memory");
}
template <int N> __device__ __forceinline__ void rwait() { asm volatile("s_waitcnt vmcnt(%c0)" :: "i"(N)); }
template <int SLOT> __device__ __forceinline__ void rmfma(f32x4& acc, v4u x) {
  asm volatile("v_mfma_f32_16x16x32_bf16 %0, v[%c2:%c3], %1, %0" : "+v"(acc) : "v"(x), "i"(RB + 4 * SLOT), "i"(RB + 4 * SLOT + 3));
}
template <int SLOT> __device__ __forceinline__ void rmfma0(f32x4& acc, v4u x) {
  asm volatile("v_mfma_f32_16x16x32_bf16 %0, v[%c2:%c3], %1, 0" : "=&v"(acc) : "v"(x), "i"(RB + 4 * SLOT), "i"(RB + 4 * SLOT + 3));
}
template <int I, int N, class Fn> __device__ __forceinline__ void sfor(Fn&& f) {
  if constexpr (I < N) { f(std::integral_constant<int, I>{}); sfor<I + 1, N>(f); }
}
constexpr int cmin(int a, int b) { return a < b ? a : b; }
struct StreamQ { int b[1]; static constexpr int id(int) { return 0; } static constexpr int kb(int P) { return P; } };
struct StreamF {
  int b[4];
  static constexpr int id(int P) { return P < 72 ? 0 : P < 104 ? 1 + ((P - 72) % 4) / 2 : P < 144 ? 1 + (P - 104) / 20 : 3; }
  static constexpr int kb(int P) { return P < 72 ? (P % 4) * 18 + P / 4 : P < 104 ? (P % 2) * 18 + (P - 72) / 4 : P < 144 ? (P % 2) * 18 + 8 + ((P - 104) % 20) / 2 : P - 144; }
};
struct StreamS {
  int b[4];
  static constexpr int id(int P) { return P < 90 ? (P % 5 < 4 ? 0 : 3) : 1 + (P - 90) / 36; }
  static constexpr int kb(int P) { return P < 90 ? (P % 5 < 4 ? ((P % 5) % 2 + 8 * ((P % 5) / 2)) * 18 : 0) + P / 5 : (P % 2) * 18 + ((P - 90) % 36) / 2; }
};
template <int P, class St> __device__ __forceinline__ void sload(v4u r, const St& st, int voff) {
  rload<P % R_, (St::kb(P) & 3) * 1024>(r, voff, st.b[St::id(P)] + (St::kb(P) >> 2) * 4096);
}
template <int P0, int N, class St> __device__ __forceinline__ void fillr(v4u r, const St& st, int voff) {
  sfor<P0, P0 + N>([&](auto pc) { sload<pc.value>(r, st, voff); });
  SB();
}
template <int P, int TOTAL, class St> __device__ __forceinline__ void refill(v4u r, const St& st, int voff) {
  if constexpr (P + R_ < TOTAL) sload<P + R_>(r, st, voff);
}
__device__ __forceinline__ StreamQ mkQ(int j, int hl, int role) { return StreamQ{{WQ_OFF + ((4 * j + hl) * 4 + 2 * role) * REV}}; }
__device__ __forceinline__ int w2pair(int j, int d, int role) { return W2_OFF + (36 * j + (d != j ? 4 * d : 32) + 2 * role) * REV; }
__device__ __forceinline__ StreamF mkF(int j, int hl) { return StreamF{{W1_OFF + (36 * j + 4 * hl) * REV, w2pair(j, 2 * hl, 0), w2pair(j, 2 * hl + 1, 0), W2_OFF + (40 * j + hl) * REV}}; }
__device__ __forceinline__ StreamS mkS(int j, int hl) { return StreamS{{W1_OFF + (36 * j + 16 + 2 * hl) * REV, w2pair(j, 2 * hl, 1), w2pair(j, 2 * hl + 1, 1), W1_OFF + (36 * j + 32 + hl) * REV}}; }
struct NoHook { template <int S> static constexpr int extra() { return 0; } template <int S> __device__ __forceinline__ void at() {} };
template <int NT, int NK, int S0, int S1, int P0, int TOTAL, bool INIT, int I0, int NKL, class St, class Hook>
__device__ __forceinline__ void segx(f32x4* const (&acc)[NT], const LAS char* const (&xb)[NK], v4u r, const St& mb, int voff, Hook& hook) {
  constexpr int XD = NT >= 4 ? 1 : (NT == 1 ? 4 : 2);
  v4u xf[XD][NK];
#pragma unroll
  for (int d = 0; d < XD; ++d)
#pragma unroll
    for (int tt = 0; tt < NK; ++tt)
      if (S0 + d < S1) xf[d][tt] = *(const LAS v4u*)(xb[tt] + (S0 + d) * 1024);
  SB();
  asm volatile("s_nop 1");
  sfor<S0, S1>([&](auto sc) {
    constexpr int s = sc.value, p = P0 + (s - S0) * NT, d = (s - S0) % XD;
    constexpr int RS = (R_ - I0 + 3) / 4;
    constexpr int lag = RS > 1 ? RS : 1;
    constexpr int k = s - S0;
    constexpr int issued = k < RS ? I0 + 4 * k : cmin(TOTAL, R_ + (k < lag ? P0 : p - lag * NT));
    rwait<issued - (p + NT) + Hook::template extra<s>()>();
    if constexpr (k < RS) sfor<I0 + 4 * k, cmin(R_, I0 + 4 * k + 4)>([&](auto qc) { sload<qc.value>(r, mb, voff); });
    hook.template at<s>();
    sfor<0, NK>([&](auto kc) {
      constexpr int tt = kc.value;
      sfor<0, NT>([&](auto tc) {
        constexpr int t = tc.value;
        if constexpr (t == NT - 1 && tt >= NKL) {}
        else if constexpr (INIT && s == S0) rmfma0<(p + t) % R_>(acc[t][tt], xf[d][tt]);
        else rmfma<(p + t) % R_>(acc[t][tt], xf[d][tt]);
        constexpr int idx = tt * NT + t;
        if constexpr (k >= lag && idx % NK == 0 && idx / NK < NT) refill<p - lag * NT + idx / NK, TOTAL>(r, mb, voff);
      });
      SB();
      if constexpr (s + XD < S1) { xf[d][tt] = *(const LAS v4u*)(xb[tt] + (s + XD) * 1024); SB(); }
    });
  });
  static_assert(I0 == R_ || P0 + (S1 - S0 - (R_ - I0 + 3) / 4) * NT + R_ >= TOTAL, "ramp: all refills are out inside the loop");
  sfor<0, NT>([&](auto tc) { refill<P0 + (S1 - 1 - S0) * NT + tc.value, TOTAL>(r, mb, voff); });
  asm volatile("s_nop 7\n\ts_nop 1");
  SB();
}
template <int NT, int NK, int S0, int S1, int P0, int TOTAL, bool INIT, class St, class Hook = NoHook>
__device__ __forceinline__ void seg(f32x4* const (&acc)[NT], const LAS char* xb, v4u r, const St& mb, int voff, Hook hook = Hook{}) {
  const LAS char* x2 = xb + 2 * REV;
  asm volatile("" : "+v"(x2));
  segx<NT, 4, S0, S1, P0, TOTAL, INIT, R_, 4>(acc, {xb, xb + REV, x2, x2 + REV}, r, mb, voff, hook);
}
template <int LO, class St> __device__ __forceinline__ void fillh(v4u r, const St& st, int voff) { fillr<LO ? R_ / 2 : 0, R_ / 2>(r, st, voff); }
__device__ __forceinline__ v2u trread(const LAS char* p) {
  typedef short s4 __attribute__((ext_vector_type(4)));
  return __builtin_bit_cast(v2u, __builtin_amdgcn_ds_read_tr16_b64_v4i16((LAS s4*)p));
}
constexpr float AL = 0.46866869f, KK = 2.3022082f / AL;
#define EACH16 _Pragma("unroll") for (int tt = 0; tt < 4; ++tt) _Pragma("unroll") for (int r = 0; r < 4; ++r)
__device__ __forceinline__ void gelu16(f32x4 (&a)[4], const float (&rstd)[4], const float (&mr)[4], f32x4 cs, f32x4 bb, v2u (&o)[4]) {
  float e[4][4], u[4][4];
  EACH16 e[tt][r] = sfnma(cs[r], mr[tt], bb[r]);
  SB();
  EACH16 u[tt][r] = sfma(a[tt][r], rstd[tt], e[tt][r]);
  SB();
  EACH16 e[tt][r] = ssqs(u[tt][r], KK);
  SB();
  EACH16 e[tt][r] = smul(e[tt][r], u[tt][r]);
  SB();
  EACH16 e[tt][r] = __builtin_amdgcn_exp2f(-e[tt][r]);
  SB();
  EACH16 e[tt][r] = sfmas(e[tt][r], AL);
  SB();
  EACH16 e[tt][r] = __builtin_amdgcn_rcpf(e[tt][r]);
  SB();
  EACH16 u[tt][r] = smul(u[tt][r], e[tt][r]);
  SB();
#pragma unroll
  for (int tt = 0; tt < 4; ++tt) o[tt] = v2u{pk2(u[tt][0], u[tt][1]), pk2(u[tt][2], u[tt][3])};
  SB();
}
__device__ __forceinline__ void gelu16p(f32x4 (&a)[4], const float (&rstd)[4], const float (&mr)[4], f32x4 cs, f32x4 bb, v2u (&o)[4]) {
  f32x4 e[4];
#pragma unroll
  for (int tt = 0; tt < 4; ++tt) e[tt] = bb - cs * mr[tt];
  SB();
#pragma unroll
  for (int tt = 0; tt < 4; ++tt) a[tt] = a[tt] * rstd[tt] + e[tt];
  SB();
#pragma unroll
  for (int tt = 0; tt < 4; ++tt) e[tt] = a[tt] * a[tt] + KK;
  SB();
#pragma unroll
  for (int tt = 0; tt < 4; ++tt) e[tt] = e[tt] * a[tt];
  SB();
  EACH16 e[tt][r] = __builtin_amdgcn_exp2f(-e[tt][r]);
  SB();
#pragma unroll
  for (int tt = 0; tt < 4; ++tt) e[tt] = e[tt] * AL + AL;
  SB();
  EACH16 e[tt][r] = __builtin_amdgcn_rcpf(e[tt][r]);
  SB();
#pragma unroll
  for (int tt = 0; tt < 4; ++tt) a[tt] = a[tt] * e[tt];
  SB();
#pragma unroll
  for (int tt = 0; tt < 4; ++tt) o[tt] = pk4(a[tt]);
  SB();
}
struct StoreHook {
  v4u d[4]; rsrc_t rs; int voff, po0, po1;
  template <int S> static constexpr int extra() { return S <= 6 ? 0 : S <= 9 ? 1 : S <= 12 ? 2 : S <= 15 ? 3 : 4; }
  template <int S> __device__ __forceinline__ void at() {
    if constexpr (S == 6 || S == 9 || S == 12 || S == 15) {
      constexpr int q = (S - 6) / 3;
      SB(); stc128(d[q], rs, voff, po0 + q * po1); SB();
    }
  }
};
template <int SL> __device__ __forceinline__ void lds_wait_ge(LAS char* L, int word, unsigned want) {
  volatile LAS unsigned* p = (volatile LAS unsigned*)(L + L_W + word * 4);
#pragma unroll 1
  for (unsigned spins = 0; spins < (1u << 22); ++spins) {
    if (__builtin_amdgcn_readfirstlane(*p) >= want) break;
    if (SL) __builtin_amdgcn_s_sleep(SL);
  }
  asm volatile("" ::: "memory");
}
template <int N> __device__ __forceinline__ void arrive(char* ws, LAS char* L, int word, int line, int i, u64 tag, int lane) {
  asm volatile("s_waitcnt vmcnt(0)" ::: "memory");
  if (lane == 0) {
    const unsigned old = __hip_atomic_fetch_add((LAS unsigned*)(L + L_W + word * 4), 1u, __ATOMIC_RELAXED, __HIP_MEMORY_SCOPE_WORKGROUP);
    if (old % N == N - 1) __hip_atomic_store(flagp(ws, line, i), tag, RLX_AGENT);
  }
}
__device__ __forceinline__ void acc8(f32x4& a, f32x4& b, v4u p) {
  a[0] = sadd(a[0], bflo(p[0])); a[1] = sadd(a[1], bfhi(p[0])); a[2] = sadd(a[2], bflo(p[1])); a[3] = sadd(a[3], bfhi(p[1]));
  b[0] = sadd(b[0], bflo(p[2])); b[1] = sadd(b[1], bfhi(p[2])); b[2] = sadd(b[2], bflo(p[3])); b[3] = sadd(b[3], bfhi(p[3]));
}

#define FRESH() int lane = lane0, c = c0, j = j0, w = w0; asm volatile("" : "+v"(lane)); asm volatile("" : "+s"(c), "+s"(j), "+s"(w)); \
  const int g = lane >> 4, m = lane & 15, voff = lane * 16, role = w >> 2, hl = w & 3; (void)g; (void)m; (void)voff; (void)role; (void)hl;

template <bool CS> __device__ __forceinline__ unsigned jtr(v4u* r, LAS char* scr, int lane) {
  const int g = lane >> 4, m = lane & 15;
#pragma unroll
  for (int i = 0; i < 4; ++i) *(LAS v4u*)(scr + i * 1024 + lane * 16) = r[i];
  const LAS char* p = scr + (4 * g + (m >> 2)) * 128 + (m & 3) * 8;
  float cs4[4] = {0.f, 0.f, 0.f, 0.f};
#pragma unroll
  for (int t = 0; t < 4; ++t) { const v2u lo = trread(p + t * 32), hi = trread(p + t * 32 + 2048); r[t] = v4u{lo[0], lo[1], hi[0], hi[1]}; }
  if (CS) {
#pragma unroll
    for (int t = 0; t < 4; ++t) { float dummy = 0.f; stat2(r[t], cs4[t], dummy); cs4[t] = red16sum(cs4[t]); }
  }
  return __float_as_uint(g < 2 ? (g == 0 ? cs4[0] : cs4[1]) : (g == 2 ? cs4[2] : cs4[3]));
}
struct JobHook {
  v4u f[8]; unsigned cs; rsrc_t rs; int lane, d1, d2, dc;
  template <int S> static constexpr int extra() { return S <= 9 ? 0 : S - 9; }
  template <int S> __device__ __forceinline__ void at() {
    if constexpr (S == 9) { SB(); stc32(cs, rs, lane * 4, dc); SB(); }
    if constexpr (S > 9) { SB(); stc128(f[S - 10], rs, lane * 16, (S < 14 ? d1 : d2) + ((S - 10) & 3) * REV); SB(); }
  }
};
__device__ __forceinline__ void wo_req(v4u rw, int j, int hl, int voff) {
  const int wa = WO_OFF + (4 * j + hl) * REV, wb = WO_OFF + (32 + 2 * (j >> 2)) * REV + hl * 9216;
  sfor<0, 27>([&](auto ic) { constexpr int i = ic.value, q = i < 18 ? i : i - 18; rload<i, (q & 3) * 1024>(rw, voff, (i < 18 ? wa : wb) + (q >> 2) * 4096); });
  SB();
}
struct WordHook {
  LAS char* L; unsigned want; int hl;
  template <int S> static constexpr int extra() { return 0; }
  template <int S> __device__ __forceinline__ void at() {
    if constexpr (S == 9) { SB(); lds_wait_ge<1>(L, 17, want); SB(); }
    if constexpr (S == GOS) { SB(); *(volatile LAS unsigned*)(L + L_W + 80 + hl * 4) = want; SB(); }
  }
};

__global__ __launch_bounds__(512, 2) __attribute__((amdgpu_num_vgpr(RB / 2 - 1)))
void k_main(char* __restrict__ ws, const u16* __restrict__ xin, const u16* __restrict__ wqkv, const u16* __restrict__ wo,
            const u16* __restrict__ w1, const u16* __restrict__ b1, const u16* __restrict__ w2, const u16* __restrict__ b2,
            u16* __restrict__ out) {
  extern __shared__ __attribute__((aligned(16))) char smem[];
  {
    u64 pc;
    asm volatile("s_getpc_b64 %0\n\tglobal_load_dword v%c2, %1, %0" : "=&s"(pc)
                 : "v"(((blockIdx.x >> 3) + min((int)threadIdx.x, CODE_SHARE / 128 - 1) * 32) * 128), "i"(RB - 1));
    SB();
  }
  LAS char* L = (LAS char*)smem;
  const int tid = threadIdx.x, lane0 = tid & 63, w0 = __builtin_amdgcn_readfirstlane(tid >> 6);
  int c0, j0;
  {
    constexpr int G = GSPREAD, per = 8 / G;
    const int b = blockIdx.x, x = b & 7, r = b >> 3;
    j0 = (x % G) * per + (r % per);
    c0 = (x / G) * (4 * G) + r / per;
  }
  const rsrc_t rs = __builtin_amdgcn_make_buffer_rsrc((void*)ws, 0, WS_BYTES, 0x00020000);
  const v4u rw = {(unsigned)(size_t)ws, (unsigned)((size_t)ws >> 32) & 0xffffu, (unsigned)WS_BYTES, 0x00020000u};
  asm volatile("" ::: "v255");
  PST(0);
  u64 tag = 0;
  auto qkv = [&](auto lc, auto& jh) {
    constexpr int Ly = lc.value;
    if (Ly) STAMP(2 + 16 * Ly);
    FRESH();
    {
      const LAS char* xr = L + L_X + (hl + 1) * REV + voff;
      const v4u ra = *(const LAS v4u*)(xr + (2 * j + role) * 1024), rb = *(const LAS v4u*)(xr + (16 + (j >> 2)) * 1024);
      *(LAS v4u*)(L + L_RES + w * 1024 + voff) = ra;
      if (w == (j & 3)) *(LAS v4u*)(L + L_RES + 8192 + voff) = rb;
    }
    const StreamQ qb = mkQ(j, hl, role);
    constexpr int I0 = Ly == 0 ? 8 : R_;
    auto fix = [&](f32x4 a, const float (&cs)[4], float rstd, float mr) {
      float u[4];
#pragma unroll
      for (int r = 0; r < 4; ++r) u[r] = smul(a[r], rstd);
#pragma unroll
      for (int r = 0; r < 4; ++r) u[r] = sfnma(cs[r], mr, u[r]);
      return v2u{pk2(u[0], u[1]), pk2(u[2], u[3])};
    };
    auto bcast = [&](f32x4 a, float (&cs)[4]) {
#pragma unroll
      for (int r = 0; r < 4; ++r) cs[r] = __uint_as_float(__builtin_amdgcn_update_dpp(0u, __float_as_uint(a[r]), 0x150, 0xf, 0xf, false));
    };
    CST(0 + 32 * Ly);
    if (role == 0) {
      f32x4 a0[5], a1[5];
      const LAS char* xa = L + L_X + voff, *xc = xa + 3 * REV;
      asm volatile("" : "+v"(xc));
      float rstd[5], mr[5], cs0[4], cs1[4];
      stat_halo(L, m, rstd[0], mr[0]);
#pragma unroll
      for (int t = 1; t < 5; ++t) stat_own(L, t - 1, m, 1.0f, rstd[t], mr[t]);
      segx<2, 5, 0, 18, 0, 36, true, I0, 5>({a0, a1}, {xa, xa + REV, xa + 2 * REV, xc, xc + REV}, rw, qb, voff, jh);
      CST(1 + 32 * Ly);
      LAS char* A = L + L_ATT + hl * ATT_STR + lane * 8;
      bcast(a0[0], cs0); bcast(a1[0], cs1);
      SB();
#pragma unroll
      for (int t = 0; t < 5; ++t) {
        if (t) *(LAS v2u*)(A + A_Q + t * 512) = fix(a0[t], cs0, rstd[t], mr[t]);
        *(LAS v2u*)(A + A_K + t * 512) = fix(a1[t], cs1, rstd[t], mr[t]);
      }
    }
    int role2 = role;
    asm volatile("" : "+s"(role2));
    if (role2) {
      f32x4 a0[5], a1[5];
      const int ta = (hl & 1) ? 3 : 1, tb = 4 - ta;
      const LAS char* x0 = L + L_X + voff, *xa = x0 + ta * REV, *xc = x0 + tb * REV;
      NoHook hk;
      __builtin_amdgcn_s_setprio(1);
      if constexpr (JR1) segx<2, 5, 0, 18, 0, 36, true, I0, 3>({a0, a1}, {x0, xa, xa + REV, xc, xc + REV}, rw, qb, voff, jh);
      else segx<2, 5, 0, 18, 0, 36, true, I0, 3>({a0, a1}, {x0, xa, xa + REV, xc, xc + REV}, rw, qb, voff, hk);
      __builtin_amdgcn_s_setprio(0);
      CST(1 + 32 * Ly);
      LAS char* A = L + L_ATT + hl * ATT_STR + A_V + m * 32 + g * 8, *E = L + L_ATT + (hl & 2) * ATT_STR + A_E + m * 32 + g * 8;
      float rstd[5], mr[5], cs0[4], cs1[4];
      int to[5] = {0, ta, ta + 1, tb, tb + 1};
      stat_halo(L, m, rstd[0], mr[0]);
#pragma unroll
      for (int t = 1; t < 5; ++t) stat_own(L, to[t] - 1, m, 1.0f, rstd[t], mr[t]);
      bcast(a0[0], cs0); bcast(a1[0], cs1);
      SB();
#pragma unroll
      for (int t = 0; t < 5; ++t) {
        *(LAS v2u*)(A + to[t] * 512) = fix(a0[t], cs0, rstd[t], mr[t]);
        if (t < 3) *(LAS v2u*)(E + to[t] * 512) = fix(a1[t], cs1, rstd[t], mr[t]);
      }
      wo_req(rw, j, hl, voff);
      if constexpr (Ly == 0 && !JR1) sfor<9, 18>([&](auto sc) { jh.template at<sc.value>(); });
    }
  };
  {
    FRESH();
    const int r8 = lane >> 3, q8 = lane & 7;
    JobHook jh;
    jh.rs = rs; jh.lane = lane; jh.d1 = jh.d2 = jh.dc = WS_BYTES;
    u64 gen = 0;
    {
      asm volatile("global_load_dword v%c2, %0, %1" :: "v"(((2 * (64 * (c >> 1) + 8 * j + w) + (c & 1)) * D_ + min(lane, 8) * 64) * 2), "s"(xin), "i"(RB - 1));
    }
    auto jobs = [&](int q0) {
      const int q = min(q0, 161), s = (q * 7282) >> 16, B = q - 9 * s;
      const u16* p1 = w1 + ((32 * s + r8) * F_ + 576 * j + 64 * B + 8 * q8);
      const u16* p2 = w2 + ((576 * j + 32 * s + r8) * D_ + 64 * B + 8 * q8);
#pragma unroll
      for (int i = 0; i < 4; ++i) jh.f[i] = *(const v4u*)(p1 + i * 8 * F_);
#pragma unroll
      for (int i = 0; i < 4; ++i) jh.f[4 + i] = *(const v4u*)(p2 + i * 8 * D_);
      if (q0 < 162) {
        jh.d1 = W1_OFF + (36 * j + 4 * B) * REV + (s < 16 ? (s - 2 * j) & 15 : s) * 1024;
        jh.d2 = W2_OFF + (36 * j + 4 * B) * REV + s * 1024;
        jh.dc = CSP_OFF + (j * 18 + s) * 2304 + B * 256;
      }
      SB();
    };
    if (w >= 5) {
      if (c < 18) {
        const int sec = w - 5;
        const u16* src = wqkv + (32 * c) * (3 * D_) + 72 * j;
        LAS char* scr = L + L_ATT + 32768 + sec * 4736;
        v4u qr[5], jo[4];
#pragma unroll
        for (int i = 0; i < 5; ++i) {
          const int ch = min(64 * i + lane, 287), row = (ch * 7282) >> 16;
          qr[i] = *(const v4u*)(src + row * (3 * D_) + sec * D_ + (ch - 9 * row) * 8);
        }
        {
          typedef unsigned v4u8 __attribute__((ext_vector_type(4), aligned(8)));
          const u16* ob = sec == 2 ? src : wo;
          const int e0 = (lane >> 2) * (3 * D_) + min(lane & 3, 2) * D_ + 16;
          const int ra = 36 * c + r8, rb = 36 * (8 * (c - 16) + 2 * (r8 >> 2)) + 16 + (r8 & 3), o0 = (c < 16 ? ra : rb) * D_ + 64 * (sec ? 8 : j) + 8 * q8;
          const int v0 = sec == 2 ? e0 : o0;
          const int s1 = sec == 2 ? 16 * 3 * D_ : (c < 16 ? 8 : 144) * D_, s2 = sec == 2 ? 36 : (c < 16 ? 20 : 36) * D_;
#pragma unroll
          for (int i = 0; i < 4; ++i) jo[i] = *(const v4u8*)(ob + (v0 + (i & 1) * s1 + (i >> 1) * s2));
        }
        SB();
        gen = __hip_atomic_load((gu64*)(ws + FL_OFF), RLX_AGENT);
        SB();
        PST(1);
        bar();
#pragma unroll
        for (int i = 0; i < 5; ++i) *(LAS v4u*)(scr + min(64 * i + lane, 287) * 16) = qr[i];
        const int qd = WQ_OFF + 16 * j * REV + (sec >> 1) * 2 * REV + (2 * c + (sec & 1)) * 1024;
#pragma unroll
        for (int h = 0; h < 4; ++h) {
          const LAS char* p = scr + (4 * g + (m >> 2)) * 144 + (h >> 1) * 72 + (h & 1) * 40 + (m & 3) * 8;
          const v2u lo = trread(p), hi = trread(p + 16 * 144);
          stc128(v4u{lo[0], lo[1], hi[0], hi[1]}, rs, voff, qd + h * 4 * REV);
        }
        if (sec == 2) {
#pragma unroll
          for (int i = 0; i < 4; ++i) *(LAS v2u*)(scr + i * 512 + lane * 8) = (lane & 3) == 3 ? v2u{0u, 0u} : v2u{jo[i][0], jo[i][1]};
#pragma unroll
          for (int p = 0; p < 2; ++p) {
            const v2u lo = trread(scr + p * 1024 + lane * 8), hi = trread(scr + p * 1024 + 512 + lane * 8);
            jo[2 * p] = v4u{lo[0], lo[1], hi[0], hi[1]}; jo[2 * p + 1] = jo[2 * p];
          }
        } else jtr<false>(jo, scr, lane);
        const int od = sec == 2 ? qd + 1024 : WO_OFF + 4 * (sec ? 8 : j) * REV + c * 1024, os = sec == 2 ? 4 * REV : REV;
#pragma unroll
        for (int t = 0; t < 4; ++t) stc128(jo[t], rs, voff, od + t * os);
        PST(2);
        arrive<3>(ws, L, 9, 320 + j, c, gen + 1, lane);
        lds_wait_ge<1>(L, 9, 3u);
        PST(3);
        jobs(112 + c * 3 + sec);
      } else {
        bar();
        gen = __hip_atomic_load((gu64*)(ws + FL_OFF), RLX_AGENT);
        asm volatile("" : "+v"(gen));
        __builtin_amdgcn_s_sleep(PVS);
        jobs((c - 18) * 8 + w);
      }
    } else {
      if (tid < 32) *(LAS unsigned*)(L + L_W + tid * 4) = 0u;
      bar();
      asm volatile("s_waitcnt vmcnt(0)");
      PST(1);
      const int cc = (w == 0 && c >= 2) ? c - 2 : c, tt = w == 0 ? 3 : w - 1;
      const u16* src = xin + ((2 * (64 * (cc >> 1) + 16 * tt + r8) + (cc & 1)) * D_ + 8 * q8);
      v4u x[2][9];
#pragma unroll
      for (int i = 0; i < 9; ++i) { x[0][i] = *(const v4u*)(src + 64 * i); x[1][i] = *(const v4u*)(src + 16 * D_ + 64 * i); }
      SB();
      PST(2);
      float s1[2] = {0.f, 0.f}, s2[2] = {0.f, 0.f};
      LAS char* dst = L + L_X + w * REV + (q8 >> 2) * 1024 + ((q8 & 1) * 32 + r8) * 16 + ((q8 >> 1) & 1) * 8;
#pragma unroll
      for (int i = 0; i < 9; ++i) {
#pragma unroll
        for (int h = 0; h < 2; ++h) {
          *(LAS v2u*)(dst + i * 2048 + h * 128) = v2u{x[h][i][0], x[h][i][1]};
          *(LAS v2u*)(dst + i * 2048 + h * 128 + 256) = v2u{x[h][i][2], x[h][i][3]};
        }
        SB();
      }
      PST(3);
      if (c >= 18) jobs((c - 18) * 8 + w);
#pragma unroll
      for (int i = 0; i < 9; ++i) { stat2(x[0][i], s1[0], s2[0]); stat2(x[1][i], s1[1], s2[1]); }
      if (w == 0) ones_row(L, lane);
#pragma unroll
      for (int h = 0; h < 2; ++h) { s1[h] = red8sum(s1[h]); s2[h] = red8sum(s2[h]); }
      if (q8 < 2) {
        const f32x4 p = {q8 ? s1[1] : s1[0], q8 ? s2[1] : s2[0], 0.f, 0.f}, z = {0.f, 0.f, 0.f, 0.f};
        if (w) *(LAS f32x4*)(L + L_SP + (w - 1) * 256 + (q8 * 8 + r8) * 16) = p;
        else { LAS char* d = L + L_SH + (q8 * 8 + r8) * 48; *(LAS f32x4*)d = p; *(LAS f32x4*)(d + 16) = z; *(LAS f32x4*)(d + 32) = z; }
      }
    }
    PST(4);
    jh.cs = jtr<true>(jh.f, L + L_ATT + w * 4096, lane);
    jtr<false>(jh.f + 4, L + L_ATT + w * 4096, lane);
    if (w == 6) {
      unsigned bv[5];
#pragma unroll
      for (int i = 0; i < 5; ++i) bv[i] = *(const unsigned*)(b1 + 576 * j + min(lane + 64 * i, 287) * 2);
      const unsigned b2v = *(const unsigned*)(b2 + (lane < 32 ? 64 * j + 2 * lane : 512 + 32 * (j >> 2) + 2 * (lane & 15)));
#pragma unroll
      for (int i = 0; i < 5; ++i) *(LAS f32x2*)(L + L_B1 + min(lane + 64 * i, 287) * 8) = f32x2{AL * bflo(bv[i]), AL * bfhi(bv[i])};
      if (lane < 48) *(LAS f32x2*)(L + L_B2 + lane * 8) = f32x2{bflo(b2v), bfhi(b2v)};
    }
    if (w == 7) {
      poll<1>(flagp(ws, 320 + j, min(lane, 17)), gen + 1);
      if (lane == 0) *(LAS u64*)(L + L_W + 48) = gen + 1;
    }
    PST(5);
    bar();
    PST(6);
    fillr<0, 8>(rw, mkQ(j, hl, role), voff);
    tag = *(LAS u64*)(L + L_W + 48);
    tag = ((u64)(unsigned)__builtin_amdgcn_readfirstlane((unsigned)(tag >> 32)) << 32) | (unsigned)__builtin_amdgcn_readfirstlane((unsigned)tag);
    qkv(std::integral_constant<int, 0>{}, jh);
    PST(7);
    PSTOUT();
  }

#pragma unroll 1
  for (int Ly = 0; Ly < 2; ++Ly) {
    CST(2 + 32 * Ly);
    bar();
    CST(3 + 32 * Ly);
    STAMP(3 + 16 * Ly);
    {
      FRESH();
      const LAS char* A = L + L_ATT + 2 * role * ATT_STR + hl * 512 + lane * 8;
      const LAS char* Ee = L + L_ATT + 2 * role * ATT_STR + hl * 512 + A_E + m * 32;
      v2u qm[2], km[2][2], vm[2][2], em[2], xk[2];
#pragma unroll
      for (int q = 0; q < 2; ++q) {
        qm[q] = *(const LAS v2u*)(A + q * ATT_STR + A_Q + 512);
        xk[q] = *(const LAS v2u*)(Ee + q * 512 + 8);
#pragma unroll
        for (int i = 0; i < 2; ++i) km[q][i] = *(const LAS v2u*)(A + q * ATT_STR + A_K + i * 512);
      }
      const v2u xq = *(const LAS v2u*)(Ee + 512);
#pragma unroll
      for (int q = 0; q < 2; ++q) {
        em[q] = trread(A + A_E + q * 512);
#pragma unroll
        for (int i = 0; i < 2; ++i) vm[q][i] = trread(A + q * ATT_STR + A_V + i * 512);
      }
      const float sc = 0.23570226039551584f * 1.4426950408889634f, NEG = -3.0e38f;
      f32x4 bp, bc;
#pragma unroll
      for (int r = 0; r < 4; ++r) {
        const int dk = m - (4 * g + r);
        bp[r] = (dk <= -9 && !(c < 2 && hl == 0)) ? 0.f : NEG;
        bc[r] = (unsigned)dk <= 7u ? 0.f : NEG;
      }
      SB();
      const f32x4 z = {0.f, 0.f, 0.f, 0.f};
      f32x4 sp[2], sq[2];
#pragma unroll
      for (int q = 0; q < 2; ++q) {
        const v4u qf = {qm[q][0], qm[q][1], g == 0 ? xq[q] : 0u, 0u};
        sp[q] = mfma(v4u{km[q][0][0], km[q][0][1], xk[0][q], 0u}, qf, z);
        sq[q] = mfma(v4u{km[q][1][0], km[q][1][1], xk[1][q], 0u}, qf, z);
      }
      float mx[2], sum[2];
#pragma unroll
      for (int q = 0; q < 2; ++q) {
        sp[q] = sp[q] * sc + bp; sq[q] = sq[q] * sc + bc;
        mx[q] = fmaxf(fmaxf(fmaxf(sp[q][0], sp[q][1]), fmaxf(sp[q][2], sp[q][3])), fmaxf(fmaxf(sq[q][0], sq[q][1]), fmaxf(sq[q][2], sq[q][3])));
      }
#pragma unroll
      for (int q = 0; q < 2; ++q) mx[q] = red16max(mx[q]);
#pragma unroll
      for (int q = 0; q < 2; ++q) {
        sp[q] = sp[q] - mx[q]; sq[q] = sq[q] - mx[q];
#pragma unroll
        for (int r = 0; r < 4; ++r) { sp[q][r] = __builtin_amdgcn_exp2f(sp[q][r]); sq[q][r] = __builtin_amdgcn_exp2f(sq[q][r]); }
        const f32x4 t4 = sp[q] + sq[q];
        sum[q] = (t4[0] + t4[1]) + (t4[2] + t4[3]);
      }
#pragma unroll
      for (int q = 0; q < 2; ++q) sum[q] = __builtin_amdgcn_rcpf(red16sum(sum[q]));
      f32x4 o[2], oe[2];
#pragma unroll
      for (int q = 0; q < 2; ++q) {
        const v2u pp = pk4(sp[q] * sum[q]), pc = pk4(sq[q] * sum[q]);
        const v4u pf = {pp[0], pp[1], pc[0], pc[1]};
        o[q] = mfma(v4u{vm[q][0][0], vm[q][0][1], vm[q][1][0], vm[q][1][1]}, pf, z);
        oe[q] = mfma(v4u{em[0][0], em[0][1], em[1][0], em[1][1]}, pf, z);
      }
      const int ob = OB_OFF + c * XBC + hl * REV;
      const v2u oa = pk4(o[0]), oc = pk4(o[1]);
      stc128(v4u{oa[0], oa[1], oc[0], oc[1]}, rs, voff, ob + (2 * j + role) * 1024);
      if (g == 2) stc64(v2u{pk2(oe[0][0], oe[0][1]), pk2(oe[1][2], oe[1][3])}, rs, m * 8, ob + 16384 + (2 * j + role) * 128);
      SB();
      CST(4 + 32 * Ly);
      STAMP(4 + 16 * Ly);
      publish(ws, (5 * Ly + 0) * 32 + c, j, tag, tid);
      CST(5 + 32 * Ly);
      if (Ly == 0 && tid == 0) __hip_atomic_store(flagp(ws, 328 + j, c), tag, RLX_AGENT);
      if (role) {
        const unsigned la = (unsigned)(size_t)(L + L_X + hl * REV + voff), lb = (unsigned)(size_t)(L + L_X + 4 * REV + hl * 9216 + voff);
        sfor<0, 27>([&](auto ic) {
          constexpr int i = ic.value;
          rput<i, (i < 18 ? i : i - 18) * 1024>(i < 18 ? la : lb);
        });
      }
      if (Ly && role == 0) fillh<0>(rw, mkF(j, hl), voff);
    }
    {
      FRESH();
      const LAS char* wb = L + L_X + role * 9216 + voff;
      const LAS char* wb2 = wb + 3 * REV;
      asm volatile("" : "+v"(wb2));
      const bool ext = hl == (j & 3);
      v4u wf[2][4];
      auto wld = [&](int s) {
#pragma unroll
        for (int i = 0; i < 4; ++i) wf[s % 2][i] = *(const LAS v4u*)((i < 3 ? wb + i * REV : wb2) + s * 1024);
      };
      f32x4 acc[6];
      {
        const v4u ra = *(LAS v4u*)(L + L_RES + w * 1024 + voff), rb = *(LAS v4u*)(L + L_RES + 8192 + voff);
        acc[0] = unpk4(role ? 0u : ra[0], role ? 0u : ra[1]); acc[1] = unpk4(role ? 0u : ra[2], role ? 0u : ra[3]);
        acc[2] = unpk4(role ? ra[0] : 0u, role ? ra[1] : 0u); acc[3] = unpk4(role ? ra[2] : 0u, role ? ra[3] : 0u);
        acc[4] = unpk4(role ? 0u : rb[0], role ? 0u : rb[1]); acc[5] = unpk4(role ? 0u : rb[2], role ? 0u : rb[3]);
      }
      SB();
      CST(6 + 32 * Ly);
      await(ws, 5 * Ly + 0, c, c, tag, w, lane);
      CST(7 + 32 * Ly);
      STAMP(5 + 16 * Ly);
      v4u of[9];
      const int ob = OB_OFF + c * XBC + hl * REV + role * 9216;
#pragma unroll
      for (int s = 0; s < 7; ++s) of[s] = ldc(rs, voff + (s & 3) * 1024, ob + (s >> 2) * 4096);
      {
        const int va = role ? g * 256 + m * 8 : voff, vb = va + (role ? 128 : 8);
#pragma unroll
        for (int s = 7; s < 9; ++s) {
          const v2u lo = ldc64(rs, va + (s - 7) * 1024, ob + 7168), hi = ldc64(rs, vb + (s - 7) * 1024, ob + 7168);
          of[s] = v4u{lo[0], lo[1], hi[0], hi[1]};
        }
      }
      SB();
      wld(0); wld(1);
      SB();
#pragma unroll
      for (int s = 0; s < 9; ++s) {
#pragma unroll
        for (int i = 0; i < 4; ++i) acc[i] = mfma(wf[s % 2][i], of[s], acc[i]);
        SB();
        if (s + 2 < 9) { wld(s + 2); SB(); }
      }
      LAS char* R = L + L_X + 6 * REV + hl * 6144 + voff;
      *(LAS f32x4*)(R + role * 2048) = role ? acc[0] : acc[2];
      *(LAS f32x4*)(R + role * 2048 + 1024) = role ? acc[1] : acc[3];
      if (ext) {
#pragma unroll
        for (int s = 0; s < 9; ++s) {
          acc[4] = mfma(*(const LAS v4u*)(wb2 + REV + s * 1024), of[s], acc[4]);
          acc[5] = mfma(*(const LAS v4u*)(wb2 + 2 * REV + s * 1024), of[s], acc[5]);
        }
        if (role) { *(LAS f32x4*)(R + 4096) = acc[4]; *(LAS f32x4*)(R + 5120) = acc[5]; }
      }
      if (role == 0) {
        if (Ly == 0) { poll<1>(flagp(ws, 328 + j, lane & 31), tag); fillh<0>(rw, mkF(j, hl), voff); }
        else fillh<1>(rw, mkF(j, hl), voff);
      }
      if (Ly) CST(25 + 32);
      bar();
      {
        const f32x4 pa = *(LAS f32x4*)(R + 2048 - role * 2048), pb = *(LAS f32x4*)(R + 3072 - role * 2048);
        const v2u oa = pk4((role ? acc[2] : acc[0]) + pa), ob2 = pk4((role ? acc[3] : acc[1]) + pb);
        const int xb = X1_OFF + c * XBC + hl * REV;
        const v4u xo = {oa[0], oa[1], ob2[0], ob2[1]};
        stc128(xo, rs, voff, xb + (2 * j + role) * 1024);
        SB();
        *(LAS v4u*)(L + L_X + hl * REV + role * 1024 + voff) = xo;
        float s1 = 0.f, s2 = 0.f;
        stat2(xo, s1, s2);
        s1 = red16sum(s1); s2 = red16sum(s2);
        if (lane < 16) *(LAS f32x2*)(L + L_SP + 1024 + hl * 256 + lane * 16 + role * 8) = f32x2{s1, s2};
        if (ext && role == 0) {
          const v2u ea = pk4(acc[4] + *(LAS f32x4*)(R + 4096)), eb = pk4(acc[5] + *(LAS f32x4*)(R + 5120));
          stc128(v4u{ea[0], ea[1], eb[0], eb[1]}, rs, voff, xb + (16 + (j >> 2)) * 1024);
        }
      }
      STAMP(6 + 16 * Ly);
      if (Ly) CST(26 + 32);
      publish(ws, (5 * Ly + 1) * 32 + c, j, tag, tid);
      if (Ly) CST(27 + 32);
      if (role) {
        fillh<0>(rw, mkS(j, hl), voff);
        if (Ly == 0 && hl) {
          const int o4 = ((hl - 1) * 48 + min(lane, 47)) * 16;
          v4u v[18];
#pragma unroll
          for (int s = 0; s < 18; ++s) v[s] = ldc(rs, o4, CSP_OFF + (j * 18 + s) * 2304);
          SB();
          f32x4 a = __builtin_bit_cast(f32x4, v[0]);
#pragma unroll
          for (int s = 1; s < 18; ++s)
#pragma unroll
            for (int r = 0; r < 4; ++r) { const unsigned u = v[s][r]; a[r] = sadd(a[r], __uint_as_float(u)); }
          *(LAS f32x4*)(L + L_CS1 + o4) = a;
        }
      }
    }
    {
      FRESH();
      const int pbc = PB_OFF + c * (64 * 9216);
      const StreamQ wq0 = mkQ(j, hl, role);
      auto stats = [&](float (&rstd)[4], float (&mr)[4]) {
#pragma unroll
        for (int tt = 0; tt < 4; ++tt) stat_own4(L, tt, m, AL, rstd[tt], mr[tt]);
      };
      auto gtile = [&](auto pkc, f32x4 (&x)[4], const float (&rstd)[4], const float (&mr)[4], int T, v2u (&o)[4]) {
        const f32x4 cs = *(LAS f32x4*)(L + L_CS1 + T * 64 + g * 16), bb = *(LAS f32x4*)(L + L_B1 + T * 64 + g * 16);
        if constexpr (pkc.value) gelu16p(x, rstd, mr, cs, bb, o); else gelu16(x, rstd, mr, cs, bb, o);
      };
      auto hpair = [&](auto pk0, auto pk1, f32x4 (&x)[4], f32x4 (&y)[4], const float (&rstd)[4], const float (&mr)[4], int T, auto&& mid) {
        v2u oa[4], ob[4];
        gtile(pk0, x, rstd, mr, T, oa);
        mid();
        gtile(pk1, y, rstd, mr, T + 1, ob);
#pragma unroll
        for (int tt = 0; tt < 4; ++tt) *(LAS v4u*)(L + L_H + tt * REV + (T >> 1) * 1024 + voff) = v4u{oa[tt][0], oa[tt][1], ob[tt][0], ob[tt][1]};
        SB();
      };
      auto poff = [&](int ch) { const int d = 2 * hl + ch; return pbc + (d != j ? (d * 8 + j) * 9216 + role * 4096 : (role * 32 + j) * 9216 + 8192); };
      auto pstr = [&](int ch) { return 2 * hl + ch != j ? 1024 : 8 * 9216; };
      auto ppack = [&](f32x4 x, f32x4 y) { const v2u a = pk4(x), b = pk4(y); return v4u{a[0], a[1], b[0], b[1]}; };
      auto pstore2 = [&](f32x4 (&x)[4], f32x4 (&y)[4], int ch, auto slc) {
#pragma unroll
        for (int q = 0; q < 4; ++q) {
          stc128(ppack(x[q], y[q]), rs, voff, poff(ch) + q * pstr(ch));
          if (slc.value) { SB(); __builtin_amdgcn_s_sleep(slc.value); SB(); }
        }
        SB();
      };
      constexpr std::integral_constant<int, 0> I0{};
      constexpr std::integral_constant<int, 1> I1{};
      auto base = [&](f32x4& a, f32x4& b, int ks, int tt, int u) {
        a = *(LAS f32x4*)(L + L_B2 + (u >> 2) * 128 + g * 16); b = *(LAS f32x4*)(L + L_B2 + (u >> 2) * 128 + 64 + g * 16);
        acc8(a, b, *(const LAS v4u*)(L + L_X + tt * REV + (u < 8 ? u >> 2 : ks) * 1024 + voff));
        if (u < 8) acc8(a, b, *(const LAS v4u*)(L + L_OWN + u * 1024 + voff));
      };
      auto finish = [&](auto nc, f32x4 a, f32x4 b, int ks, int tt, int u, auto&& mid) {
        constexpr int N = nc.value;
        v4u pv[N];
        const int po = pbc + j * 8 * 9216 + u * 1024;
#pragma unroll
        for (int i = 0; i < N; ++i) pv[i] = ldc(rs, voff, po + (i + ((N == 7 && i >= j) ? 1 : 0)) * 9216);
        SB();
        mid(a, b);
#pragma unroll
        for (int i = 0; i < N; ++i) acc8(a, b, pv[i]);
        const v4u o = ppack(a, b);
        if (Ly == 0) stc128(o, rs, voff, X2_OFF + c * XBC + tt * REV + ks * 1024);
        else {
          u16* p = out + (size_t)(2 * (64 * (c >> 1) + 16 * tt + m) + (c & 1)) * D_ + 32 * ks + 4 * g;
          *(v2u*)p = v2u{o[0], o[1]}; *(v2u*)(p + 16) = v2u{o[2], o[3]};
        }
      };
      auto nomid = [](f32x4&, f32x4&) {};
      if (role == 0) {
        const StreamF mb = mkF(j, hl);
        __builtin_amdgcn_s_setprio(1);
        {
          f32x4 acc[4][4];
          seg<4, 4, 0, 2, 0, R_ / 2, true>({acc[0], acc[1], acc[2], acc[3]}, L + L_X + voff, rw, mb, voff);
          if (Ly == 0) fillh<1>(rw, mb, voff);
          fillr<R_, 8>(rw, mb, voff);
          if (Ly) CST(28 + 32);
          if (hl == 0) {
            poll8<0>(ws, 5 * Ly + 1, c, c, tag, lane);
            *(volatile LAS unsigned*)(L + L_W + 60) = Ly + 1;
            if (Ly == 0 && blockIdx.x == 0 && lane == 0) __hip_atomic_store((gu64*)(ws + FL_OFF), tag, RLX_AGENT);
          } else lds_wait_ge<1>(L, 15, Ly + 1);
          if (Ly) CST(29 + 32);
          STAMP(7 + 16 * Ly);
          stage8(rs, X1_OFF + c * XBC, L, j, w, lane);
          if (Ly) CST(30 + 32);
          lds_wait_ge<0>(L, 16, 4 * (Ly + 1));
          STAMP(8 + 16 * Ly);
          CST(8 + 32 * Ly);
          seg<4, 4, 2, 18, 8, 72, false>({acc[0], acc[1], acc[2], acc[3]}, L + L_X + voff, rw, mb, voff, WordHook{L, 4u * (Ly + 1), hl});
          CST(9 + 32 * Ly);
          float rstd[4], mr[4];
          stats(rstd, mr);
          constexpr int Q4 = R_ / 4;
          hpair(I0, I0, acc[0], acc[1], rstd, mr, 4 * hl, [&] { fillr<72, Q4>(rw, mb, voff); });
          fillr<72 + Q4, Q4>(rw, mb, voff);
          hpair(I0, I0, acc[2], acc[3], rstd, mr, 4 * hl + 2, [&] { fillr<72 + 2 * Q4, Q4>(rw, mb, voff); });
          fillr<72 + 3 * Q4, R_ - 3 * Q4>(rw, mb, voff);
        }
        CST(10 + 32 * Ly);
        bar();
        CST(11 + 32 * Ly);
        {
          f32x4 acc[4][4];
          seg<4, 4, 0, 8, 72, 162, true>({acc[0], acc[1], acc[2], acc[3]}, L + L_H + voff, rw, mb, voff);
          CST(12 + 32 * Ly);
          bar();
          CST(13 + 32 * Ly);
          seg<2, 4, 8, 18, 104, 162, false>({acc[0], acc[1]}, L + L_H + voff, rw, mb, voff);
          pstore2(acc[0], acc[1], 0, std::integral_constant<int, TRICKLE>{});
          CST(20 + 32 * Ly);
          seg<2, 4, 8, 18, 124, 162, false>({acc[2], acc[3]}, L + L_H + voff, rw, mb, voff);
          __builtin_amdgcn_s_setprio(0);
          pstore2(acc[2], acc[3], 1, std::integral_constant<int, TRICKLE>{});
          CST(14 + 32 * Ly);
        }
        arrive<4>(ws, L, 0, (5 * Ly + 2) * 32 + c, j, tag, lane);
        CST(15 + 32 * Ly);
        lds_wait_ge<3>(L, 4 + hl, Ly + 1);
        CST(18 + 32 * Ly);
        f32x4 own[4];
        seg<1, 4, 0, 18, 144, 162, true>({own}, L + L_H + voff, rw, mb, voff);
#pragma unroll
        for (int tt = 0; tt < 4; ++tt) *(LAS v2u*)(L + L_OWN + ((hl >> 1) * 4 + tt) * 1024 + voff + 8 * (hl & 1)) = pk4(own[tt]);
        asm volatile("s_waitcnt lgkmcnt(0)" ::: "memory");
        if (lane == 0) __hip_atomic_fetch_add((LAS unsigned*)(L + L_W + 44), 1u, __ATOMIC_RELAXED, __HIP_MEMORY_SCOPE_WORKGROUP);
        CST(17 + 32 * Ly);
        lds_wait_ge<1>(L, 2, Ly + 1);
        CST(16 + 32 * Ly);
        f32x4 sa, sb;
        finish(std::integral_constant<int, 7>{}, sa, sb, 2 * j, hl, hl,
               [&](f32x4& a, f32x4& b) { lds_wait_ge<1>(L, 11, 4 * (Ly + 1)); base(a, b, 2 * j, hl, hl); });
        if (hl == 3) {
          base(sa, sb, 16 + (j >> 2), j & 3, 8);
          if (j >= 4) lds_wait_ge<1>(L, 3, Ly + 1);
          finish(std::integral_constant<int, 8>{}, sa, sb, 16 + (j >> 2), j & 3, 8, nomid);
        }
      }
      int role2 = role;
      asm volatile("" : "+s"(role2));
      if (role2 != 0) {
        const StreamS mb = mkS(j, hl);
        {
          f32x4 acc[5][4];
          seg<5, 4, 0, 2, 0, R_ / 2, true>({acc[0], acc[1], acc[2], acc[3], acc[4]}, L + L_X + voff, rw, mb, voff);
          if (Ly) CST(28 + 32);
          lds_wait_ge<1>(L, 15, Ly + 1);
          if (Ly) CST(29 + 32);
          stage8(rs, X1_OFF + c * XBC, L, j, w, lane);
          if (Ly) CST(30 + 32);
          lds_wait_ge<1>(L, 16, 4 * (Ly + 1));
          lds_wait_ge<1>(L, 17, 4 * (Ly + 1));
          float rstd[4], mr[4];
          stats(rstd, mr);
          lds_wait_ge<2>(L, 20 + hl, 4 * (Ly + 1));
          fillr<R_ / 2, R_ / 2 + 10>(rw, mb, voff);
          CST(8 + 32 * Ly);
          seg<5, 4, 2, 18, 10, 90, false>({acc[0], acc[1], acc[2], acc[3], acc[4]}, L + L_X + voff, rw, mb, voff);
          CST(9 + 32 * Ly);
          bar();
          CST(11 + 32 * Ly);
          fillr<90, R_ / 4>(rw, mb, voff);
          hpair(I0, std::integral_constant<int, G2PK>{}, acc[0], acc[1], rstd, mr, 16 + 2 * hl, [] {});
          CST(10 + 32 * Ly);
          fillr<90 + R_ / 4, R_ / 4>(rw, mb, voff);
          v2u o[4];
          gtile(I1, acc[4], rstd, mr, 32 + hl, o);
          fillr<90 + 2 * (R_ / 4), R_ / 4>(rw, mb, voff);
          hpair(I1, I1, acc[2], acc[3], rstd, mr, 24 + 2 * hl, [] {});
          fillr<90 + 3 * (R_ / 4), R_ - 3 * (R_ / 4)>(rw, mb, voff);
#pragma unroll
          for (int tt = 0; tt < 4; ++tt) *(LAS v2u*)(L + L_H + tt * REV + (16 + (hl >> 1)) * 1024 + voff + 8 * (hl & 1)) = o[tt];
          CST(12 + 32 * Ly);
        }
        bar();
        CST(13 + 32 * Ly);
        {
          f32x4 acc[2][4];
          seg<2, 4, 0, 18, 90, 162, true>({acc[0], acc[1]}, L + L_H + voff, rw, mb, voff);
          CST(20 + 32 * Ly);
          __builtin_amdgcn_s_setprio(1);
          StoreHook hk{{ppack(acc[0][0], acc[1][0]), ppack(acc[0][1], acc[1][1]), ppack(acc[0][2], acc[1][2]), ppack(acc[0][3], acc[1][3])}, rs, voff, poff(0), pstr(0)};
          seg<2, 4, 0, 18, 126, 162, true>({acc[0], acc[1]}, L + L_H + voff, rw, mb, voff, hk);
          *(volatile LAS unsigned*)(L + L_W + 16 + hl * 4) = Ly + 1;
          CST(23 + 32 * Ly);
          pstore2(acc[0], acc[1], 1, I0);
          CST(14 + 32 * Ly);
        }
        u64 fv = 0;
        if (hl == 1) fv = __hip_atomic_load(flagp(ws, (5 * Ly + 2) * 32 + c, lane & 7), RLX_AGENT);
        arrive<4>(ws, L, 1, (5 * Ly + 3) * 32 + c, j, tag, lane);
        if (hl == 1) {
          if (!__all(fv == tag)) poll8<0>(ws, 5 * Ly + 2, c, c, tag, lane);
          *(volatile LAS unsigned*)(L + L_W + 8) = Ly + 1;
        }
        CST(15 + 32 * Ly);
        if (Ly == 0) fillh<0>(rw, wq0, voff);
        lds_wait_ge<1>(L, 11, 4 * (Ly + 1));
        f32x4 sa, sb;
        base(sa, sb, 2 * j + 1, hl, 4 + hl);
        SB();
        CST(17 + 32 * Ly);
        if (hl == 0) { poll8<0>(ws, 5 * Ly + 3, c, c, tag, lane); *(volatile LAS unsigned*)(L + L_W + 12) = Ly + 1; }
        else lds_wait_ge<1>(L, 3, Ly + 1);
        CST(16 + 32 * Ly);
        finish(std::integral_constant<int, 7>{}, sa, sb, 2 * j + 1, hl, 4 + hl, nomid);
        __builtin_amdgcn_s_setprio(0);
      }
      CST(22 + 32 * Ly);
      { const int tid = threadIdx.x ^ 256; STAMP(12 + 16 * Ly); }
      if (Ly == 0) {
        const int cprev = c >= 2 ? c - 2 : c;
        arrive<8>(ws, L, 14, 4 * 32 + c, j, tag, lane);
        CST(19);
        if (role == 0) { lds_wait_ge<2>(L, 14, 8); fillh<0>(rw, wq0, voff); }
        if (role) fillh<1>(rw, wq0, voff);
        CST(21);
        await(ws, 4, c, cprev, tag, w, lane);
        CST(31);
        STAMP(17);
        stage<true>(rs, X2_OFF + c * XBC, X2_OFF + cprev * XBC + 3 * REV, L, w, lane);
        if (role == 0) fillh<1>(rw, wq0, voff);
        bar();
        NoHook nh;
        qkv(std::integral_constant<int, 1>{}, nh);
      }
    }
  }
}

extern "C" void kernel_launch(void* const* d_in, const int* in_sizes, int n_in, void* d_out, int out_size, void* d_ws,
                              size_t ws_size, hipStream_t stream) {
  const u16* x = (const u16*)d_in[0];
  const u16* wqkv = (const u16*)d_in[1];
  const u16* wo = (const u16*)d_in[2];
  const u16* w1 = (const u16*)d_in[4];
  const u16* b1 = (const u16*)d_in[5];
  const u16* w2 = (const u16*)d_in[6];
  const u16* b2 = (const u16*)d_in[7];
  (void)hipFuncSetAttribute((const void*)k_main, hipFuncAttributeMaxDynamicSharedMemorySize, LDS_BYTES);
  k_main<<<256, 512, LDS_BYTES, stream>>>((char*)d_ws, x, wqkv, wo, w1, b1, w2, b2, (u16*)d_out);
}
```

```cpp
#include <hip/hip_runtime.h>
#include <hip/hip_bf16.h>
#include <type_traits>

#define GAS __attribute__((address_space(1)))
#define LAS __attribute__((address_space(3)))
typedef unsigned short u16;
typedef unsigned v4u __attribute__((ext_vector_type(4)));
typedef unsigned v2u __attribute__((ext_vector_type(2)));
typedef float f32x4 __attribute__((ext_vector_type(4)));
typedef float f32x2 __attribute__((ext_vector_type(2)));
typedef short bf16x8 __attribute__((ext_vector_type(8)));
typedef __bf16 bf16x2 __attribute__((ext_vector_type(2)));
typedef GAS unsigned gu32;
typedef unsigned long long u64;
typedef GAS u64 gu64;
typedef __amdgpu_buffer_rsrc_t rsrc_t;
#define RLX_AGENT __ATOMIC_RELAXED, __HIP_MEMORY_SCOPE_AGENT

#define TRICKLE 4
#define G2PK 0
#define GOS 8
#define JR1 1
#define PV 3
#define PVS 1
#define EARLY 8
#define G1PK 0
#define F4PK 0
#define FALL4 0
#define GSPREAD 2

constexpr int S_ = 2048, D_ = 576, F_ = 4608;
constexpr int REV = 18 * 1024;
constexpr int XBC = 4 * REV;
constexpr int XBSZ = 32 * XBC;
constexpr int FL_OFF = 0;
constexpr int WQ_OFF = 131072;
constexpr int WO_OFF = WQ_OFF + 128 * REV;
constexpr int W1_OFF = WO_OFF + 36 * REV;
constexpr int W2_OFF = W1_OFF + 288 * REV;
constexpr int CSP_OFF = W2_OFF + 288 * REV;
constexpr int X2_OFF = CSP_OFF + 8 * 18 * 2304 + 4096 - (8 * 18 * 2304) % 4096;
constexpr int OB_OFF = X2_OFF + XBSZ;
constexpr int X1_OFF = OB_OFF + XBSZ;
constexpr int PB_OFF = X1_OFF + XBSZ;
constexpr int PBSZ = 32 * 8 * 8 * 9216;
constexpr int ST_OFF = PB_OFF + PBSZ;
constexpr int ST2_OFF = ST_OFF + 256 * 64 * 8;
constexpr int WS_BYTES = ST2_OFF + 2 * 256 * 8 * 64 * 8;
constexpr int L_X = 0, L_H = 73728, L_ATT = 92160, ATT_STR = 10240, A_Q = 0, A_K = 2560, A_V = 5120, A_E = 7680;
constexpr int L_RES = 137216, L_OWN = 147456, L_CS1 = L_OWN + 8192, L_B1 = L_CS1 + 2304, L_SP = L_B1 + 2304,
              L_SH = L_SP + 2048, L_B2 = L_SH + 768, L_W = L_B2 + 384, L_END = L_W + 128;
static_assert(L_END <= 163840, "lds");
constexpr int LDS_BYTES = 163840;
constexpr int CODE_SHARE = 1920;

__device__ __forceinline__ float bflo(unsigned u) { return __uint_as_float(u << 16); }
__device__ __forceinline__ float bfhi(unsigned u) { return __uint_as_float(u & 0xffff0000u); }
__device__ __forceinline__ unsigned pk2(float a, float b) {
  f32x2 v = {a, b};
  return __builtin_bit_cast(unsigned, __builtin_convertvector(v, bf16x2));
}
__device__ __forceinline__ v2u pk4(f32x4 v) { return v2u{pk2(v[0], v[1]), pk2(v[2], v[3])}; }
__device__ __forceinline__ f32x4 unpk4(unsigned a, unsigned b) { return f32x4{bflo(a), bfhi(a), bflo(b), bfhi(b)}; }
__device__ __forceinline__ f32x4 mfma(v4u a, v4u b, f32x4 c) {
  return __builtin_amdgcn_mfma_f32_16x16x32_bf16(__builtin_bit_cast(bf16x8, a), __builtin_bit_cast(bf16x8, b), c, 0, 0, 0);
}
__device__ __forceinline__ v4u ldc(rsrc_t r, int voff, int soff) { return __builtin_amdgcn_raw_buffer_load_b128(r, voff, soff, 16); }
__device__ __forceinline__ void stc128(v4u v, rsrc_t r, int voff, int soff) {
  __builtin_amdgcn_raw_buffer_store_b128(v, r, voff, soff, 16);
  asm volatile("s_nop 1" :: "v"(v));
}
__device__ __forceinline__ v2u ldc64(rsrc_t r, int voff, int soff) { return __builtin_amdgcn_raw_buffer_load_b64(r, voff, soff, 16); }
__device__ __forceinline__ void stc64(v2u v, rsrc_t r, int voff, int soff) { __builtin_amdgcn_raw_buffer_store_b64(v, r, voff, soff, 16); }
__device__ __forceinline__ void stc32(unsigned v, rsrc_t r, int voff, int soff) { __builtin_amdgcn_raw_buffer_store_b32(v, r, voff, soff, 16); }
#define SB() __builtin_amdgcn_sched_barrier(0)
__device__ __forceinline__ void bar() {
  asm volatile("s_waitcnt lgkmcnt(0)" ::: "memory");
  __builtin_amdgcn_s_barrier();
  asm volatile("" ::: "memory");
}
__device__ __forceinline__ float red16max(float x) {
  auto r = __builtin_amdgcn_permlane16_swap(__float_as_uint(x), __float_as_uint(x), false, false);
  x = fmaxf(__uint_as_float(r[0]), __uint_as_float(r[1]));
  auto q = __builtin_amdgcn_permlane32_swap(__float_as_uint(x), __float_as_uint(x), false, false);
  return fmaxf(__uint_as_float(q[0]), __uint_as_float(q[1]));
}
__device__ __forceinline__ float red16sum(float x) {
  auto r = __builtin_amdgcn_permlane16_swap(__float_as_uint(x), __float_as_uint(x), false, false);
  x = __uint_as_float(r[0]) + __uint_as_float(r[1]);
  auto q = __builtin_amdgcn_permlane32_swap(__float_as_uint(x), __float_as_uint(x), false, false);
  return __uint_as_float(q[0]) + __uint_as_float(q[1]);
}

#define PST(i) do {} while (0)
#define PSTOUT() do {} while (0)
#define STAMP(i) do {} while (0)
#define CST(i) do {} while (0)

__device__ __forceinline__ gu64* flagp(char* ws, int line, int i) { return (gu64*)(ws + FL_OFF + 256 + line * 256 + i * 8); }
__device__ __forceinline__ void publish(char* ws, int line, int i, u64 tag, int tid) {
  asm volatile("s_waitcnt vmcnt(0)" ::: "memory");
  bar();
  if (tid == 0) __hip_atomic_store(flagp(ws, line, i), tag, RLX_AGENT);
}
template <int SL> __device__ __forceinline__ void poll(gu64* p, u64 tag) {
#pragma unroll 1
  for (unsigned spins = 0; spins < (1u << 22); ++spins) {
    const u64 v = __hip_atomic_load(p, RLX_AGENT);
    if (__all(v == tag)) break;
    if (SL) __builtin_amdgcn_s_sleep(SL);
  }
  __builtin_amdgcn_fence(__ATOMIC_ACQUIRE, "wavefront");
}
template <int SL> __device__ __forceinline__ void poll8(char* ws, int ph, int c, int c2, u64 tag, int lane) {
  poll<SL>(flagp(ws, ph * 32 + ((lane & 8) ? c2 : c), lane & 7), tag);
}
__device__ __forceinline__ void await(char* ws, int ph, int c, int c2, u64 tag, int w, int lane) {
  if (w == 0) poll8<0>(ws, ph, c, c2, tag, lane);
  bar();
}

__device__ __forceinline__ float sfma(float a, float b, float c) { float d; asm("v_fma_f32 %0, %1, %2, %3" : "=v"(d) : "v"(a), "v"(b), "v"(c)); return d; }
__device__ __forceinline__ float sfnma(float a, float b, float c) { float d; asm("v_fma_f32 %0, -%1, %2, %3" : "=v"(d) : "v"(a), "v"(b), "v"(c)); return d; }
__device__ __forceinline__ float sfmas(float a, float s) { float d; asm("v_fma_f32 %0, %1, %2, %2" : "=v"(d) : "v"(a), "s"(s)); return d; }
__device__ __forceinline__ float ssqs(float a, float s) { float d; asm("v_fma_f32 %0, %1, %1, %2" : "=v"(d) : "v"(a), "s"(s)); return d; }
__device__ __forceinline__ float smul(float a, float b) { float d; asm("v_mul_f32 %0, %1, %2" : "=v"(d) : "v"(a), "v"(b)); return d; }
__device__ __forceinline__ float sadd(float a, float b) { float d; asm("v_add_f32 %0, %1, %2" : "=v"(d) : "v"(a), "v"(b)); return d; }
__device__ __forceinline__ void stat2(v4u x, float& s1, float& s2) {
  const bf16x2 ones = __builtin_bit_cast(bf16x2, 0x3f803f80u);
#pragma unroll
  for (int i = 0; i < 4; ++i) {
    const unsigned u = x[i];
    const bf16x2 v = __builtin_bit_cast(bf16x2, u);
    s1 = __builtin_amdgcn_fdot2_f32_bf16(v, ones, s1, false);
    s2 = __builtin_amdgcn_fdot2_f32_bf16(v, v, s2, false);
  }
}
__device__ __forceinline__ void stat_fin(float s1, float s2, float scale, float& rstd, float& mr) {
  const float inv = 1.0f / D_, mean = smul(s1, inv);
  const float var = fmaxf(sfnma(mean, mean, smul(s2, inv)), 0.f);
  float r = __builtin_amdgcn_rsqf(sadd(var, 1e-5f));
  asm volatile("s_nop 0" : "+v"(r));
  rstd = smul(r, scale);
  mr = smul(mean, rstd);
}
__device__ __forceinline__ void stat_own(const LAS char* L, int tt, int m, float scale, float& rstd, float& mr) {
  const f32x4 p = *(const LAS f32x4*)(L + L_SP + tt * 256 + m * 16);
  stat_fin(sadd(p[0], p[2]), sadd(p[1], p[3]), scale, rstd, mr);
}
__device__ __forceinline__ void stat_own4(const LAS char* L, int tt, int m, float scale, float& rstd, float& mr) {
  const f32x4 p = *(const LAS f32x4*)(L + L_SP + tt * 256 + m * 16), q = *(const LAS f32x4*)(L + L_SP + 1024 + tt * 256 + m * 16);
  stat_fin(sadd(sadd(p[0], p[2]), sadd(q[0], q[2])), sadd(sadd(p[1], p[3]), sadd(q[1], q[3])), scale, rstd, mr);
}
__device__ __forceinline__ void stat_halo(const LAS char* L, int m, float& rstd, float& mr) {
  const f32x4 p = *(const LAS f32x4*)(L + L_SH + m * 48), q = *(const LAS f32x4*)(L + L_SH + m * 48 + 16), r = *(const LAS f32x4*)(L + L_SH + m * 48 + 32);
  stat_fin(sadd(sadd(sadd(p[0], p[2]), sadd(q[0], q[2])), sadd(r[0], r[2])), sadd(sadd(sadd(p[1], p[3]), sadd(q[1], q[3])), sadd(r[1], r[3])), 1.0f, rstd, mr);
}
__device__ __forceinline__ void ones_row(LAS char* L, int lane) {
  if ((lane & 15) == 0) {
#pragma unroll
    for (int s = 0; s < 18; ++s) *(LAS v4u*)(L + L_X + s * 1024 + lane * 16) = v4u{0x3f803f80u, 0x3f803f80u, 0x3f803f80u, 0x3f803f80u};
  }
}
template <int CTRL> __device__ __forceinline__ float dppadd(float x) {
  return x + __uint_as_float(__builtin_amdgcn_update_dpp(0u, __float_as_uint(x), CTRL, 0xf, 0xf, true));
}
__device__ __forceinline__ float red8sum(float x) { return dppadd<0x141>(dppadd<0x4E>(dppadd<0xB1>(x))); }
template <bool HALO> __device__ __forceinline__ void stage(rsrc_t rs, int xo, int ho, LAS char* L, int w, int lane) {
  const int tt = w & 3, kh = w >> 2, voff = lane * 16, hw = min(w, 5);
  v4u x[9], h[3];
#pragma unroll
  for (int s = 0; s < 9; ++s) x[s] = ldc(rs, voff + (s & 3) * 1024, xo + tt * REV + kh * 9216 + (s >> 2) * 4096);
  if (HALO) {
#pragma unroll
    for (int s = 0; s < 3; ++s) h[s] = ldc(rs, voff + s * 1024, ho + hw * 3072);
  }
  SB();
  float s1[2] = {0.f, 0.f}, s2[2] = {0.f, 0.f};
#pragma unroll
  for (int s = 0; s < 9; ++s) {
    *(LAS v4u*)(L + L_X + (tt + (HALO ? 1 : 0)) * REV + kh * 9216 + s * 1024 + voff) = x[s];
    stat2(x[s], s1[s & 1], s2[s & 1]);
    SB();
  }
  float a = red16sum(s1[0] + s1[1]), b = red16sum(s2[0] + s2[1]);
  if (lane < 16) *(LAS f32x2*)(L + L_SP + tt * 256 + lane * 16 + kh * 8) = f32x2{a, b};
  if (HALO) {
    a = 0.f; b = 0.f;
    const v4u ones = {0x3f803f80u, 0x3f803f80u, 0x3f803f80u, 0x3f803f80u};
#pragma unroll
    for (int s = 0; s < 3; ++s) {
      *(LAS v4u*)(L + L_X + hw * 3072 + s * 1024 + voff) = (lane & 15) ? h[s] : ones;
      stat2(h[s], a, b);
    }
    a = red16sum(a); b = red16sum(b);
    if (lane < 16) *(LAS f32x2*)(L + L_SH + lane * 48 + hw * 8) = f32x2{a, b};
  }
}

__device__ __forceinline__ void stage8(rsrc_t rs, int xo, LAS char* L, int j, int w, int lane) {
  const int tt = w & 3, kh = w >> 2, voff = lane * 16;
  v4u x[8];
#pragma unroll
  for (int i = 0; i < 4; ++i) {
    const int q = 2 + 8 * kh + 2 * i, ks = q < 16 ? (q + 2 * j) & 15 : q;
    x[2 * i] = ldc(rs, voff, xo + tt * REV + ks * 1024);
    x[2 * i + 1] = ldc(rs, voff + 1024, xo + tt * REV + ks * 1024);
  }
  SB();
  float s1[2] = {0.f, 0.f}, s2[2] = {0.f, 0.f};
#pragma unroll
  for (int s = 0; s < 8; ++s) {
    *(LAS v4u*)(L + L_X + tt * REV + (2 + 8 * kh + s) * 1024 + voff) = x[s];
    stat2(x[s], s1[s & 1], s2[s & 1]);
    SB();
  }
  const float a = red16sum(s1[0] + s1[1]), b = red16sum(s2[0] + s2[1]);
  if (lane < 16) *(LAS f32x2*)(L + L_SP + tt * 256 + lane * 16 + kh * 8) = f32x2{a, b};
  asm volatile("s_waitcnt lgkmcnt(0)" ::: "memory");
  if (lane == 0) __hip_atomic_fetch_add((LAS unsigned*)(L + L_W + 64 + kh * 4), 1u, __ATOMIC_RELAXED, __HIP_MEMORY_SCOPE_WORKGROUP);
}

#define RING 28
constexpr int R_ = RING;
constexpr int RB = 256 - 4 * R_;
template <int SLOT, int OFF> __device__ __forceinline__ void rload(v4u rs, int voff, int so) {
  asm volatile("buffer_load_dwordx4 v[%c3:%c4], %0, %1, %2 offen offset:%c5 sc1"
               :: "v"(voff), "s"(rs), "s"(so), "i"(RB + 4 * SLOT), "i"(RB + 4 * SLOT + 3), "i"(OFF));
}
template <int SLOT, int OFF> __device__ __forceinline__ void rput(unsigned lds) {
  asm volatile("ds_write_b128 %0, v[%c1:%c2] offset:%c3" :: "v"(lds), "i"(RB + 4 * SLOT), "i"(RB + 4 * SLOT + 3), "i"(OFF) : "memory");
}
template <int N> __device__ __forceinline__ void rwait() { asm volatile("s_waitcnt vmcnt(%c0)" :: "i"(N)); }
template <int SLOT> __device__ __forceinline__ void rmfma(f32x4& acc, v4u x) {
  asm volatile("v_mfma_f32_16x16x32_bf16 %0, v[%c2:%c3], %1, %0" : "+v"(acc) : "v"(x), "i"(RB + 4 * SLOT), "i"(RB + 4 * SLOT + 3));
}
template <int SLOT> __device__ __forceinline__ void rmfma0(f32x4& acc, v4u x) {
  asm volatile("v_mfma_f32_16x16x32_bf16 %0, v[%c2:%c3], %1, 0" : "=&v"(acc) : "v"(x), "i"(RB + 4 * SLOT), "i"(RB + 4 * SLOT + 3));
}
template <int I, int N, class Fn> __device__ __forceinline__ void sfor(Fn&& f) {
  if constexpr (I < N) { f(std::integral_constant<int, I>{}); sfor<I + 1, N>(f); }
}
constexpr int cmin(int a, int b) { return a < b ? a : b; }
struct StreamQ { int b[1]; static constexpr int id(int) { return 0; } static constexpr int kb(int P) { return P; } };
struct StreamF {
  int b[4];
  static constexpr int E = FALL4 ? 18 : EARLY, P1 = 72 + 4 * E, N2 = FALL4 ? 2 : 2 * (18 - E);
  static constexpr int id(int P) { return P < 72 ? 0 : P < P1 ? 1 + ((P - 72) % 4) / 2 : P < 144 ? 1 + (P - P1) / N2 : 3; }
  static constexpr int kb(int P) { return P < 72 ? (P % 4) * 18 + P / 4 : P < P1 ? (P % 2) * 18 + (P - 72) / 4 : P < 144 ? (P % 2) * 18 + E + ((P - P1) % N2) / 2 : P - 144; }
};
struct StreamS {
  int b[4];
  static constexpr int id(int P) { return P < 90 ? (P % 5 < 4 ? 0 : 3) : 1 + (P - 90) / 36; }
  static constexpr int kb(int P) { return P < 90 ? (P % 5 < 4 ? ((P % 5) % 2 + 8 * ((P % 5) / 2)) * 18 : 0) + P / 5 : (P % 2) * 18 + ((P - 90) % 36) / 2; }
};
template <int P, class St> __device__ __forceinline__ void sload(v4u r, const St& st, int voff) {
  rload<P % R_, (St::kb(P) & 3) * 1024>(r, voff, st.b[St::id(P)] + (St::kb(P) >> 2) * 4096);
}
template <int P0, int N, class St> __device__ __forceinline__ void fillr(v4u r, const St& st, int voff) {
  sfor<P0, P0 + N>([&](auto pc) { sload<pc.value>(r, st, voff); });
  SB();
}
template <int P, int TOTAL, class St> __device__ __forceinline__ void refill(v4u r, const St& st, int voff) {
  if constexpr (P + R_ < TOTAL) sload<P + R_>(r, st, voff);
}
__device__ __forceinline__ StreamQ mkQ(int j, int hl, int role) { return StreamQ{{WQ_OFF + ((4 * j + hl) * 4 + 2 * role) * REV}}; }
__device__ __forceinline__ int w2pair(int j, int d, int role) { return W2_OFF + (36 * j + (d != j ? 4 * d : 32) + 2 * role) * REV; }
__device__ __forceinline__ StreamF mkF(int j, int hl) { return StreamF{{W1_OFF + (36 * j + 4 * hl) * REV, w2pair(j, 2 * hl, 0), w2pair(j, 2 * hl + 1, 0), W2_OFF + (40 * j + hl) * REV}}; }
__device__ __forceinline__ StreamS mkS(int j, int hl) { return StreamS{{W1_OFF + (36 * j + 16 + 2 * hl) * REV, w2pair(j, 2 * hl, 1), w2pair(j, 2 * hl + 1, 1), W1_OFF + (36 * j + 32 + hl) * REV}}; }
struct NoHook { template <int S> static constexpr int extra() { return 0; } template <int S> __device__ __forceinline__ void at() {} };
template <int NT, int NK, int S0, int S1, int P0, int TOTAL, bool INIT, int I0, int NKL, class St, class Hook>
__device__ __forceinline__ void segx(f32x4* const (&acc)[NT], const LAS char* const (&xb)[NK], v4u r, const St& mb, int voff, Hook& hook) {
  constexpr int XD = NT >= 4 ? 1 : (NT == 1 ? 4 : 2);
  v4u xf[XD][NK];
#pragma unroll
  for (int d = 0; d < XD; ++d)
#pragma unroll
    for (int tt = 0; tt < NK; ++tt)
      if (S0 + d < S1) xf[d][tt] = *(const LAS v4u*)(xb[tt] + (S0 + d) * 1024);
  SB();
  asm volatile("s_nop 1");
  sfor<S0, S1>([&](auto sc) {
    constexpr int s = sc.value, p = P0 + (s - S0) * NT, d = (s - S0) % XD;
    constexpr int RS = (R_ - I0 + 3) / 4;
    constexpr int lag = RS > 1 ? RS : 1;
    constexpr int k = s - S0;
    constexpr int issued = k < RS ? I0 + 4 * k : cmin(TOTAL, R_ + (k < lag ? P0 : p - lag * NT));
    rwait<issued - (p + NT) + Hook::template extra<s>()>();
    if constexpr (k < RS) sfor<I0 + 4 * k, cmin(R_, I0 + 4 * k + 4)>([&](auto qc) { sload<qc.value>(r, mb, voff); });
    hook.template at<s>();
    sfor<0, NK>([&](auto kc) {
      constexpr int tt = kc.value;
      sfor<0, NT>([&](auto tc) {
        constexpr int t = tc.value;
        if constexpr (t == NT - 1 && tt >= NKL) {}
        else if constexpr (INIT && s == S0) rmfma0<(p + t) % R_>(acc[t][tt], xf[d][tt]);
        else rmfma<(p + t) % R_>(acc[t][tt], xf[d][tt]);
        constexpr int idx = tt * NT + t;
        if constexpr (k >= lag && idx % NK == 0 && idx / NK < NT) refill<p - lag * NT + idx / NK, TOTAL>(r, mb, voff);
      });
      SB();
      if constexpr (s + XD < S1) { xf[d][tt] = *(const LAS v4u*)(xb[tt] + (s + XD) * 1024); SB(); }
    });
  });
  static_assert(I0 == R_ || P0 + (S1 - S0 - (R_ - I0 + 3) / 4) * NT + R_ >= TOTAL, "ramp: all refills are out inside the loop");
  sfor<0, NT>([&](auto tc) { refill<P0 + (S1 - 1 - S0) * NT + tc.value, TOTAL>(r, mb, voff); });
  asm volatile("s_nop 7\n\ts_nop 1");
  SB();
}
template <int NT, int NK, int S0, int S1, int P0, int TOTAL, bool INIT, class St, class Hook = NoHook>
__device__ __forceinline__ void seg(f32x4* const (&acc)[NT], const LAS char* xb, v4u r, const St& mb, int voff, Hook hook = Hook{}) {
  const LAS char* x2 = xb + 2 * REV;
  asm volatile("" : "+v"(x2));
  segx<NT, 4, S0, S1, P0, TOTAL, INIT, R_, 4>(acc, {xb, xb + REV, x2, x2 + REV}, r, mb, voff, hook);
}
template <int LO, class St> __device__ __forceinline__ void fillh(v4u r, const St& st, int voff) { fillr<LO ? R_ / 2 : 0, R_ / 2>(r, st, voff); }
__device__ __forceinline__ v2u trread(const LAS char* p) {
  typedef short s4 __attribute__((ext_vector_type(4)));
  return __builtin_bit_cast(v2u, __builtin_amdgcn_ds_read_tr16_b64_v4i16((LAS s4*)p));
}
constexpr float AL = 0.46866869f, KK = 2.3022082f / AL;
#define EACH16 _Pragma("unroll") for (int tt = 0; tt < 4; ++tt) _Pragma("unroll") for (int r = 0; r < 4; ++r)
__device__ __forceinline__ void gelu16(f32x4 (&a)[4], const float (&rstd)[4], const float (&mr)[4], f32x4 cs, f32x4 bb, v2u (&o)[4]) {
  float e[4][4], u[4][4];
  EACH16 e[tt][r] = sfnma(cs[r], mr[tt], bb[r]);
  SB();
  EACH16 u[tt][r] = sfma(a[tt][r], rstd[tt], e[tt][r]);
  SB();
  EACH16 e[tt][r] = ssqs(u[tt][r], KK);
  SB();
  EACH16 e[tt][r] = smul(e[tt][r], u[tt][r]);
  SB();
  EACH16 e[tt][r] = __builtin_amdgcn_exp2f(-e[tt][r]);
  SB();
  EACH16 e[tt][r] = sfmas(e[tt][r], AL);
  SB();
  EACH16 e[tt][r] = __builtin_amdgcn_rcpf(e[tt][r]);
  SB();
  EACH16 u[tt][r] = smul(u[tt][r], e[tt][r]);
  SB();
#pragma unroll
  for (int tt = 0; tt < 4; ++tt) o[tt] = v2u{pk2(u[tt][0], u[tt][1]), pk2(u[tt][2], u[tt][3])};
  SB();
}
__device__ __forceinline__ void gelu16p(f32x4 (&a)[4], const float (&rstd)[4], const float (&mr)[4], f32x4 cs, f32x4 bb, v2u (&o)[4]) {
  f32x4 e[4];
#pragma unroll
  for (int tt = 0; tt < 4; ++tt) e[tt] = bb - cs * mr[tt];
  SB();
#pragma unroll
  for (int tt = 0; tt < 4; ++tt) a[tt] = a[tt] * rstd[tt] + e[tt];
  SB();
#pragma unroll
  for (int tt = 0; tt < 4; ++tt) e[tt] = a[tt] * a[tt] + KK;
  SB();
#pragma unroll
  for (int tt = 0; tt < 4; ++tt) e[tt] = e[tt] * a[tt];
  SB();
  EACH16 e[tt][r] = __builtin_amdgcn_exp2f(-e[tt][r]);
  SB();
#pragma unroll
  for (int tt = 0; tt < 4; ++tt) e[tt] = e[tt] * AL + AL;
  SB();
  EACH16 e[tt][r] = __builtin_amdgcn_rcpf(e[tt][r]);
  SB();
#pragma unroll
  for (int tt = 0; tt < 4; ++tt) a[tt] = a[tt] * e[tt];
  SB();
#pragma unroll
  for (int tt = 0; tt < 4; ++tt) o[tt] = pk4(a[tt]);
  SB();
}
struct StoreHook {
  v4u d[4]; rsrc_t rs; int voff, po0, po1;
  template <int S> static constexpr int extra() { return S <= 6 ? 0 : S <= 9 ? 1 : S <= 12 ? 2 : S <= 15 ? 3 : 4; }
  template <int S> __device__ __forceinline__ void at() {
    if constexpr (S == 6 || S == 9 || S == 12 || S == 15) {
      constexpr int q = (S - 6) / 3;
      SB(); stc128(d[q], rs, voff, po0 + q * po1); SB();
    }
  }
};
template <int SL> __device__ __forceinline__ void lds_wait_ge(LAS char* L, int word, unsigned want) {
  volatile LAS unsigned* p = (volatile LAS unsigned*)(L + L_W + word * 4);
#pragma unroll 1
  for (unsigned spins = 0; spins < (1u << 22); ++spins) {
    if (__builtin_amdgcn_readfirstlane(*p) >= want) break;
    if (SL) __builtin_amdgcn_s_sleep(SL);
  }
  asm volatile("" ::: "memory");
}
template <int N> __device__ __forceinline__ void arrive(char* ws, LAS char* L, int word, int line, int i, u64 tag, int lane) {
  asm volatile("s_waitcnt vmcnt(0)" ::: "memory");
  if (lane == 0) {
    const unsigned old = __hip_atomic_fetch_add((LAS unsigned*)(L + L_W + word * 4), 1u, __ATOMIC_RELAXED, __HIP_MEMORY_SCOPE_WORKGROUP);
    if (old % N == N - 1) __hip_atomic_store(flagp(ws, line, i), tag, RLX_AGENT);
  }
}
__device__ __forceinline__ void acc8(f32x4& a, f32x4& b, v4u p) {
  a[0] = sadd(a[0], bflo(p[0])); a[1] = sadd(a[1], bfhi(p[0])); a[2] = sadd(a[2], bflo(p[1])); a[3] = sadd(a[3], bfhi(p[1]));
  b[0] = sadd(b[0], bflo(p[2])); b[1] = sadd(b[1], bfhi(p[2])); b[2] = sadd(b[2], bflo(p[3])); b[3] = sadd(b[3], bfhi(p[3]));
}

#define FRESH() int lane = lane0, c = c0, j = j0, w = w0; asm volatile("" : "+v"(lane)); asm volatile("" : "+s"(c), "+s"(j), "+s"(w)); \
  const int g = lane >> 4, m = lane & 15, voff = lane * 16, role = w >> 2, hl = w & 3; (void)g; (void)m; (void)voff; (void)role; (void)hl;

template <bool CS> __device__ __forceinline__ unsigned jtr(v4u* r, LAS char* scr, int lane) {
  const int g = lane >> 4, m = lane & 15;
#pragma unroll
  for (int i = 0; i < 4; ++i) *(LAS v4u*)(scr + i * 1024 + lane * 16) = r[i];
  const LAS char* p = scr + (4 * g + (m >> 2)) * 128 + (m & 3) * 8;
  float cs4[4] = {0.f, 0.f, 0.f, 0.f};
#pragma unroll
  for (int t = 0; t < 4; ++t) { const v2u lo = trread(p + t * 32), hi = trread(p + t * 32 + 2048); r[t] = v4u{lo[0], lo[1], hi[0], hi[1]}; }
  if (CS) {
#pragma unroll
    for (int t = 0; t < 4; ++t) { float dummy = 0.f; stat2(r[t], cs4[t], dummy); cs4[t] = red16sum(cs4[t]); }
  }
  return __float_as_uint(g < 2 ? (g == 0 ? cs4[0] : cs4[1]) : (g == 2 ? cs4[2] : cs4[3]));
}
struct JobHook {
  v4u f[8]; unsigned cs; rsrc_t rs; int lane, d1, d2, dc;
  template <int S> static constexpr int extra() { return S <= 9 ? 0 : S - 9; }
  template <int S> __device__ __forceinline__ void at() {
    if constexpr (S == 9) { SB(); stc32(cs, rs, lane * 4, dc); SB(); }
    if constexpr (S > 9) { SB(); stc128(f[S - 10], rs, lane * 16, (S < 14 ? d1 : d2) + ((S - 10) & 3) * REV); SB(); }
  }
};
__device__ __forceinline__ void wo_req(v4u rw, int j, int hl, int voff) {
  const int wa = WO_OFF + (4 * j + hl) * REV, wb = WO_OFF + (32 + 2 * (j >> 2)) * REV + hl * 9216;
  sfor<0, 27>([&](auto ic) { constexpr int i = ic.value, q = i < 18 ? i : i - 18; rload<i, (q & 3) * 1024>(rw, voff, (i < 18 ? wa : wb) + (q >> 2) * 4096); });
  SB();
}
struct WordHook {
  LAS char* L; unsigned want; int hl;
  template <int S> static constexpr int extra() { return 0; }
  template <int S> __device__ __forceinline__ void at() {
    if constexpr (S == 9) { SB(); lds_wait_ge<1>(L, 17, want); SB(); }
    if constexpr (S == GOS) { SB(); *(volatile LAS unsigned*)(L + L_W + 80 + hl * 4) = want; SB(); }
  }
};

__global__ __launch_bounds__(512, 2) __attribute__((amdgpu_num_vgpr(RB / 2 - 1)))
void k_main(char* __restrict__ ws, const u16* __restrict__ xin, const u16* __restrict__ wqkv, const u16* __restrict__ wo,
            const u16* __restrict__ w1, const u16* __restrict__ b1, const u16* __restrict__ w2, const u16* __restrict__ b2,
            u16* __restrict__ out) {
  extern __shared__ __attribute__((aligned(16))) char smem[];
  {
    u64 pc;
    asm volatile("s_getpc_b64 %0\n\tglobal_load_dword v%c2, %1, %0" : "=&s"(pc)
                 : "v"(((blockIdx.x >> 3) + min((int)threadIdx.x, CODE_SHARE / 128 - 1) * 32) * 128), "i"(RB - 1));
    SB();
  }
  LAS char* L = (LAS char*)smem;
  const int tid = threadIdx.x, lane0 = tid & 63, w0 = __builtin_amdgcn_readfirstlane(tid >> 6);
  int c0, j0;
  {
    constexpr int G = GSPREAD, per = 8 / G;
    const int b = blockIdx.x, x = b & 7, r = b >> 3;
    j0 = (x % G) * per + (r % per);
    c0 = (x / G) * (4 * G) + r / per;
  }
  const rsrc_t rs = __builtin_amdgcn_make_buffer_rsrc((void*)ws, 0, WS_BYTES, 0x00020000);
  const v4u rw = {(unsigned)(size_t)ws, (unsigned)((size_t)ws >> 32) & 0xffffu, (unsigned)WS_BYTES, 0x00020000u};
  asm volatile("" ::: "v255");
  PST(0);
  u64 tag = 0;
  auto qkv = [&](auto lc, auto& jh) {
    constexpr int Ly = lc.value;
    if (Ly) STAMP(2 + 16 * Ly);
    FRESH();
    {
      const LAS char* xr = L + L_X + (hl + 1) * REV + voff;
      const v4u ra = *(const LAS v4u*)(xr + (2 * j + role) * 1024), rb = *(const LAS v4u*)(xr + (16 + (j >> 2)) * 1024);
      *(LAS v4u*)(L + L_RES + w * 1024 + voff) = ra;
      if (w == (j & 3)) *(LAS v4u*)(L + L_RES + 8192 + voff) = rb;
    }
    const StreamQ qb = mkQ(j, hl, role);
    constexpr int I0 = Ly == 0 ? 8 : R_;
    auto fix = [&](f32x4 a, const float (&cs)[4], float rstd, float mr) {
      float u[4];
#pragma unroll
      for (int r = 0; r < 4; ++r) u[r] = smul(a[r], rstd);
#pragma unroll
      for (int r = 0; r < 4; ++r) u[r] = sfnma(cs[r], mr, u[r]);
      return v2u{pk2(u[0], u[1]), pk2(u[2], u[3])};
    };
    auto bcast = [&](f32x4 a, float (&cs)[4]) {
#pragma unroll
      for (int r = 0; r < 4; ++r) cs[r] = __uint_as_float(__builtin_amdgcn_update_dpp(0u, __float_as_uint(a[r]), 0x150, 0xf, 0xf, false));
    };
    CST(0 + 32 * Ly);
    if (role == 0) {
      f32x4 a0[5], a1[5];
      const LAS char* xa = L + L_X + voff, *xc = xa + 3 * REV;
      asm volatile("" : "+v"(xc));
      float rstd[5], mr[5], cs0[4], cs1[4];
      stat_halo(L, m, rstd[0], mr[0]);
#pragma unroll
      for (int t = 1; t < 5; ++t) stat_own(L, t - 1, m, 1.0f, rstd[t], mr[t]);
      segx<2, 5, 0, 18, 0, 36, true, I0, 5>({a0, a1}, {xa, xa + REV, xa + 2 * REV, xc, xc + REV}, rw, qb, voff, jh);
      CST(1 + 32 * Ly);
      LAS char* A = L + L_ATT + hl * ATT_STR + lane * 8;
      bcast(a0[0], cs0); bcast(a1[0], cs1);
      SB();
#pragma unroll
      for (int t = 0; t < 5; ++t) {
        if (t) *(LAS v2u*)(A + A_Q + t * 512) = fix(a0[t], cs0, rstd[t], mr[t]);
        *(LAS v2u*)(A + A_K + t * 512) = fix(a1[t], cs1, rstd[t], mr[t]);
      }
    }
    int role2 = role;
    asm volatile("" : "+s"(role2));
    if (role2) {
      f32x4 a0[5], a1[5];
      const int ta = (hl & 1) ? 3 : 1, tb = 4 - ta;
      const LAS char* x0 = L + L_X + voff, *xa = x0 + ta * REV, *xc = x0 + tb * REV;
      NoHook hk;
      __builtin_amdgcn_s_setprio(1);
      if constexpr (JR1) segx<2, 5, 0, 18, 0, 36, true, I0, 3>({a0, a1}, {x0, xa, xa + REV, xc, xc + REV}, rw, qb, voff, jh);
      else segx<2, 5, 0, 18, 0, 36, true, I0, 3>({a0, a1}, {x0, xa, xa + REV, xc, xc + REV}, rw, qb, voff, hk);
      __builtin_amdgcn_s_setprio(0);
      CST(1 + 32 * Ly);
      LAS char* A = L + L_ATT + hl * ATT_STR + A_V + m * 32 + g * 8, *E = L + L_ATT + (hl & 2) * ATT_STR + A_E + m * 32 + g * 8;
      float rstd[5], mr[5], cs0[4], cs1[4];
      int to[5] = {0, ta, ta + 1, tb, tb + 1};
      stat_halo(L, m, rstd[0], mr[0]);
#pragma unroll
      for (int t = 1; t < 5; ++t) stat_own(L, to[t] - 1, m, 1.0f, rstd[t], mr[t]);
      bcast(a0[0], cs0); bcast(a1[0], cs1);
      SB();
#pragma unroll
      for (int t = 0; t < 5; ++t) {
        *(LAS v2u*)(A + to[t] * 512) = fix(a0[t], cs0, rstd[t], mr[t]);
        if (t < 3) *(LAS v2u*)(E + to[t] * 512) = fix(a1[t], cs1, rstd[t], mr[t]);
      }
      wo_req(rw, j, hl, voff);
      if constexpr (Ly == 0 && !JR1) sfor<9, 18>([&](auto sc) { jh.template at<sc.value>(); });
    }
  };
  {
    FRESH();
    const int r8 = lane >> 3, q8 = lane & 7;
    JobHook jh;
    jh.rs = rs; jh.lane = lane; jh.d1 = jh.d2 = jh.dc = WS_BYTES;
    u64 gen = 0;
    {
      asm volatile("global_load_dword v%c2, %0, %1" :: "v"(((2 * (64 * (c >> 1) + 8 * j + w) + (c & 1)) * D_ + min(lane, 8) * 64) * 2), "s"(xin), "i"(RB - 1));
    }
    auto jobs = [&](int q0) {
      const int q = min(q0, 161), s = (q * 7282) >> 16, B = q - 9 * s;
      const u16* p1 = w1 + ((32 * s + r8) * F_ + 576 * j + 64 * B + 8 * q8);
      const u16* p2 = w2 + ((576 * j + 32 * s + r8) * D_ + 64 * B + 8 * q8);
#pragma unroll
      for (int i = 0; i < 4; ++i) jh.f[i] = *(const v4u*)(p1 + i * 8 * F_);
#pragma unroll
      for (int i = 0; i < 4; ++i) jh.f[4 + i] = *(const v4u*)(p2 + i * 8 * D_);
      if (q0 < 162) {
        jh.d1 = W1_OFF + (36 * j + 4 * B) * REV + (s < 16 ? (s - 2 * j) & 15 : s) * 1024;
        jh.d2 = W2_OFF + (36 * j + 4 * B) * REV + s * 1024;
        jh.dc = CSP_OFF + (j * 18 + s) * 2304 + B * 256;
      }
      SB();
    };
    if (w >= 5) {
      if (c < 18) {
        const int sec = w - 5;
        const u16* src = wqkv + (32 * c) * (3 * D_) + 72 * j;
        LAS char* scr = L + L_ATT + 32768 + sec * 4736;
        v4u qr[5], jo[4];
#pragma unroll
        for (int i = 0; i < 5; ++i) {
          const int ch = min(64 * i + lane, 287), row = (ch * 7282) >> 16;
          qr[i] = *(const v4u*)(src + row * (3 * D_) + sec * D_ + (ch - 9 * row) * 8);
        }
        {
          typedef unsigned v4u8 __attribute__((ext_vector_type(4), aligned(8)));
          const u16* ob = sec == 2 ? src : wo;
          const int e0 = (lane >> 2) * (3 * D_) + min(lane & 3, 2) * D_ + 16;
          const int ra = 36 * c + r8, rb = 36 * (8 * (c - 16) + 2 * (r8 >> 2)) + 16 + (r8 & 3), o0 = (c < 16 ? ra : rb) * D_ + 64 * (sec ? 8 : j) + 8 * q8;
          const int v0 = sec == 2 ? e0 : o0;
          const int s1 = sec == 2 ? 16 * 3 * D_ : (c < 16 ? 8 : 144) * D_, s2 = sec == 2 ? 36 : (c < 16 ? 20 : 36) * D_;
#pragma unroll
          for (int i = 0; i < 4; ++i) jo[i] = *(const v4u8*)(ob + (v0 + (i & 1) * s1 + (i >> 1) * s2));
        }
        SB();
        gen = __hip_atomic_load((gu64*)(ws + FL_OFF), RLX_AGENT);
        SB();
        PST(1);
        bar();
#pragma unroll
        for (int i = 0; i < 5; ++i) *(LAS v4u*)(scr + min(64 * i + lane, 287) * 16) = qr[i];
        const int qd = WQ_OFF + 16 * j * REV + (sec >> 1) * 2 * REV + (2 * c + (sec & 1)) * 1024;
#pragma unroll
        for (int h = 0; h < 4; ++h) {
          const LAS char* p = scr + (4 * g + (m >> 2)) * 144 + (h >> 1) * 72 + (h & 1) * 40 + (m & 3) * 8;
          const v2u lo = trread(p), hi = trread(p + 16 * 144);
          stc128(v4u{lo[0], lo[1], hi[0], hi[1]}, rs, voff, qd + h * 4 * REV);
        }
        if (sec == 2) {
#pragma unroll
          for (int i = 0; i < 4; ++i) *(LAS v2u*)(scr + i * 512 + lane * 8) = (lane & 3) == 3 ? v2u{0u, 0u} : v2u{jo[i][0], jo[i][1]};
#pragma unroll
          for (int p = 0; p < 2; ++p) {
            const v2u lo = trread(scr + p * 1024 + lane * 8), hi = trread(scr + p * 1024 + 512 + lane * 8);
            jo[2 * p] = v4u{lo[0], lo[1], hi[0], hi[1]}; jo[2 * p + 1] = jo[2 * p];
          }
        } else jtr<false>(jo, scr, lane);
        const int od = sec == 2 ? qd + 1024 : WO_OFF + 4 * (sec ? 8 : j) * REV + c * 1024, os = sec == 2 ? 4 * REV : REV;
#pragma unroll
        for (int t = 0; t < 4; ++t) stc128(jo[t], rs, voff, od + t * os);
        PST(2);
        arrive<3>(ws, L, 9, 320 + j, c, gen + 1, lane);
        lds_wait_ge<1>(L, 9, 3u);
        PST(3);
        jobs(112 + c * 3 + sec);
      } else {
        bar();
        gen = __hip_atomic_load((gu64*)(ws + FL_OFF), RLX_AGENT);
        asm volatile("" : "+v"(gen));
        __builtin_amdgcn_s_sleep(PVS);
        jobs((c - 18) * 8 + w);
      }
    } else {
      if (tid < 32) *(LAS unsigned*)(L + L_W + tid * 4) = 0u;
      bar();
      asm volatile("s_waitcnt vmcnt(0)");
      PST(1);
      const int cc = (w == 0 && c >= 2) ? c - 2 : c, tt = w == 0 ? 3 : w - 1;
      const u16* src = xin + ((2 * (64 * (cc >> 1) + 16 * tt + r8) + (cc & 1)) * D_ + 8 * q8);
      v4u x[2][9];
#pragma unroll
      for (int i = 0; i < 9; ++i) { x[0][i] = *(const v4u*)(src + 64 * i); x[1][i] = *(const v4u*)(src + 16 * D_ + 64 * i); }
      SB();
      PST(2);
      float s1[2] = {0.f, 0.f}, s2[2] = {0.f, 0.f};
      LAS char* dst = L + L_X + w * REV + (q8 >> 2) * 1024 + ((q8 & 1) * 32 + r8) * 16 + ((q8 >> 1) & 1) * 8;
#pragma unroll
      for (int i = 0; i < 9; ++i) {
#pragma unroll
        for (int h = 0; h < 2; ++h) {
          *(LAS v2u*)(dst + i * 2048 + h * 128) = v2u{x[h][i][0], x[h][i][1]};
          *(LAS v2u*)(dst + i * 2048 + h * 128 + 256) = v2u{x[h][i][2], x[h][i][3]};
        }
        SB();
      }
      PST(3);
      if (c >= 18) jobs((c - 18) * 8 + w);
#pragma unroll
      for (int i = 0; i < 9; ++i) { stat2(x[0][i], s1[0], s2[0]); stat2(x[1][i], s1[1], s2[1]); }
      if (w == 0) ones_row(L, lane);
#pragma unroll
      for (int h = 0; h < 2; ++h) { s1[h] = red8sum(s1[h]); s2[h] = red8sum(s2[h]); }
      if (q8 < 2) {
        const f32x4 p = {q8 ? s1[1] : s1[0], q8 ? s2[1] : s2[0], 0.f, 0.f}, z = {0.f, 0.f, 0.f, 0.f};
        if (w) *(LAS f32x4*)(L + L_SP + (w - 1) * 256 + (q8 * 8 + r8) * 16) = p;
        else { LAS char* d = L + L_SH + (q8 * 8 + r8) * 48; *(LAS f32x4*)d = p; *(LAS f32x4*)(d + 16) = z; *(LAS f32x4*)(d + 32) = z; }
      }
    }
    PST(4);
    jh.cs = jtr<true>(jh.f, L + L_ATT + w * 4096, lane);
    jtr<false>(jh.f + 4, L + L_ATT + w * 4096, lane);
    if (w == 6) {
      unsigned bv[5];
#pragma unroll
      for (int i = 0; i < 5; ++i) bv[i] = *(const unsigned*)(b1 + 576 * j + min(lane + 64 * i, 287) * 2);
      const unsigned b2v = *(const unsigned*)(b2 + (lane < 32 ? 64 * j + 2 * lane : 512 + 32 * (j >> 2) + 2 * (lane & 15)));
#pragma unroll
      for (int i = 0; i < 5; ++i) *(LAS f32x2*)(L + L_B1 + min(lane + 64 * i, 287) * 8) = f32x2{AL * bflo(bv[i]), AL * bfhi(bv[i])};
      if (lane < 48) *(LAS f32x2*)(L + L_B2 + lane * 8) = f32x2{bflo(b2v), bfhi(b2v)};
    }
    if (w == 7) {
      poll<1>(flagp(ws, 320 + j, min(lane, 17)), gen + 1);
      if (lane == 0) *(LAS u64*)(L + L_W + 48) = gen + 1;
    }
    PST(5);
    bar();
    PST(6);
    fillr<0, 8>(rw, mkQ(j, hl, role), voff);
    tag = *(LAS u64*)(L + L_W + 48);
    tag = ((u64)(unsigned)__builtin_amdgcn_readfirstlane((unsigned)(tag >> 32)) << 32) | (unsigned)__builtin_amdgcn_readfirstlane((unsigned)tag);
    qkv(std::integral_constant<int, 0>{}, jh);
    PST(7);
    PSTOUT();
  }

#pragma unroll 1
  for (int Ly = 0; Ly < 2; ++Ly) {
    CST(2 + 32 * Ly);
    bar();
    CST(3 + 32 * Ly);
    STAMP(3 + 16 * Ly);
    {
      FRESH();
      const LAS char* A = L + L_ATT + 2 * role * ATT_STR + hl * 512 + lane * 8;
      const LAS char* Ee = L + L_ATT + 2 * role * ATT_STR + hl * 512 + A_E + m * 32;
      v2u qm[2], km[2][2], vm[2][2], em[2], xk[2];
#pragma unroll
      for (int q = 0; q < 2; ++q) {
        qm[q] = *(const LAS v2u*)(A + q * ATT_STR + A_Q + 512);
        xk[q] = *(const LAS v2u*)(Ee + q * 512 + 8);
#pragma unroll
        for (int i = 0; i < 2; ++i) km[q][i] = *(const LAS v2u*)(A + q * ATT_STR + A_K + i * 512);
      }
      const v2u xq = *(const LAS v2u*)(Ee + 512);
#pragma unroll
      for (int q = 0; q < 2; ++q) {
        em[q] = trread(A + A_E + q * 512);
#pragma unroll
        for (int i = 0; i < 2; ++i) vm[q][i] = trread(A + q * ATT_STR + A_V + i * 512);
      }
      const float sc = 0.23570226039551584f * 1.4426950408889634f, NEG = -3.0e38f;
      f32x4 bp, bc;
#pragma unroll
      for (int r = 0; r < 4; ++r) {
        const int dk = m - (4 * g + r);
        bp[r] = (dk <= -9 && !(c < 2 && hl == 0)) ? 0.f : NEG;
        bc[r] = (unsigned)dk <= 7u ? 0.f : NEG;
      }
      SB();
      const f32x4 z = {0.f, 0.f, 0.f, 0.f};
      f32x4 sp[2], sq[2];
#pragma unroll
      for (int q = 0; q < 2; ++q) {
        const v4u qf = {qm[q][0], qm[q][1], g == 0 ? xq[q] : 0u, 0u};
        sp[q] = mfma(v4u{km[q][0][0], km[q][0][1], xk[0][q], 0u}, qf, z);
        sq[q] = mfma(v4u{km[q][1][0], km[q][1][1], xk[1][q], 0u}, qf, z);
      }
      float mx[2], sum[2];
#pragma unroll
      for (int q = 0; q < 2; ++q) {
        sp[q] = sp[q] * sc + bp; sq[q] = sq[q] * sc + bc;
        mx[q] = fmaxf(fmaxf(fmaxf(sp[q][0], sp[q][1]), fmaxf(sp[q][2], sp[q][3])), fmaxf(fmaxf(sq[q][0], sq[q][1]), fmaxf(sq[q][2], sq[q][3])));
      }
#pragma unroll
      for (int q = 0; q < 2; ++q) mx[q] = red16max(mx[q]);
#pragma unroll
      for (int q = 0; q < 2; ++q) {
        sp[q] = sp[q] - mx[q]; sq[q] = sq[q] - mx[q];
#pragma unroll
        for (int r = 0; r < 4; ++r) { sp[q][r] = __builtin_amdgcn_exp2f(sp[q][r]); sq[q][r] = __builtin_amdgcn_exp2f(sq[q][r]); }
        const f32x4 t4 = sp[q] + sq[q];
        sum[q] = (t4[0] + t4[1]) + (t4[2] + t4[3]);
      }
#pragma unroll
      for (int q = 0; q < 2; ++q) sum[q] = __builtin_amdgcn_rcpf(red16sum(sum[q]));
      f32x4 o[2], oe[2];
#pragma unroll
      for (int q = 0; q < 2; ++q) {
        const v2u pp = pk4(sp[q] * sum[q]), pc = pk4(sq[q] * sum[q]);
        const v4u pf = {pp[0], pp[1], pc[0], pc[1]};
        o[q] = mfma(v4u{vm[q][0][0], vm[q][0][1], vm[q][1][0], vm[q][1][1]}, pf, z);
        oe[q] = mfma(v4u{em[0][0], em[0][1], em[1][0], em[1][1]}, pf, z);
      }
      const int ob = OB_OFF + c * XBC + hl * REV;
      const v2u oa = pk4(o[0]), oc = pk4(o[1]);
      stc128(v4u{oa[0], oa[1], oc[0], oc[1]}, rs, voff, ob + (2 * j + role) * 1024);
      if (g == 2) stc64(v2u{pk2(oe[0][0], oe[0][1]), pk2(oe[1][2], oe[1][3])}, rs, m * 8, ob + 16384 + (2 * j + role) * 128);
      SB();
      CST(4 + 32 * Ly);
      STAMP(4 + 16 * Ly);
      publish(ws, (5 * Ly + 0) * 32 + c, j, tag, tid);
      CST(5 + 32 * Ly);
      if (Ly == 0 && tid == 0) __hip_atomic_store(flagp(ws, 328 + j, c), tag, RLX_AGENT);
      if (role) {
        const unsigned la = (unsigned)(size_t)(L + L_X + hl * REV + voff), lb = (unsigned)(size_t)(L + L_X + 4 * REV + hl * 9216 + voff);
        sfor<0, 27>([&](auto ic) {
          constexpr int i = ic.value;
          rput<i, (i < 18 ? i : i - 18) * 1024>(i < 18 ? la : lb);
        });
      }
      if (Ly && role == 0) fillh<0>(rw, mkF(j, hl), voff);
    }
    {
      FRESH();
      const LAS char* wb = L + L_X + role * 9216 + voff;
      const LAS char* wb2 = wb + 3 * REV;
      asm volatile("" : "+v"(wb2));
      const bool ext = hl == (j & 3);
      v4u wf[2][4];
      auto wld = [&](int s) {
#pragma unroll
        for (int i = 0; i < 4; ++i) wf[s % 2][i] = *(const LAS v4u*)((i < 3 ? wb + i * REV : wb2) + s * 1024);
      };
      f32x4 acc[6];
      {
        const v4u ra = *(LAS v4u*)(L + L_RES + w * 1024 + voff), rb = *(LAS v4u*)(L + L_RES + 8192 + voff);
        acc[0] = unpk4(role ? 0u : ra[0], role ? 0u : ra[1]); acc[1] = unpk4(role ? 0u : ra[2], role ? 0u : ra[3]);
        acc[2] = unpk4(role ? ra[0] : 0u, role ? ra[1] : 0u); acc[3] = unpk4(role ? ra[2] : 0u, role ? ra[3] : 0u);
        acc[4] = unpk4(role ? 0u : rb[0], role ? 0u : rb[1]); acc[5] = unpk4(role ? 0u : rb[2], role ? 0u : rb[3]);
      }
      SB();
      CST(6 + 32 * Ly);
      await(ws, 5 * Ly + 0, c, c, tag, w, lane);
      CST(7 + 32 * Ly);
      STAMP(5 + 16 * Ly);
      v4u of[9];
      const int ob = OB_OFF + c * XBC + hl * REV + role * 9216;
#pragma unroll
      for (int s = 0; s < 7; ++s) of[s] = ldc(rs, voff + (s & 3) * 1024, ob + (s >> 2) * 4096);
      {
        const int va = role ? g * 256 + m * 8 : voff, vb = va + (role ? 128 : 8);
#pragma unroll
        for (int s = 7; s < 9; ++s) {
          const v2u lo = ldc64(rs, va + (s - 7) * 1024, ob + 7168), hi = ldc64(rs, vb + (s - 7) * 1024, ob + 7168);
          of[s] = v4u{lo[0], lo[1], hi[0], hi[1]};
        }
      }
      SB();
      wld(0); wld(1);
      SB();
#pragma unroll
      for (int s = 0; s < 9; ++s) {
#pragma unroll
        for (int i = 0; i < 4; ++i) acc[i] = mfma(wf[s % 2][i], of[s], acc[i]);
        SB();
        if (s + 2 < 9) { wld(s + 2); SB(); }
      }
      LAS char* R = L + L_X + 6 * REV + hl * 6144 + voff;
      *(LAS f32x4*)(R + role * 2048) = role ? acc[0] : acc[2];
      *(LAS f32x4*)(R + role * 2048 + 1024) = role ? acc[1] : acc[3];
      if (ext) {
#pragma unroll
        for (int s = 0; s < 9; ++s) {
          acc[4] = mfma(*(const LAS v4u*)(wb2 + REV + s * 1024), of[s], acc[4]);
          acc[5] = mfma(*(const LAS v4u*)(wb2 + 2 * REV + s * 1024), of[s], acc[5]);
        }
        if (role) { *(LAS f32x4*)(R + 4096) = acc[4]; *(LAS f32x4*)(R + 5120) = acc[5]; }
      }
      if (role == 0) {
        if (Ly == 0) { poll<1>(flagp(ws, 328 + j, lane & 31), tag); fillh<0>(rw, mkF(j, hl), voff); }
        else fillh<1>(rw, mkF(j, hl), voff);
      }
      if (Ly) CST(25 + 32);
      bar();
      {
        const f32x4 pa = *(LAS f32x4*)(R + 2048 - role * 2048), pb = *(LAS f32x4*)(R + 3072 - role * 2048);
        const v2u oa = pk4((role ? acc[2] : acc[0]) + pa), ob2 = pk4((role ? acc[3] : acc[1]) + pb);
        const int xb = X1_OFF + c * XBC + hl * REV;
        const v4u xo = {oa[0], oa[1], ob2[0], ob2[1]};
        stc128(xo, rs, voff, xb + (2 * j + role) * 1024);
        SB();
        *(LAS v4u*)(L + L_X + hl * REV + role * 1024 + voff) = xo;
        float s1 = 0.f, s2 = 0.f;
        stat2(xo, s1, s2);
        s1 = red16sum(s1); s2 = red16sum(s2);
        if (lane < 16) *(LAS f32x2*)(L + L_SP + 1024 + hl * 256 + lane * 16 + role * 8) = f32x2{s1, s2};
        if (ext && role == 0) {
          const v2u ea = pk4(acc[4] + *(LAS f32x4*)(R + 4096)), eb = pk4(acc[5] + *(LAS f32x4*)(R + 5120));
          stc128(v4u{ea[0], ea[1], eb[0], eb[1]}, rs, voff, xb + (16 + (j >> 2)) * 1024);
        }
      }
      STAMP(6 + 16 * Ly);
      if (Ly) CST(26 + 32);
      publish(ws, (5 * Ly + 1) * 32 + c, j, tag, tid);
      if (Ly) CST(27 + 32);
      if (role) {
        fillh<0>(rw, mkS(j, hl), voff);
        if (Ly == 0 && hl) {
          const int o4 = ((hl - 1) * 48 + min(lane, 47)) * 16;
          v4u v[18];
#pragma unroll
          for (int s = 0; s < 18; ++s) v[s] = ldc(rs, o4, CSP_OFF + (j * 18 + s) * 2304);
          SB();
          f32x4 a = __builtin_bit_cast(f32x4, v[0]);
#pragma unroll
          for (int s = 1; s < 18; ++s)
#pragma unroll
            for (int r = 0; r < 4; ++r) { const unsigned u = v[s][r]; a[r] = sadd(a[r], __uint_as_float(u)); }
          *(LAS f32x4*)(L + L_CS1 + o4) = a;
        }
      }
    }
    {
      FRESH();
      const int pbc = PB_OFF + c * (64 * 9216);
      const StreamQ wq0 = mkQ(j, hl, role);
      auto stats = [&](float (&rstd)[4], float (&mr)[4]) {
#pragma unroll
        for (int tt = 0; tt < 4; ++tt) stat_own4(L, tt, m, AL, rstd[tt], mr[tt]);
      };
      auto gtile = [&](auto pkc, f32x4 (&x)[4], const float (&rstd)[4], const float (&mr)[4], int T, v2u (&o)[4]) {
        const f32x4 cs = *(LAS f32x4*)(L + L_CS1 + T * 64 + g * 16), bb = *(LAS f32x4*)(L + L_B1 + T * 64 + g * 16);
        if constexpr (pkc.value) gelu16p(x, rstd, mr, cs, bb, o); else gelu16(x, rstd, mr, cs, bb, o);
      };
      auto hpair = [&](auto pk0, auto pk1, f32x4 (&x)[4], f32x4 (&y)[4], const float (&rstd)[4], const float (&mr)[4], int T, auto&& mid) {
        v2u oa[4], ob[4];
        gtile(pk0, x, rstd, mr, T, oa);
        mid();
        gtile(pk1, y, rstd, mr, T + 1, ob);
#pragma unroll
        for (int tt = 0; tt < 4; ++tt) *(LAS v4u*)(L + L_H + tt * REV + (T >> 1) * 1024 + voff) = v4u{oa[tt][0], oa[tt][1], ob[tt][0], ob[tt][1]};
        SB();
      };
      auto poff = [&](int ch) { const int d = 2 * hl + ch; return pbc + (d != j ? (d * 8 + j) * 9216 + role * 4096 : (role * 32 + j) * 9216 + 8192); };
      auto pstr = [&](int ch) { return 2 * hl + ch != j ? 1024 : 8 * 9216; };
      auto ppack = [&](f32x4 x, f32x4 y) { const v2u a = pk4(x), b = pk4(y); return v4u{a[0], a[1], b[0], b[1]}; };
      auto pstore2 = [&](f32x4 (&x)[4], f32x4 (&y)[4], int ch, auto slc) {
#pragma unroll
        for (int q = 0; q < 4; ++q) {
          stc128(ppack(x[q], y[q]), rs, voff, poff(ch) + q * pstr(ch));
          if (slc.value) { SB(); __builtin_amdgcn_s_sleep(slc.value); SB(); }
        }
        SB();
      };
      constexpr std::integral_constant<int, 0> I0{};
      constexpr std::integral_constant<int, 1> I1{};
      auto base = [&](f32x4& a, f32x4& b, int ks, int tt, int u) {
        a = *(LAS f32x4*)(L + L_B2 + (u >> 2) * 128 + g * 16); b = *(LAS f32x4*)(L + L_B2 + (u >> 2) * 128 + 64 + g * 16);
        acc8(a, b, *(const LAS v4u*)(L + L_X + tt * REV + (u < 8 ? u >> 2 : ks) * 1024 + voff));
        if (u < 8) acc8(a, b, *(const LAS v4u*)(L + L_OWN + u * 1024 + voff));
      };
      auto finish = [&](auto nc, f32x4 a, f32x4 b, int ks, int tt, int u, auto&& mid) {
        constexpr int N = nc.value;
        v4u pv[N];
        const int po = pbc + j * 8 * 9216 + u * 1024;
#pragma unroll
        for (int i = 0; i < N; ++i) pv[i] = ldc(rs, voff, po + (i + ((N == 7 && i >= j) ? 1 : 0)) * 9216);
        SB();
        mid(a, b);
#pragma unroll
        for (int i = 0; i < N; ++i) acc8(a, b, pv[i]);
        const v4u o = ppack(a, b);
        if (Ly == 0) stc128(o, rs, voff, X2_OFF + c * XBC + tt * REV + ks * 1024);
        else {
          u16* p = out + (size_t)(2 * (64 * (c >> 1) + 16 * tt + m) + (c & 1)) * D_ + 32 * ks + 4 * g;
          *(v2u*)p = v2u{o[0], o[1]}; *(v2u*)(p + 16) = v2u{o[2], o[3]};
        }
      };
      auto nomid = [](f32x4&, f32x4&) {};
      if (role == 0) {
        const StreamF mb = mkF(j, hl);
        __builtin_amdgcn_s_setprio(1);
        {
          f32x4 acc[4][4];
          seg<4, 4, 0, 2, 0, R_ / 2, true>({acc[0], acc[1], acc[2], acc[3]}, L + L_X + voff, rw, mb, voff);
          if (Ly == 0) fillh<1>(rw, mb, voff);
          fillr<R_, 8>(rw, mb, voff);
          if (Ly) CST(28 + 32);
          if (hl == 0) {
            poll8<0>(ws, 5 * Ly + 1, c, c, tag, lane);
            *(volatile LAS unsigned*)(L + L_W + 60) = Ly + 1;
            if (Ly == 0 && blockIdx.x == 0 && lane == 0) __hip_atomic_store((gu64*)(ws + FL_OFF), tag, RLX_AGENT);
          } else lds_wait_ge<1>(L, 15, Ly + 1);
          if (Ly) CST(29 + 32);
          STAMP(7 + 16 * Ly);
          stage8(rs, X1_OFF + c * XBC, L, j, w, lane);
          if (Ly) CST(30 + 32);
          lds_wait_ge<0>(L, 16, 4 * (Ly + 1));
          STAMP(8 + 16 * Ly);
          CST(8 + 32 * Ly);
          seg<4, 4, 2, 18, 8, 72, false>({acc[0], acc[1], acc[2], acc[3]}, L + L_X + voff, rw, mb, voff, WordHook{L, 4u * (Ly + 1), hl});
          CST(9 + 32 * Ly);
          float rstd[4], mr[4];
          stats(rstd, mr);
          constexpr int Q4 = R_ / 4;
          hpair(I0, I0, acc[0], acc[1], rstd, mr, 4 * hl, [&] { fillr<72, Q4>(rw, mb, voff); });
          fillr<72 + Q4, Q4>(rw, mb, voff);
          hpair(I0, std::integral_constant<int, F4PK>{}, acc[2], acc[3], rstd, mr, 4 * hl + 2, [&] { fillr<72 + 2 * Q4, Q4>(rw, mb, voff); });
          fillr<72 + 3 * Q4, R_ - 3 * Q4>(rw, mb, voff);
        }
        CST(10 + 32 * Ly);
        if (EARLY) bar();
        CST(11 + 32 * Ly);
        {
          f32x4 acc[4][4];
          if constexpr (EARLY > 0) seg<4, 4, 0, EARLY ? EARLY : 1, 72, 162, true>({acc[0], acc[1], acc[2], acc[3]}, L + L_H + voff, rw, mb, voff);
          CST(12 + 32 * Ly);
          bar();
          CST(13 + 32 * Ly);
          seg<2, 4, EARLY, 18, StreamF::P1, 162, EARLY == 0>({acc[0], acc[1]}, L + L_H + voff, rw, mb, voff);
          pstore2(acc[0], acc[1], 0, std::integral_constant<int, TRICKLE>{});
          CST(20 + 32 * Ly);
          seg<2, 4, EARLY, 18, StreamF::P1 + StreamF::N2, 162, EARLY == 0>({acc[2], acc[3]}, L + L_H + voff, rw, mb, voff);
          __builtin_amdgcn_s_setprio(0);
          pstore2(acc[2], acc[3], 1, std::integral_constant<int, TRICKLE>{});
          CST(14 + 32 * Ly);
        }
        arrive<4>(ws, L, 0, (5 * Ly + 2) * 32 + c, j, tag, lane);
        CST(15 + 32 * Ly);
        lds_wait_ge<3>(L, 4 + hl, Ly + 1);
        CST(18 + 32 * Ly);
        f32x4 own[4];
        seg<1, 4, 0, 18, 144, 162, true>({own}, L + L_H + voff, rw, mb, voff);
#pragma unroll
        for (int tt = 0; tt < 4; ++tt) *(LAS v2u*)(L + L_OWN + ((hl >> 1) * 4 + tt) * 1024 + voff + 8 * (hl & 1)) = pk4(own[tt]);
        asm volatile("s_waitcnt lgkmcnt(0)" ::: "memory");
        if (lane == 0) __hip_atomic_fetch_add((LAS unsigned*)(L + L_W + 44), 1u, __ATOMIC_RELAXED, __HIP_MEMORY_SCOPE_WORKGROUP);
        CST(17 + 32 * Ly);
        lds_wait_ge<1>(L, 2, Ly + 1);
        CST(16 + 32 * Ly);
        f32x4 sa, sb;
        finish(std::integral_constant<int, 7>{}, sa, sb, 2 * j, hl, hl,
               [&](f32x4& a, f32x4& b) { lds_wait_ge<1>(L, 11, 4 * (Ly + 1)); base(a, b, 2 * j, hl, hl); });
        if (hl == 3) {
          base(sa, sb, 16 + (j >> 2), j & 3, 8);
          if (j >= 4) lds_wait_ge<1>(L, 3, Ly + 1);
          finish(std::integral_constant<int, 8>{}, sa, sb, 16 + (j >> 2), j & 3, 8, nomid);
        }
      }
      int role2 = role;
      asm volatile("" : "+s"(role2));
      if (role2 != 0) {
        const StreamS mb = mkS(j, hl);
        {
          f32x4 acc[5][4];
          seg<5, 4, 0, 2, 0, R_ / 2, true>({acc[0], acc[1], acc[2], acc[3], acc[4]}, L + L_X + voff, rw, mb, voff);
          if (Ly) CST(28 + 32);
          lds_wait_ge<1>(L, 15, Ly + 1);
          if (Ly) CST(29 + 32);
          stage8(rs, X1_OFF + c * XBC, L, j, w, lane);
          if (Ly) CST(30 + 32);
          lds_wait_ge<1>(L, 16, 4 * (Ly + 1));
          lds_wait_ge<1>(L, 17, 4 * (Ly + 1));
          float rstd[4], mr[4];
          stats(rstd, mr);
          lds_wait_ge<2>(L, 20 + hl, 4 * (Ly + 1));
          fillr<R_ / 2, R_ / 2 + 10>(rw, mb, voff);
          CST(8 + 32 * Ly);
          seg<5, 4, 2, 18, 10, 90, false>({acc[0], acc[1], acc[2], acc[3], acc[4]}, L + L_X + voff, rw, mb, voff);
          CST(9 + 32 * Ly);
          if (EARLY) bar();
          CST(11 + 32 * Ly);
          fillr<90, R_ / 4>(rw, mb, voff);
          hpair(std::integral_constant<int, G1PK>{}, std::integral_constant<int, G2PK>{}, acc[0], acc[1], rstd, mr, 16 + 2 * hl, [] {});
          CST(10 + 32 * Ly);
          fillr<90 + R_ / 4, R_ / 4>(rw, mb, voff);
          v2u o[4];
          gtile(I1, acc[4], rstd, mr, 32 + hl, o);
          fillr<90 + 2 * (R_ / 4), R_ / 4>(rw, mb, voff);
          hpair(I1, I1, acc[2], acc[3], rstd, mr, 24 + 2 * hl, [] {});
          fillr<90 + 3 * (R_ / 4), R_ - 3 * (R_ / 4)>(rw, mb, voff);
#pragma unroll
          for (int tt = 0; tt < 4; ++tt) *(LAS v2u*)(L + L_H + tt * REV + (16 + (hl >> 1)) * 1024 + voff + 8 * (hl & 1)) = o[tt];
          CST(12 + 32 * Ly);
        }
        bar();
        CST(13 + 32 * Ly);
        {
          f32x4 acc[2][4];
          seg<2, 4, 0, 18, 90, 162, true>({acc[0], acc[1]}, L + L_H + voff, rw, mb, voff);
          CST(20 + 32 * Ly);
          __builtin_amdgcn_s_setprio(1);
          StoreHook hk{{ppack(acc[0][0], acc[1][0]), ppack(acc[0][1], acc[1][1]), ppack(acc[0][2], acc[1][2]), ppack(acc[0][3], acc[1][3])}, rs, voff, poff(0), pstr(0)};
          seg<2, 4, 0, 18, 126, 162, true>({acc[0], acc[1]}, L + L_H + voff, rw, mb, voff, hk);
          *(volatile LAS unsigned*)(L + L_W + 16 + hl * 4) = Ly + 1;
          CST(23 + 32 * Ly);
          pstore2(acc[0], acc[1], 1, I0);
          CST(14 + 32 * Ly);
        }
        u64 fv = 0;
        if (hl == 1) fv = __hip_atomic_load(flagp(ws, (5 * Ly + 2) * 32 + c, lane & 7), RLX_AGENT);
        arrive<4>(ws, L, 1, (5 * Ly + 3) * 32 + c, j, tag, lane);
        if (hl == 1) {
          if (!__all(fv == tag)) poll8<0>(ws, 5 * Ly + 2, c, c, tag, lane);
          *(volatile LAS unsigned*)(L + L_W + 8) = Ly + 1;
        }
        CST(15 + 32 * Ly);
        if (Ly == 0) fillh<0>(rw, wq0, voff);
        lds_wait_ge<1>(L, 11, 4 * (Ly + 1));
        f32x4 sa, sb;
        base(sa, sb, 2 * j + 1, hl, 4 + hl);
        SB();
        CST(17 + 32 * Ly);
        if (hl == 0) { poll8<0>(ws, 5 * Ly + 3, c, c, tag, lane); *(volatile LAS unsigned*)(L + L_W + 12) = Ly + 1; }
        else lds_wait_ge<1>(L, 3, Ly + 1);
        CST(16 + 32 * Ly);
        finish(std::integral_constant<int, 7>{}, sa, sb, 2 * j + 1, hl, 4 + hl, nomid);
        __builtin_amdgcn_s_setprio(0);
      }
      CST(22 + 32 * Ly);
      { const int tid = threadIdx.x ^ 256; STAMP(12 + 16 * Ly); }
      if (Ly == 0) {
        const int cprev = c >= 2 ? c - 2 : c;
        arrive<8>(ws, L, 14, 4 * 32 + c, j, tag, lane);
        CST(19);
        if (role == 0) { lds_wait_ge<2>(L, 14, 8); fillh<0>(rw, wq0, voff); }
        if (role) fillh<1>(rw, wq0, voff);
        CST(21);
        await(ws, 4, c, cprev, tag, w, lane);
        CST(31);
        STAMP(17);
        stage<true>(rs, X2_OFF + c * XBC, X2_OFF + cprev * XBC + 3 * REV, L, w, lane);
        if (role == 0) fillh<1>(rw, wq0, voff);
        bar();
        NoHook nh;
        qkv(std::integral_constant<int, 1>{}, nh);
      }
    }
  }
}

extern "C" void kernel_launch(void* const* d_in, const int* in_sizes, int n_in, void* d_out, int out_size, void* d_ws,
                              size_t ws_size, hipStream_t stream) {
  const u16* x = (const u16*)d_in[0];
  const u16* wqkv = (const u16*)d_in[1];
  const u16* wo = (const u16*)d_in[2];
  const u16* w1 = (const u16*)d_in[4];
  const u16* b1 = (const u16*)d_in[5];
  const u16* w2 = (const u16*)d_in[6];
  const u16* b2 = (const u16*)d_in[7];
  (void)hipFuncSetAttribute((const void*)k_main, hipFuncAttributeMaxDynamicSharedMemorySize, LDS_BYTES);
  k_main<<<256, 512, LDS_BYTES, stream>>>((char*)d_ws, x, wqkv, wo, w1, b1, w2, b2, (u16*)d_out);
}
```

```cpp
#include <hip/hip_runtime.h>
#include <hip/hip_bf16.h>
#include <type_traits>

#define GAS __attribute__((address_space(1)))
#define LAS __attribute__((address_space(3)))
typedef unsigned short u16;
typedef unsigned v4u __attribute__((ext_vector_type(4)));
typedef unsigned v2u __attribute__((ext_vector_type(2)));
typedef float f32x4 __attribute__((ext_vector_type(4)));
typedef float f32x2 __attribute__((ext_vector_type(2)));
typedef short bf16x8 __attribute__((ext_vector_type(8)));
typedef __bf16 bf16x2 __attribute__((ext_vector_type(2)));
typedef GAS unsigned gu32;
typedef unsigned long long u64;
typedef GAS u64 gu64;
typedef __amdgpu_buffer_rsrc_t rsrc_t;
#define RLX_AGENT __ATOMIC_RELAXED, __HIP_MEMORY_SCOPE_AGENT

#define TRICKLE 4
#define G2PK 0
#define GOS 8
#define JR1 1
#define PV 3
#define PVS 1
#define EARLY 8
#define G1PK 0
#define F4PK 0
#define FALL4 0
#define WOF 1
#define QP 1
#define GSPREAD 2

constexpr int S_ = 2048, D_ = 576, F_ = 4608;
constexpr int REV = 18 * 1024;
constexpr int XBC = 4 * REV;
constexpr int XBSZ = 32 * XBC;
constexpr int FL_OFF = 0;
constexpr int WQ_OFF = 131072;
constexpr int WO_OFF = WQ_OFF + 128 * REV;
constexpr int W1_OFF = WO_OFF + 64 * REV;
constexpr int W2_OFF = W1_OFF + 288 * REV;
constexpr int CSP_OFF = W2_OFF + 288 * REV;
constexpr int X2_OFF = CSP_OFF + 8 * 18 * 2304 + 4096 - (8 * 18 * 2304) % 4096;
constexpr int OB_OFF = X2_OFF + XBSZ;
constexpr int X1_OFF = OB_OFF + XBSZ;
constexpr int PB_OFF = X1_OFF + XBSZ;
constexpr int PBSZ = 32 * 8 * 8 * 9216;
constexpr int WS_BYTES = PB_OFF + PBSZ;
constexpr int L_X = 0, L_H = 73728, L_ATT = 92160, ATT_STR = 10240, A_Q = 0, A_K = 2560, A_V = 5120, A_E = 7680;
constexpr int L_RES = 137216, L_OWN = 147456, L_CS1 = L_OWN + 8192, L_B1 = L_CS1 + 2304, L_SP = L_B1 + 2304,
              L_SH = L_SP + 2048, L_B2 = L_SH + 768, L_W = L_B2 + 384, L_END = L_W + 128;
static_assert(L_END <= 163840, "lds");
constexpr int LDS_BYTES = 163840;
constexpr int CODE_SHARE = 2176;

__device__ __forceinline__ float bflo(unsigned u) { return __uint_as_float(u << 16); }
__device__ __forceinline__ float bfhi(unsigned u) { return __uint_as_float(u & 0xffff0000u); }
__device__ __forceinline__ unsigned pk2(float a, float b) {
  f32x2 v = {a, b};
  return __builtin_bit_cast(unsigned, __builtin_convertvector(v, bf16x2));
}
__device__ __forceinline__ v2u pk4(f32x4 v) { return v2u{pk2(v[0], v[1]), pk2(v[2], v[3])}; }
__device__ __forceinline__ f32x4 unpk4(unsigned a, unsigned b) { return f32x4{bflo(a), bfhi(a), bflo(b), bfhi(b)}; }
__device__ __forceinline__ f32x4 mfma(v4u a, v4u b, f32x4 c) {
  return __builtin_amdgcn_mfma_f32_16x16x32_bf16(__builtin_bit_cast(bf16x8, a), __builtin_bit_cast(bf16x8, b), c, 0, 0, 0);
}
__device__ __forceinline__ v4u ldc(rsrc_t r, int voff, int soff) { return __builtin_amdgcn_raw_buffer_load_b128(r, voff, soff, 16); }
__device__ __forceinline__ void stc128(v4u v, rsrc_t r, int voff, int soff) {
  __builtin_amdgcn_raw_buffer_store_b128(v, r, voff, soff, 16);
  asm volatile("s_nop 1" :: "v"(v));
}
__device__ __forceinline__ v2u ldc64(rsrc_t r, int voff, int soff) { return __builtin_amdgcn_raw_buffer_load_b64(r, voff, soff, 16); }
__device__ __forceinline__ void stc64(v2u v, rsrc_t r, int voff, int soff) { __builtin_amdgcn_raw_buffer_store_b64(v, r, voff, soff, 16); }
__device__ __forceinline__ void stc32(unsigned v, rsrc_t r, int voff, int soff) { __builtin_amdgcn_raw_buffer_store_b32(v, r, voff, soff, 16); }
#define SB() __builtin_amdgcn_sched_barrier(0)
__device__ __forceinline__ void bar() {
  asm volatile("s_waitcnt lgkmcnt(0)" ::: "memory");
  __builtin_amdgcn_s_barrier();
  asm volatile("" ::: "memory");
}
__device__ __forceinline__ float red16max(float x) {
  auto r = __builtin_amdgcn_permlane16_swap(__float_as_uint(x), __float_as_uint(x), false, false);
  x = fmaxf(__uint_as_float(r[0]), __uint_as_float(r[1]));
  auto q = __builtin_amdgcn_permlane32_swap(__float_as_uint(x), __float_as_uint(x), false, false);
  return fmaxf(__uint_as_float(q[0]), __uint_as_float(q[1]));
}
__device__ __forceinline__ float red16sum(float x) {
  auto r = __builtin_amdgcn_permlane16_swap(__float_as_uint(x), __float_as_uint(x), false, false);
  x = __uint_as_float(r[0]) + __uint_as_float(r[1]);
  auto q = __builtin_amdgcn_permlane32_swap(__float_as_uint(x), __float_as_uint(x), false, false);
  return __uint_as_float(q[0]) + __uint_as_float(q[1]);
}

__device__ __forceinline__ gu64* flagp(char* ws, int line, int i) { return (gu64*)(ws + FL_OFF + 256 + line * 256 + i * 8); }
__device__ __forceinline__ void publish(char* ws, int line, int i, u64 tag, int tid) {
  asm volatile("s_waitcnt vmcnt(0)" ::: "memory");
  bar();
  if (tid == 0) __hip_atomic_store(flagp(ws, line, i), tag, RLX_AGENT);
}
template <int SL> __device__ __forceinline__ void poll(gu64* p, u64 tag) {
#pragma unroll 1
  for (unsigned spins = 0; spins < (1u << 19); ++spins) {
    const u64 v = __hip_atomic_load(p, RLX_AGENT);
    if (__all(v == tag)) break;
    if (SL) __builtin_amdgcn_s_sleep(SL);
  }
  __builtin_amdgcn_fence(__ATOMIC_ACQUIRE, "wavefront");
}
template <int SL> __device__ __forceinline__ void poll8(char* ws, int ph, int c, int c2, u64 tag, int lane) {
  poll<SL>(flagp(ws, ph * 32 + ((lane & 8) ? c2 : c), lane & 7), tag);
}
__device__ __forceinline__ void await(char* ws, int ph, int c, int c2, u64 tag, int w, int lane) {
  if (w == 0) poll8<0>(ws, ph, c, c2, tag, lane);
  bar();
}

__device__ __forceinline__ float sfma(float a, float b, float c) { float d; asm("v_fma_f32 %0, %1, %2, %3" : "=v"(d) : "v"(a), "v"(b), "v"(c)); return d; }
__device__ __forceinline__ float sfnma(float a, float b, float c) { float d; asm("v_fma_f32 %0, -%1, %2, %3" : "=v"(d) : "v"(a), "v"(b), "v"(c)); return d; }
__device__ __forceinline__ float sfmas(float a, float s) { float d; asm("v_fma_f32 %0, %1, %2, %2" : "=v"(d) : "v"(a), "s"(s)); return d; }
__device__ __forceinline__ float ssqs(float a, float s) { float d; asm("v_fma_f32 %0, %1, %1, %2" : "=v"(d) : "v"(a), "s"(s)); return d; }
__device__ __forceinline__ float smul(float a, float b) { float d; asm("v_mul_f32 %0, %1, %2" : "=v"(d) : "v"(a), "v"(b)); return d; }
__device__ __forceinline__ float sadd(float a, float b) { float d; asm("v_add_f32 %0, %1, %2" : "=v"(d) : "v"(a), "v"(b)); return d; }
__device__ __forceinline__ void stat2(v4u x, float& s1, float& s2) {
  const bf16x2 ones = __builtin_bit_cast(bf16x2, 0x3f803f80u);
#pragma unroll
  for (int i = 0; i < 4; ++i) {
    const unsigned u = x[i];
    const bf16x2 v = __builtin_bit_cast(bf16x2, u);
    s1 = __builtin_amdgcn_fdot2_f32_bf16(v, ones, s1, false);
    s2 = __builtin_amdgcn_fdot2_f32_bf16(v, v, s2, false);
  }
}
__device__ __forceinline__ void stat_fin(float s1, float s2, float scale, float& rstd, float& mr) {
  const float inv = 1.0f / D_, mean = smul(s1, inv);
  const float var = fmaxf(sfnma(mean, mean, smul(s2, inv)), 0.f);
  float r = __builtin_amdgcn_rsqf(sadd(var, 1e-5f));
  asm volatile("s_nop 0" : "+v"(r));
  rstd = smul(r, scale);
  mr = smul(mean, rstd);
}
__device__ __forceinline__ void stat_own(const LAS char* L, int tt, int m, float scale, float& rstd, float& mr) {
  const f32x4 p = *(const LAS f32x4*)(L + L_SP + tt * 256 + m * 16);
  stat_fin(sadd(p[0], p[2]), sadd(p[1], p[3]), scale, rstd, mr);
}
__device__ __forceinline__ void stat_own4(const LAS char* L, int tt, int m, float scale, float& rstd, float& mr) {
  const f32x4 p = *(const LAS f32x4*)(L + L_SP + tt * 256 + m * 16), q = *(const LAS f32x4*)(L + L_SP + 1024 + tt * 256 + m * 16);
  stat_fin(sadd(sadd(p[0], p[2]), sadd(q[0], q[2])), sadd(sadd(p[1], p[3]), sadd(q[1], q[3])), scale, rstd, mr);
}
__device__ __forceinline__ void stat_halo(const LAS char* L, int m, float& rstd, float& mr) {
  const f32x4 p = *(const LAS f32x4*)(L + L_SH + m * 48), q = *(const LAS f32x4*)(L + L_SH + m * 48 + 16), r = *(const LAS f32x4*)(L + L_SH + m * 48 + 32);
  stat_fin(sadd(sadd(sadd(p[0], p[2]), sadd(q[0], q[2])), sadd(r[0], r[2])), sadd(sadd(sadd(p[1], p[3]), sadd(q[1], q[3])), sadd(r[1], r[3])), 1.0f, rstd, mr);
}
__device__ __forceinline__ void ones_row(LAS char* L, int lane) {
  if ((lane & 15) == 0) {
#pragma unroll
    for (int s = 0; s < 18; ++s) *(LAS v4u*)(L + L_X + s * 1024 + lane * 16) = v4u{0x3f803f80u, 0x3f803f80u, 0x3f803f80u, 0x3f803f80u};
  }
}
template <int CTRL> __device__ __forceinline__ float dppadd(float x) {
  return x + __uint_as_float(__builtin_amdgcn_update_dpp(0u, __float_as_uint(x), CTRL, 0xf, 0xf, true));
}
__device__ __forceinline__ float red8sum(float x) { return dppadd<0x141>(dppadd<0x4E>(dppadd<0xB1>(x))); }
__device__ __forceinline__ void stage(rsrc_t rs, int xo, int ho, LAS char* L, int kh, int tt, int lane) {
  const int voff = lane * 16, hw = kh * 3 + min(tt, 2);
  v4u x[9], h[3];
#pragma unroll
  for (int s = 0; s < 3; ++s) h[s] = ldc(rs, voff + s * 1024, ho + hw * 3072);
#pragma unroll
  for (int s = 0; s < 9; ++s) x[s] = ldc(rs, voff + (s & 3) * 1024, xo + tt * REV + kh * 9216 + (s >> 2) * 4096);
  SB();
  const v4u ones = {0x3f803f80u, 0x3f803f80u, 0x3f803f80u, 0x3f803f80u};
  float s1[3] = {0.f, 0.f, 0.f}, s2[3] = {0.f, 0.f, 0.f};
#pragma unroll
  for (int s = 0; s < 3; ++s) {
    *(LAS v4u*)(L + L_X + hw * 3072 + s * 1024 + voff) = (lane & 15) ? h[s] : ones;
    stat2(h[s], s1[2], s2[2]);
    SB();
  }
#pragma unroll
  for (int s = 0; s < 9; ++s) {
    *(LAS v4u*)(L + L_X + (tt + 1) * REV + kh * 9216 + s * 1024 + voff) = x[s];
    stat2(x[s], s1[s & 1], s2[s & 1]);
    SB();
  }
  const float a = red16sum(sadd(s1[0], s1[1])), b = red16sum(sadd(s2[0], s2[1])), ha = red16sum(s1[2]), hb = red16sum(s2[2]);
  if (lane < 16) {
    *(LAS f32x2*)(L + L_SP + tt * 256 + lane * 16 + kh * 8) = f32x2{a, b};
    *(LAS f32x2*)(L + L_SH + lane * 48 + hw * 8) = f32x2{ha, hb};
  }
  asm volatile("s_waitcnt lgkmcnt(0)" ::: "memory");
  if (lane == 0) __hip_atomic_fetch_add((LAS unsigned*)(L + L_W + 96 + kh * 4), 1u, __ATOMIC_RELAXED, __HIP_MEMORY_SCOPE_WORKGROUP);
}

__device__ __forceinline__ void stage8(rsrc_t rs, int xo, LAS char* L, int j, int w, int lane) {
  const int tt = w & 3, kh = w >> 2, voff = lane * 16;
  v4u x[8];
#pragma unroll
  for (int i = 0; i < 4; ++i) {
    const int q = 2 + 8 * kh + 2 * i, ks = q < 16 ? (q + 2 * j) & 15 : q;
    x[2 * i] = ldc(rs, voff, xo + tt * REV + ks * 1024);
    x[2 * i + 1] = ldc(rs, voff + 1024, xo + tt * REV + ks * 1024);
  }
  SB();
  float s1[2] = {0.f, 0.f}, s2[2] = {0.f, 0.f};
#pragma unroll
  for (int s = 0; s < 8; ++s) {
    *(LAS v4u*)(L + L_X + tt * REV + (2 + 8 * kh + s) * 1024 + voff) = x[s];
    stat2(x[s], s1[s & 1], s2[s & 1]);
    SB();
  }
  const float a = red16sum(s1[0] + s1[1]), b = red16sum(s2[0] + s2[1]);
  if (lane < 16) *(LAS f32x2*)(L + L_SP + tt * 256 + lane * 16 + kh * 8) = f32x2{a, b};
  asm volatile("s_waitcnt lgkmcnt(0)" ::: "memory");
  if (lane == 0) __hip_atomic_fetch_add((LAS unsigned*)(L + L_W + 64 + kh * 4), 1u, __ATOMIC_RELAXED, __HIP_MEMORY_SCOPE_WORKGROUP);
}

#define RING 28
constexpr int R_ = RING;
constexpr int RB = 256 - 4 * R_;
template <int SLOT, int OFF> __device__ __forceinline__ void rload(v4u rs, int voff, int so) {
  asm volatile("buffer_load_dwordx4 v[%c3:%c4], %0, %1, %2 offen offset:%c5 sc1"
               :: "v"(voff), "s"(rs), "s"(so), "i"(RB + 4 * SLOT), "i"(RB + 4 * SLOT + 3), "i"(OFF));
}
template <int SLOT, int OFF> __device__ __forceinline__ void rput(unsigned lds) {
  asm volatile("ds_write_b128 %0, v[%c1:%c2] offset:%c3" :: "v"(lds), "i"(RB + 4 * SLOT), "i"(RB + 4 * SLOT + 3), "i"(OFF) : "memory");
}
template <int N> __device__ __forceinline__ void rwait() { asm volatile("s_waitcnt vmcnt(%c0)" :: "i"(N)); }
template <int SLOT> __device__ __forceinline__ void rmfma(f32x4& acc, v4u x) {
  asm volatile("v_mfma_f32_16x16x32_bf16 %0, v[%c2:%c3], %1, %0" : "+v"(acc) : "v"(x), "i"(RB + 4 * SLOT), "i"(RB + 4 * SLOT + 3));
}
template <int SLOT> __device__ __forceinline__ void rmfma0(f32x4& acc, v4u x) {
  asm volatile("v_mfma_f32_16x16x32_bf16 %0, v[%c2:%c3], %1, 0" : "=&v"(acc) : "v"(x), "i"(RB + 4 * SLOT), "i"(RB + 4 * SLOT + 3));
}
template <int I, int N, class Fn> __device__ __forceinline__ void sfor(Fn&& f) {
  if constexpr (I < N) { f(std::integral_constant<int, I>{}); sfor<I + 1, N>(f); }
}
constexpr int cmin(int a, int b) { return a < b ? a : b; }
struct StreamQ { int b[1]; static constexpr int id(int) { return 0; } static constexpr int kb(int P) { return P; } };
struct StreamF {
  int b[4];
  static constexpr int E = FALL4 ? 18 : EARLY, P1 = 72 + 4 * E, N2 = FALL4 ? 2 : 2 * (18 - E);
  static constexpr int id(int P) { return P < 72 ? 0 : P < P1 ? 1 + ((P - 72) % 4) / 2 : P < 144 ? 1 + (P - P1) / N2 : 3; }
  static constexpr int kb(int P) { return P < 72 ? (P % 4) * 18 + P / 4 : P < P1 ? (P % 2) * 18 + (P - 72) / 4 : P < 144 ? (P % 2) * 18 + E + ((P - P1) % N2) / 2 : P - 144; }
};
struct StreamS {
  int b[4];
  static constexpr int id(int P) { return P < 90 ? (P % 5 < 4 ? 0 : 3) : 1 + (P - 90) / 36; }
  static constexpr int kb(int P) { return P < 90 ? (P % 5 < 4 ? ((P % 5) % 2 + 8 * ((P % 5) / 2)) * 18 : 0) + P / 5 : (P % 2) * 18 + ((P - 90) % 36) / 2; }
};
template <int P, class St> __device__ __forceinline__ void sload(v4u r, const St& st, int voff) {
  rload<P % R_, (St::kb(P) & 3) * 1024>(r, voff, st.b[St::id(P)] + (St::kb(P) >> 2) * 4096);
}
template <int P0, int N, class St> __device__ __forceinline__ void fillr(v4u r, const St& st, int voff) {
  sfor<P0, P0 + N>([&](auto pc) { sload<pc.value>(r, st, voff); });
  SB();
}
template <int P, int TOTAL, class St> __device__ __forceinline__ void refill(v4u r, const St& st, int voff) {
  if constexpr (P + R_ < TOTAL) sload<P + R_>(r, st, voff);
}
__device__ __forceinline__ StreamQ mkQ(int j, int hl, int role) { return StreamQ{{WQ_OFF + ((4 * j + hl) * 4 + 2 * role) * REV}}; }
__device__ __forceinline__ int w2pair(int j, int d, int role) { return W2_OFF + (36 * j + (d != j ? 4 * d : 32) + 2 * role) * REV; }
__device__ __forceinline__ StreamF mkF(int j, int hl) { return StreamF{{W1_OFF + (36 * j + 4 * hl) * REV, w2pair(j, 2 * hl, 0), w2pair(j, 2 * hl + 1, 0), W2_OFF + (40 * j + hl) * REV}}; }
__device__ __forceinline__ StreamS mkS(int j, int hl) { return StreamS{{W1_OFF + (36 * j + 16 + 2 * hl) * REV, w2pair(j, 2 * hl, 1), w2pair(j, 2 * hl + 1, 1), W1_OFF + (36 * j + 32 + hl) * REV}}; }
struct NoHook { template <int S> static constexpr int extra() { return 0; } template <int S> __device__ __forceinline__ void at() {} };
template <int NT, int NK, int S0, int S1, int P0, int TOTAL, bool INIT, int I0, int NKL, class St, class Hook>
__device__ __forceinline__ void segx(f32x4* const (&acc)[NT], const LAS char* const (&xb)[NK], v4u r, const St& mb, int voff, Hook& hook) {
  constexpr int XD = NT >= 4 ? 1 : (NT == 1 ? 4 : 2);
  v4u xf[XD][NK];
#pragma unroll
  for (int d = 0; d < XD; ++d)
#pragma unroll
    for (int tt = 0; tt < NK; ++tt)
      if (S0 + d < S1) xf[d][tt] = *(const LAS v4u*)(xb[tt] + (S0 + d) * 1024);
  SB();
  asm volatile("s_nop 1");
  sfor<S0, S1>([&](auto sc) {
    constexpr int s = sc.value, p = P0 + (s - S0) * NT, d = (s - S0) % XD;
    constexpr int RS = (R_ - I0 + 3) / 4;
    constexpr int lag = RS > 1 ? RS : 1;
    constexpr int k = s - S0;
    constexpr int issued = k < RS ? I0 + 4 * k : cmin(TOTAL, R_ + (k < lag ? P0 : p - lag * NT));
    rwait<issued - (p + NT) + Hook::template extra<s>()>();
    if constexpr (k < RS) sfor<I0 + 4 * k, cmin(R_, I0 + 4 * k + 4)>([&](auto qc) { sload<qc.value>(r, mb, voff); });
    hook.template at<s>();
    sfor<0, NK>([&](auto kc) {
      constexpr int tt = kc.value;
      sfor<0, NT>([&](auto tc) {
        constexpr int t = tc.value;
        if constexpr (t == NT - 1 && tt >= NKL) {}
        else if constexpr (INIT && s == S0) rmfma0<(p + t) % R_>(acc[t][tt], xf[d][tt]);
        else rmfma<(p + t) % R_>(acc[t][tt], xf[d][tt]);
        constexpr int idx = tt * NT + t;
        if constexpr (k >= lag && idx % NK == 0 && idx / NK < NT) refill<p - lag * NT + idx / NK, TOTAL>(r, mb, voff);
      });
      SB();
      if constexpr (s + XD < S1) { xf[d][tt] = *(const LAS v4u*)(xb[tt] + (s + XD) * 1024); SB(); }
    });
  });
  static_assert(I0 == R_ || P0 + (S1 - S0 - (R_ - I0 + 3) / 4) * NT + R_ >= TOTAL, "ramp: all refills are out inside the loop");
  sfor<0, NT>([&](auto tc) { refill<P0 + (S1 - 1 - S0) * NT + tc.value, TOTAL>(r, mb, voff); });
  asm volatile("s_nop 7\n\ts_nop 1");
  SB();
}
template <int NT, int NK, int S0, int S1, int P0, int TOTAL, bool INIT, class St, class Hook = NoHook>
__device__ __forceinline__ void seg(f32x4* const (&acc)[NT], const LAS char* xb, v4u r, const St& mb, int voff, Hook hook = Hook{}) {
  const LAS char* x2 = xb + 2 * REV;
  asm volatile("" : "+v"(x2));
  segx<NT, 4, S0, S1, P0, TOTAL, INIT, R_, 4>(acc, {xb, xb + REV, x2, x2 + REV}, r, mb, voff, hook);
}
template <int LO, class St> __device__ __forceinline__ void fillh(v4u r, const St& st, int voff) { fillr<LO ? R_ / 2 : 0, R_ / 2>(r, st, voff); }
__device__ __forceinline__ v2u trread(const LAS char* p) {
  typedef short s4 __attribute__((ext_vector_type(4)));
  return __builtin_bit_cast(v2u, __builtin_amdgcn_ds_read_tr16_b64_v4i16((LAS s4*)p));
}
constexpr float AL = 0.46866869f, KK = 2.3022082f / AL;
#define EACH16 _Pragma("unroll") for (int tt = 0; tt < 4; ++tt) _Pragma("unroll") for (int r = 0; r < 4; ++r)
__device__ __forceinline__ void gelu16(f32x4 (&a)[4], const float (&rstd)[4], const float (&mr)[4], f32x4 cs, f32x4 bb, v2u (&o)[4]) {
  float e[4][4], u[4][4];
  EACH16 e[tt][r] = sfnma(cs[r], mr[tt], bb[r]);
  SB();
  EACH16 u[tt][r] = sfma(a[tt][r], rstd[tt], e[tt][r]);
  SB();
  EACH16 e[tt][r] = ssqs(u[tt][r], KK);
  SB();
  EACH16 e[tt][r] = smul(e[tt][r], u[tt][r]);
  SB();
  EACH16 e[tt][r] = __builtin_amdgcn_exp2f(-e[tt][r]);
  SB();
  EACH16 e[tt][r] = sfmas(e[tt][r], AL);
  SB();
  EACH16 e[tt][r] = __builtin_amdgcn_rcpf(e[tt][r]);
  SB();
  EACH16 u[tt][r] = smul(u[tt][r], e[tt][r]);
  SB();
#pragma unroll
  for (int tt = 0; tt < 4; ++tt) o[tt] = v2u{pk2(u[tt][0], u[tt][1]), pk2(u[tt][2], u[tt][3])};
  SB();
}
__device__ __forceinline__ void gelu16p(f32x4 (&a)[4], const float (&rstd)[4], const float (&mr)[4], f32x4 cs, f32x4 bb, v2u (&o)[4]) {
  f32x4 e[4];
#pragma unroll
  for (int tt = 0; tt < 4; ++tt) e[tt] = bb - cs * mr[tt];
  SB();
#pragma unroll
  for (int tt = 0; tt < 4; ++tt) a[tt] = a[tt] * rstd[tt] + e[tt];
  SB();
#pragma unroll
  for (int tt = 0; tt < 4; ++tt) e[tt] = a[tt] * a[tt] + KK;
  SB();
#pragma unroll
  for (int tt = 0; tt < 4; ++tt) e[tt] = e[tt] * a[tt];
  SB();
  EACH16 e[tt][r] = __builtin_amdgcn_exp2f(-e[tt][r]);
  SB();
#pragma unroll
  for (int tt = 0; tt < 4; ++tt) e[tt] = e[tt] * AL + AL;
  SB();
  EACH16 e[tt][r] = __builtin_amdgcn_rcpf(e[tt][r]);
  SB();
#pragma unroll
  for (int tt = 0; tt < 4; ++tt) a[tt] = a[tt] * e[tt];
  SB();
#pragma unroll
  for (int tt = 0; tt < 4; ++tt) o[tt] = pk4(a[tt]);
  SB();
}
struct StoreHook {
  v4u d[4]; rsrc_t rs; int voff, po0, po1;
  template <int S> static constexpr int extra() { return S <= 6 ? 0 : S <= 9 ? 1 : S <= 12 ? 2 : S <= 15 ? 3 : 4; }
  template <int S> __device__ __forceinline__ void at() {
    if constexpr (S == 6 || S == 9 || S == 12 || S == 15) {
      constexpr int q = (S - 6) / 3;
      SB(); stc128(d[q], rs, voff, po0 + q * po1); SB();
    }
  }
};
template <int SL> __device__ __forceinline__ void lds_wait_ge(LAS char* L, int word, unsigned want) {
  volatile LAS unsigned* p = (volatile LAS unsigned*)(L + L_W + word * 4);
#pragma unroll 1
  for (unsigned spins = 0; spins < (1u << 22); ++spins) {
    if (__builtin_amdgcn_readfirstlane(*p) >= want) break;
    if (SL) __builtin_amdgcn_s_sleep(SL);
  }
  asm volatile("" ::: "memory");
}
template <int N> __device__ __forceinline__ void arrive(char* ws, LAS char* L, int word, int line, int i, u64 tag, int lane) {
  asm volatile("s_waitcnt vmcnt(0)" ::: "memory");
  if (lane == 0) {
    const unsigned old = __hip_atomic_fetch_add((LAS unsigned*)(L + L_W + word * 4), 1u, __ATOMIC_RELAXED, __HIP_MEMORY_SCOPE_WORKGROUP);
    if (old % N == N - 1) __hip_atomic_store(flagp(ws, line, i), tag, RLX_AGENT);
  }
}
__device__ __forceinline__ void acc8(f32x4& a, f32x4& b, v4u p) {
  a[0] = sadd(a[0], bflo(p[0])); a[1] = sadd(a[1], bfhi(p[0])); a[2] = sadd(a[2], bflo(p[1])); a[3] = sadd(a[3], bfhi(p[1]));
  b[0] = sadd(b[0], bflo(p[2])); b[1] = sadd(b[1], bfhi(p[2])); b[2] = sadd(b[2], bflo(p[3])); b[3] = sadd(b[3], bfhi(p[3]));
}

#define FRESH() int lane = lane0, c = c0, j = j0, w = w0; asm volatile("" : "+v"(lane)); asm volatile("" : "+s"(c), "+s"(j), "+s"(w)); \
  const int g = lane >> 4, m = lane & 15, voff = lane * 16, role = w >> 2, hl = w & 3; (void)g; (void)m; (void)voff; (void)role; (void)hl;

template <bool CS> __device__ __forceinline__ unsigned jtr(v4u* r, LAS char* scr, int lane) {
  const int g = lane >> 4, m = lane & 15;
#pragma unroll
  for (int i = 0; i < 4; ++i) *(LAS v4u*)(scr + i * 1024 + lane * 16) = r[i];
  const LAS char* p = scr + (4 * g + (m >> 2)) * 128 + (m & 3) * 8;
  float cs4[4] = {0.f, 0.f, 0.f, 0.f};
#pragma unroll
  for (int t = 0; t < 4; ++t) { const v2u lo = trread(p + t * 32), hi = trread(p + t * 32 + 2048); r[t] = v4u{lo[0], lo[1], hi[0], hi[1]}; }
  if (CS) {
#pragma unroll
    for (int t = 0; t < 4; ++t) { float dummy = 0.f; stat2(r[t], cs4[t], dummy); cs4[t] = red16sum(cs4[t]); }
  }
  return __float_as_uint(g < 2 ? (g == 0 ? cs4[0] : cs4[1]) : (g == 2 ? cs4[2] : cs4[3]));
}
struct JobHook {
  v4u f[8]; unsigned cs; rsrc_t rs; int lane, d1, d2, dc;
  template <int S> static constexpr int extra() { return S <= 9 ? 0 : S - 9; }
  template <int S> __device__ __forceinline__ void at() {
    if constexpr (S == 9) { SB(); stc32(cs, rs, lane * 4, dc); SB(); }
    if constexpr (S > 9) { SB(); stc128(f[S - 10], rs, lane * 16, (S < 14 ? d1 : d2) + ((S - 10) & 3) * REV); SB(); }
  }
};
__device__ __forceinline__ void wo_req(v4u rw, int j, int hl, int voff) {
  const int wa = WO_OFF + (8 * j + hl) * REV, wb = WO_OFF + (8 * j + 4 + 2 * (j >> 2)) * REV + hl * 9216;
  sfor<0, 27>([&](auto ic) { constexpr int i = ic.value, q = i < 18 ? i : i - 18; rload<i, (q & 3) * 1024>(rw, voff, (i < 18 ? wa : wb) + (q >> 2) * 4096); });
  SB();
}
struct HalfHook {
  LAS char* L;
  template <int S> static constexpr int extra() { return 0; }
  template <int S> __device__ __forceinline__ void at() { if constexpr (S == 7) { SB(); lds_wait_ge<1>(L, 25, 4u); SB(); } }
};
struct WordHook {
  LAS char* L; unsigned want; int hl;
  template <int S> static constexpr int extra() { return 0; }
  template <int S> __device__ __forceinline__ void at() {
    if constexpr (S == 9) { SB(); lds_wait_ge<1>(L, 17, want); SB(); }
    if constexpr (S == GOS) { SB(); *(volatile LAS unsigned*)(L + L_W + 80 + hl * 4) = want; SB(); }
  }
};

__global__ __launch_bounds__(512, 2) __attribute__((amdgpu_num_vgpr(RB / 2 - 1)))
void k_main(char* __restrict__ ws, const u16* __restrict__ xin, const u16* __restrict__ wqkv, const u16* __restrict__ wo,
            const u16* __restrict__ w1, const u16* __restrict__ b1, const u16* __restrict__ w2, const u16* __restrict__ b2,
            u16* __restrict__ out) {
  extern __shared__ __attribute__((aligned(16))) char smem[];
  {
    u64 pc;
    asm volatile("s_getpc_b64 %0\n\tglobal_load_dword v%c2, %1, %0" : "=&s"(pc)
                 : "v"(((blockIdx.x >> 3) + min((int)threadIdx.x, CODE_SHARE / 128 - 1) * 32) * 128), "i"(RB - 1));
    SB();
  }
  LAS char* L = (LAS char*)smem;
  const int tid = threadIdx.x, lane0 = tid & 63, w0 = __builtin_amdgcn_readfirstlane(tid >> 6);
  int c0, j0;
  {
    constexpr int G = GSPREAD, per = 8 / G;
    const int b = blockIdx.x, x = b & 7, r = b >> 3;
    j0 = (x % G) * per + (r % per);
    c0 = (x / G) * (4 * G) + r / per;
  }
  const rsrc_t rs = __builtin_amdgcn_make_buffer_rsrc((void*)ws, 0, WS_BYTES, 0x00020000);
  const v4u rw = {(unsigned)(size_t)ws, (unsigned)((size_t)ws >> 32) & 0xffffu, (unsigned)WS_BYTES, 0x00020000u};
  asm volatile("" ::: "v255");
  u64 tag = 0;
  auto qkv = [&](auto lc, auto& jh) {
    constexpr int Ly = lc.value;
    FRESH();
    auto keep = [&]() {
      const LAS char* xr = L + L_X + (hl + 1) * REV + voff;
      const v4u ra = *(const LAS v4u*)(xr + (2 * j + role) * 1024), rb = *(const LAS v4u*)(xr + (16 + (j >> 2)) * 1024);
      *(LAS v4u*)(L + L_RES + w * 1024 + voff) = ra;
      if (w == (j & 3)) *(LAS v4u*)(L + L_RES + 8192 + voff) = rb;
    };
    const StreamQ qb = mkQ(j, hl, role);
    constexpr int I0 = Ly == 0 ? 8 : R_;
    auto fix = [&](f32x4 a, const float (&cs)[4], float rstd, float mr) {
      float u[4];
#pragma unroll
      for (int r = 0; r < 4; ++r) u[r] = smul(a[r], rstd);
#pragma unroll
      for (int r = 0; r < 4; ++r) u[r] = sfnma(cs[r], mr, u[r]);
      return v2u{pk2(u[0], u[1]), pk2(u[2], u[3])};
    };
    auto bcast = [&](f32x4 a, float (&cs)[4]) {
#pragma unroll
      for (int r = 0; r < 4; ++r) cs[r] = __uint_as_float(__builtin_amdgcn_update_dpp(0u, __float_as_uint(a[r]), 0x150, 0xf, 0xf, false));
    };
    if (role == 0) {
      f32x4 a0[5], a1[5];
      const LAS char* xa = L + L_X + voff, *xc = xa + 3 * REV;
      asm volatile("" : "+v"(xc));
      if (Ly) { lds_wait_ge<1>(L, 24, 4u); lds_wait_ge<1>(L, 25, 4u); }
      keep();
      float rstd[5], mr[5], cs0[4], cs1[4];
      stat_halo(L, m, rstd[0], mr[0]);
#pragma unroll
      for (int t = 1; t < 5; ++t) stat_own(L, t - 1, m, 1.0f, rstd[t], mr[t]);
      segx<2, 5, 0, 18, 0, 36, true, I0, 5>({a0, a1}, {xa, xa + REV, xa + 2 * REV, xc, xc + REV}, rw, qb, voff, jh);
      LAS char* A = L + L_ATT + hl * ATT_STR + lane * 8;
      bcast(a0[0], cs0); bcast(a1[0], cs1);
      SB();
#pragma unroll
      for (int t = 0; t < 5; ++t) {
        if (t) *(LAS v2u*)(A + A_Q + t * 512) = pk4(a0[t] * rstd[t] - f32x4{cs0[0], cs0[1], cs0[2], cs0[3]} * mr[t]);
        *(LAS v2u*)(A + A_K + t * 512) = pk4(a1[t] * rstd[t] - f32x4{cs1[0], cs1[1], cs1[2], cs1[3]} * mr[t]);
      }
    }
    int role2 = role;
    asm volatile("" : "+s"(role2));
    if (role2) {
      f32x4 a0[5], a1[5];
      const int ta = (hl & 1) ? 3 : 1, tb = 4 - ta;
      const LAS char* x0 = L + L_X + voff, *xa = x0 + ta * REV, *xc = x0 + tb * REV;
      HalfHook hk{L};
      __builtin_amdgcn_s_setprio(1);
      if (Ly) lds_wait_ge<0>(L, 24, 4u);
      if constexpr (Ly == 0) segx<2, 5, 0, 18, 0, 36, true, I0, 3>({a0, a1}, {x0, xa, xa + REV, xc, xc + REV}, rw, qb, voff, jh);
      else segx<2, 5, 0, 18, 0, 36, true, I0, 3>({a0, a1}, {x0, xa, xa + REV, xc, xc + REV}, rw, qb, voff, hk);
      __builtin_amdgcn_s_setprio(0);
      if (Ly == 0 && WOF) wo_req(rw, j, hl, voff);
      keep();
      LAS char* A = L + L_ATT + hl * ATT_STR + A_V + m * 32 + g * 8, *E = L + L_ATT + (hl & 2) * ATT_STR + A_E + m * 32 + g * 8;
      float rstd[5], mr[5], cs0[4], cs1[4];
      int to[5] = {0, ta, ta + 1, tb, tb + 1};
      stat_halo(L, m, rstd[0], mr[0]);
#pragma unroll
      for (int t = 1; t < 5; ++t) stat_own(L, to[t] - 1, m, 1.0f, rstd[t], mr[t]);
      bcast(a0[0], cs0); bcast(a1[0], cs1);
      SB();
#pragma unroll
      for (int t = 0; t < 5; ++t) {
        *(LAS v2u*)(A + to[t] * 512) = fix(a0[t], cs0, rstd[t], mr[t]);
        if (t < 3) *(LAS v2u*)(E + to[t] * 512) = fix(a1[t], cs1, rstd[t], mr[t]);
      }
      if (Ly || !WOF) wo_req(rw, j, hl, voff);
    }
  };
  {
    FRESH();
    const int r8 = lane >> 3, q8 = lane & 7;
    JobHook jh;
    jh.rs = rs; jh.lane = lane; jh.d1 = jh.d2 = jh.dc = WS_BYTES;
    u64 gen = 0;
    {
      asm volatile("global_load_dword v%c2, %0, %1" :: "v"(((2 * (64 * (c >> 1) + 8 * j + w) + (c & 1)) * D_ + min(lane, 8) * 64) * 2), "s"(xin), "i"(RB - 1));
    }
    auto jobs = [&](int q0) {
      const int q = min(q0, 161), s = (q * 7282) >> 16, B = q - 9 * s;
      const u16* p1 = w1 + ((32 * s + r8) * F_ + 576 * j + 64 * B + 8 * q8);
      const u16* p2 = w2 + ((576 * j + 32 * s + r8) * D_ + 64 * B + 8 * q8);
#pragma unroll
      for (int i = 0; i < 4; ++i) jh.f[i] = *(const v4u*)(p1 + i * 8 * F_);
#pragma unroll
      for (int i = 0; i < 4; ++i) jh.f[4 + i] = *(const v4u*)(p2 + i * 8 * D_);
      if (q0 < 162) {
        jh.d1 = W1_OFF + (36 * j + 4 * B) * REV + (s < 16 ? (s - 2 * j) & 15 : s) * 1024;
        jh.d2 = W2_OFF + (36 * j + 4 * B) * REV + s * 1024;
        jh.dc = CSP_OFF + (j * 18 + s) * 2304 + B * 256;
      }
      SB();
    };
    if (w >= 5) {
      if (c < 18) {
        if (QP) __builtin_amdgcn_s_setprio(3);
        const int sec = w - 5;
        const u16* src = wqkv + (32 * c) * (3 * D_) + 72 * j;
        LAS char* scr = L + L_ATT + 32768 + sec * 4736;
        v4u qr[5], jo[4];
#pragma unroll
        for (int i = 0; i < 5; ++i) {
          const int ch = min(64 * i + lane, 287), row = (ch * 7282) >> 16;
          qr[i] = *(const v4u*)(src + row * (3 * D_) + sec * D_ + (ch - 9 * row) * 8);
        }
        {
          typedef unsigned v4u8 __attribute__((ext_vector_type(4), aligned(8)));
          const u16* ob = sec == 2 ? src : wo;
          const int e0 = (lane >> 2) * (3 * D_) + min(lane & 3, 2) * D_ + 16;
          const int ra = 36 * c + r8, rb = 36 * (8 * (c - 16) + 2 * (r8 >> 2)) + 16 + (r8 & 3), o0 = (c < 16 ? ra : rb) * D_ + 64 * (sec ? 8 : j) + 8 * q8;
          const int v0 = sec == 2 ? e0 : o0;
          const int s1 = sec == 2 ? 16 * 3 * D_ : (c < 16 ? 8 : 144) * D_, s2 = sec == 2 ? 36 : (c < 16 ? 20 : 36) * D_;
#pragma unroll
          for (int i = 0; i < 4; ++i) jo[i] = *(const v4u8*)(ob + (v0 + (i & 1) * s1 + (i >> 1) * s2));
        }
        SB();
        gen = __hip_atomic_load((gu64*)(ws + FL_OFF), RLX_AGENT);
        SB();
        bar();
#pragma unroll
        for (int i = 0; i < 5; ++i) *(LAS v4u*)(scr + min(64 * i + lane, 287) * 16) = qr[i];
        const int qd = WQ_OFF + 16 * j * REV + (sec >> 1) * 2 * REV + (2 * c + (sec & 1)) * 1024;
#pragma unroll
        for (int h = 0; h < 4; ++h) {
          const LAS char* p = scr + (4 * g + (m >> 2)) * 144 + (h >> 1) * 72 + (h & 1) * 40 + (m & 3) * 8;
          const v2u lo = trread(p), hi = trread(p + 16 * 144);
          stc128(v4u{lo[0], lo[1], hi[0], hi[1]}, rs, voff, qd + h * 4 * REV);
        }
        if (sec == 2) {
#pragma unroll
          for (int i = 0; i < 4; ++i) *(LAS v2u*)(scr + i * 512 + lane * 8) = (lane & 3) == 3 ? v2u{0u, 0u} : v2u{jo[i][0], jo[i][1]};
#pragma unroll
          for (int p = 0; p < 2; ++p) {
            const v2u lo = trread(scr + p * 1024 + lane * 8), hi = trread(scr + p * 1024 + 512 + lane * 8);
            jo[2 * p] = v4u{lo[0], lo[1], hi[0], hi[1]}; jo[2 * p + 1] = jo[2 * p];
          }
        } else jtr<false>(jo, scr, lane);
        const int od = sec == 2 ? qd + 1024 : WO_OFF + (8 * j + 4 * sec) * REV + c * 1024, os = sec == 2 ? 4 * REV : REV;
#pragma unroll
        for (int t = 0; t < 4; ++t) stc128(jo[t], rs, voff, od + t * os);
        arrive<3>(ws, L, 9, 320 + j, c, gen + 1, lane);
        lds_wait_ge<1>(L, 9, 3u);
        __builtin_amdgcn_s_setprio(0);
        jobs(112 + c * 3 + sec);
      } else {
        bar();
        gen = __hip_atomic_load((gu64*)(ws + FL_OFF), RLX_AGENT);
        asm volatile("" : "+v"(gen));
        __builtin_amdgcn_s_sleep(PVS);
        jobs((c - 18) * 8 + w);
      }
    } else {
      if (tid < 32) *(LAS unsigned*)(L + L_W + tid * 4) = 0u;
      bar();
      asm volatile("s_waitcnt vmcnt(0)");
      const int cc = (w == 0 && c >= 2) ? c - 2 : c, tt = w == 0 ? 3 : w - 1;
      const u16* src = xin + ((2 * (64 * (cc >> 1) + 16 * tt + r8) + (cc & 1)) * D_ + 8 * q8);
      v4u x[2][9];
#pragma unroll
      for (int i = 0; i < 9; ++i) { x[0][i] = *(const v4u*)(src + 64 * i); x[1][i] = *(const v4u*)(src + 16 * D_ + 64 * i); }
      SB();
      float s1[2] = {0.f, 0.f}, s2[2] = {0.f, 0.f};
      LAS char* dst = L + L_X + w * REV + (q8 >> 2) * 1024 + ((q8 & 1) * 32 + r8) * 16 + ((q8 >> 1) & 1) * 8;
#pragma unroll
      for (int i = 0; i < 9; ++i) {
#pragma unroll
        for (int h = 0; h < 2; ++h) {
          *(LAS v2u*)(dst + i * 2048 + h * 128) = v2u{x[h][i][0], x[h][i][1]};
          *(LAS v2u*)(dst + i * 2048 + h * 128 + 256) = v2u{x[h][i][2], x[h][i][3]};
        }
        SB();
      }
      if (c >= 18) jobs((c - 18) * 8 + w);
#pragma unroll
      for (int i = 0; i < 9; ++i) { stat2(x[0][i], s1[0], s2[0]); stat2(x[1][i], s1[1], s2[1]); }
      if (w == 0) ones_row(L, lane);
#pragma unroll
      for (int h = 0; h < 2; ++h) { s1[h] = red8sum(s1[h]); s2[h] = red8sum(s2[h]); }
      if (q8 < 2) {
        const f32x4 p = {q8 ? s1[1] : s1[0], q8 ? s2[1] : s2[0], 0.f, 0.f}, z = {0.f, 0.f, 0.f, 0.f};
        if (w) *(LAS f32x4*)(L + L_SP + (w - 1) * 256 + (q8 * 8 + r8) * 16) = p;
        else { LAS char* d = L + L_SH + (q8 * 8 + r8) * 48; *(LAS f32x4*)d = p; *(LAS f32x4*)(d + 16) = z; *(LAS f32x4*)(d + 32) = z; }
      }
    }
    jh.cs = jtr<true>(jh.f, L + L_ATT + w * 4096, lane);
    jtr<false>(jh.f + 4, L + L_ATT + w * 4096, lane);
    if (w == 6) {
      unsigned bv[5];
#pragma unroll
      for (int i = 0; i < 5; ++i) bv[i] = *(const unsigned*)(b1 + 576 * j + min(lane + 64 * i, 287) * 2);
      const unsigned b2v = *(const unsigned*)(b2 + (lane < 32 ? 64 * j + 2 * lane : 512 + 32 * (j >> 2) + 2 * (lane & 15)));
#pragma unroll
      for (int i = 0; i < 5; ++i) *(LAS f32x2*)(L + L_B1 + min(lane + 64 * i, 287) * 8) = f32x2{AL * bflo(bv[i]), AL * bfhi(bv[i])};
      if (lane < 48) *(LAS f32x2*)(L + L_B2 + lane * 8) = f32x2{bflo(b2v), bfhi(b2v)};
    }
    if (w == 7) {
      poll<1>(flagp(ws, 320 + j, min(lane, 17)), gen + 1);
      if (lane == 0) *(LAS u64*)(L + L_W + 48) = gen + 1;
    }
    bar();
    fillr<0, 8>(rw, mkQ(j, hl, role), voff);
    tag = *(LAS u64*)(L + L_W + 48);
    tag = ((u64)(unsigned)__builtin_amdgcn_readfirstlane((unsigned)(tag >> 32)) << 32) | (unsigned)__builtin_amdgcn_readfirstlane((unsigned)tag);
    qkv(std::integral_constant<int, 0>{}, jh);
  }

#pragma unroll 1
  for (int Ly = 0; Ly < 2; ++Ly) {
    {
      FRESH();
      const float sc = 0.23570226039551584f * 1.4426950408889634f, NEG = -3.0e38f;
      f32x4 bp, bc;
#pragma unroll
      for (int r = 0; r < 4; ++r) {
        const int dk = m - (4 * g + r);
        bp[r] = (dk <= -9 && !(c < 2 && hl == 0)) ? 0.f : NEG;
        bc[r] = (unsigned)dk <= 7u ? 0.f : NEG;
      }
      asm volatile("" : "+v"(bp), "+v"(bc));
      bar();
      const LAS char* A = L + L_ATT + 2 * role * ATT_STR + hl * 512 + lane * 8;
      const LAS char* Ee = L + L_ATT + 2 * role * ATT_STR + hl * 512 + A_E + m * 32;
      v2u qm[2], km[2][2], vm[2][2], em[2], xk[2];
#pragma unroll
      for (int q = 0; q < 2; ++q) {
        qm[q] = *(const LAS v2u*)(A + q * ATT_STR + A_Q + 512);
        xk[q] = *(const LAS v2u*)(Ee + q * 512 + 8);
#pragma unroll
        for (int i = 0; i < 2; ++i) km[q][i] = *(const LAS v2u*)(A + q * ATT_STR + A_K + i * 512);
      }
      const v2u xq = *(const LAS v2u*)(Ee + 512);
#pragma unroll
      for (int q = 0; q < 2; ++q) {
        em[q] = trread(A + A_E + q * 512);
#pragma unroll
        for (int i = 0; i < 2; ++i) vm[q][i] = trread(A + q * ATT_STR + A_V + i * 512);
      }
      SB();
      const f32x4 z = {0.f, 0.f, 0.f, 0.f};
      f32x4 sp[2], sq[2];
#pragma unroll
      for (int q = 0; q < 2; ++q) {
        const v4u qf = {qm[q][0], qm[q][1], g == 0 ? xq[q] : 0u, 0u};
        sp[q] = mfma(v4u{km[q][0][0], km[q][0][1], xk[0][q], 0u}, qf, z);
        sq[q] = mfma(v4u{km[q][1][0], km[q][1][1], xk[1][q], 0u}, qf, z);
      }
      float mx[2], sum[2];
#pragma unroll
      for (int q = 0; q < 2; ++q) {
        sp[q] = sp[q] * sc + bp; sq[q] = sq[q] * sc + bc;
        mx[q] = fmaxf(fmaxf(fmaxf(sp[q][0], sp[q][1]), fmaxf(sp[q][2], sp[q][3])), fmaxf(fmaxf(sq[q][0], sq[q][1]), fmaxf(sq[q][2], sq[q][3])));
      }
#pragma unroll
      for (int q = 0; q < 2; ++q) mx[q] = red16max(mx[q]);
#pragma unroll
      for (int q = 0; q < 2; ++q) {
        sp[q] = sp[q] - mx[q]; sq[q] = sq[q] - mx[q];
#pragma unroll
        for (int r = 0; r < 4; ++r) { sp[q][r] = __builtin_amdgcn_exp2f(sp[q][r]); sq[q][r] = __builtin_amdgcn_exp2f(sq[q][r]); }
        const f32x4 t4 = sp[q] + sq[q];
        sum[q] = (t4[0] + t4[1]) + (t4[2] + t4[3]);
      }
#pragma unroll
      for (int q = 0; q < 2; ++q) sum[q] = __builtin_amdgcn_rcpf(red16sum(sum[q]));
      f32x4 o[2], oe[2];
#pragma unroll
      for (int q = 0; q < 2; ++q) {
        const v2u pp = pk4(sp[q] * sum[q]), pc = pk4(sq[q] * sum[q]);
        const v4u pf = {pp[0], pp[1], pc[0], pc[1]};
        o[q] = mfma(v4u{vm[q][0][0], vm[q][0][1], vm[q][1][0], vm[q][1][1]}, pf, z);
        oe[q] = mfma(v4u{em[0][0], em[0][1], em[1][0], em[1][1]}, pf, z);
      }
      const int ob = OB_OFF + c * XBC + hl * REV;
      const v2u oa = pk4(o[0]), oc = pk4(o[1]);
      stc128(v4u{oa[0], oa[1], oc[0], oc[1]}, rs, voff, ob + (2 * j + role) * 1024);
      if (g == 2) stc64(v2u{pk2(oe[0][0], oe[0][1]), pk2(oe[1][2], oe[1][3])}, rs, m * 8, ob + 16384 + (2 * j + role) * 128);
      SB();
      publish(ws, (5 * Ly + 0) * 32 + c, j, tag, tid);
      if (Ly == 0 && tid == 0) __hip_atomic_store(flagp(ws, 328 + j, c), tag, RLX_AGENT);
      if (role) {
        const unsigned la = (unsigned)(size_t)(L + L_X + hl * REV + voff), lb = (unsigned)(size_t)(L + L_X + 4 * REV + hl * 9216 + voff);
        sfor<0, 27>([&](auto ic) {
          constexpr int i = ic.value;
          rput<i, (i < 18 ? i : i - 18) * 1024>(i < 18 ? la : lb);
        });
      }
      if (Ly && role == 0) fillh<0>(rw, mkF(j, hl), voff);
    }
    {
      FRESH();
      const LAS char* wb = L + L_X + role * 9216 + voff;
      const LAS char* wb2 = wb + 3 * REV;
      asm volatile("" : "+v"(wb2));
      const bool ext = hl == (j & 3);
      f32x4 acc[6];
      {
        const v4u ra = *(LAS v4u*)(L + L_RES + w * 1024 + voff), rb = *(LAS v4u*)(L + L_RES + 8192 + voff);
        acc[0] = unpk4(role ? 0u : ra[0], role ? 0u : ra[1]); acc[1] = unpk4(role ? 0u : ra[2], role ? 0u : ra[3]);
        acc[2] = unpk4(role ? ra[0] : 0u, role ? ra[1] : 0u); acc[3] = unpk4(role ? ra[2] : 0u, role ? ra[3] : 0u);
        acc[4] = unpk4(role ? 0u : rb[0], role ? 0u : rb[1]); acc[5] = unpk4(role ? 0u : rb[2], role ? 0u : rb[3]);
      }
      SB();
      await(ws, 5 * Ly + 0, c, c, tag, w, lane);
      v4u of[9];
      const int ob = OB_OFF + c * XBC + hl * REV + role * 9216;
#pragma unroll
      for (int s = 0; s < 7; ++s) of[s] = ldc(rs, voff + (s & 3) * 1024, ob + (s >> 2) * 4096);
      {
        const int va = role ? g * 256 + m * 8 : voff, vb = va + (role ? 128 : 8);
#pragma unroll
        for (int s = 7; s < 9; ++s) {
          const v2u lo = ldc64(rs, va + (s - 7) * 1024, ob + 7168), hi = ldc64(rs, vb + (s - 7) * 1024, ob + 7168);
          of[s] = v4u{lo[0], lo[1], hi[0], hi[1]};
        }
      }
      SB();
      auto proj = [&](auto nc) {
        constexpr int N = nc.value;
        v4u wf[2][N];
        auto wld = [&](int s) {
#pragma unroll
          for (int i = 0; i < N; ++i) wf[s % 2][i] = *(const LAS v4u*)((i < 3 ? wb + i * REV : wb2 + (i - 3) * REV) + s * 1024);
        };
        wld(0); wld(1);
        SB();
#pragma unroll
        for (int s = 0; s < 9; ++s) {
#pragma unroll
          for (int i = 0; i < N; ++i) acc[i] = mfma(wf[s % 2][i], of[s], acc[i]);
          SB();
          if (s + 2 < 9) { wld(s + 2); SB(); }
        }
      };
      if (ext) { asm volatile("; six" ::: "memory"); proj(std::integral_constant<int, 6>{}); }
      else { asm volatile("; four" ::: "memory"); proj(std::integral_constant<int, 4>{}); }
      LAS char* R = L + L_X + 6 * REV + hl * 6144 + voff;
      *(LAS f32x4*)(R + role * 2048) = role ? acc[0] : acc[2];
      *(LAS f32x4*)(R + role * 2048 + 1024) = role ? acc[1] : acc[3];
      if (ext && role) { *(LAS f32x4*)(R + 4096) = acc[4]; *(LAS f32x4*)(R + 5120) = acc[5]; }
      if (role == 0) {
        if (Ly == 0) { poll<1>(flagp(ws, 328 + j, lane & 31), tag); fillh<0>(rw, mkF(j, hl), voff); }
        else fillh<1>(rw, mkF(j, hl), voff);
      }
      bar();
      {
        const f32x4 pa = *(LAS f32x4*)(R + 2048 - role * 2048), pb = *(LAS f32x4*)(R + 3072 - role * 2048);
        const v2u oa = pk4((role ? acc[2] : acc[0]) + pa), ob2 = pk4((role ? acc[3] : acc[1]) + pb);
        const int xb = X1_OFF + c * XBC + hl * REV;
        const v4u xo = {oa[0], oa[1], ob2[0], ob2[1]};
        stc128(xo, rs, voff, xb + (2 * j + role) * 1024);
        if (ext && role == 0) {
          const v2u ea = pk4(acc[4] + *(LAS f32x4*)(R + 4096)), eb = pk4(acc[5] + *(LAS f32x4*)(R + 5120));
          stc128(v4u{ea[0], ea[1], eb[0], eb[1]}, rs, voff, xb + (16 + (j >> 2)) * 1024);
        }
        SB();
        *(LAS v4u*)(L + L_X + hl * REV + role * 1024 + voff) = xo;
        float s1 = 0.f, s2 = 0.f;
        stat2(xo, s1, s2);
        s1 = red16sum(s1); s2 = red16sum(s2);
        if (lane < 16) *(LAS f32x2*)(L + L_SP + 1024 + hl * 256 + lane * 16 + role * 8) = f32x2{s1, s2};
      }
      publish(ws, (5 * Ly + 1) * 32 + c, j, tag, tid);
      if (role) {
        fillh<0>(rw, mkS(j, hl), voff);
        if (Ly == 0 && hl) {
          const int o4 = ((hl - 1) * 48 + min(lane, 47)) * 16;
          v4u v[18];
#pragma unroll
          for (int s = 0; s < 18; ++s) v[s] = ldc(rs, o4, CSP_OFF + (j * 18 + s) * 2304);
          SB();
          f32x4 a = __builtin_bit_cast(f32x4, v[0]);
#pragma unroll
          for (int s = 1; s < 18; ++s)
#pragma unroll
            for (int r = 0; r < 4; ++r) { const unsigned u = v[s][r]; a[r] = sadd(a[r], __uint_as_float(u)); }
          *(LAS f32x4*)(L + L_CS1 + o4) = a;
        }
      }
    }
    {
      FRESH();
      const int pbc = PB_OFF + c * (64 * 9216);
      const StreamQ wq0 = mkQ(j, hl, role);
      auto stats = [&](float (&rstd)[4], float (&mr)[4]) {
#pragma unroll
        for (int tt = 0; tt < 4; ++tt) stat_own4(L, tt, m, AL, rstd[tt], mr[tt]);
      };
      auto gtile = [&](auto pkc, f32x4 (&x)[4], const float (&rstd)[4], const float (&mr)[4], int T, v2u (&o)[4]) {
        const f32x4 cs = *(LAS f32x4*)(L + L_CS1 + T * 64 + g * 16), bb = *(LAS f32x4*)(L + L_B1 + T * 64 + g * 16);
        if constexpr (pkc.value) gelu16p(x, rstd, mr, cs, bb, o); else gelu16(x, rstd, mr, cs, bb, o);
      };
      auto hpair = [&](auto pk0, auto pk1, f32x4 (&x)[4], f32x4 (&y)[4], const float (&rstd)[4], const float (&mr)[4], int T, auto&& mid) {
        v2u oa[4], ob[4];
        gtile(pk0, x, rstd, mr, T, oa);
        mid();
        gtile(pk1, y, rstd, mr, T + 1, ob);
#pragma unroll
        for (int tt = 0; tt < 4; ++tt) *(LAS v4u*)(L + L_H + tt * REV + (T >> 1) * 1024 + voff) = v4u{oa[tt][0], oa[tt][1], ob[tt][0], ob[tt][1]};
        SB();
      };
      auto poff = [&](int ch) { const int d = 2 * hl + ch; return pbc + (d != j ? (d * 8 + j) * 9216 + role * 4096 : (role * 32 + j) * 9216 + 8192); };
      auto pstr = [&](int ch) { return 2 * hl + ch != j ? 1024 : 8 * 9216; };
      auto ppack = [&](f32x4 x, f32x4 y) { const v2u a = pk4(x), b = pk4(y); return v4u{a[0], a[1], b[0], b[1]}; };
      auto pstore2 = [&](f32x4 (&x)[4], f32x4 (&y)[4], int ch, auto slc) {
#pragma unroll
        for (int q = 0; q < 4; ++q) {
          stc128(ppack(x[q], y[q]), rs, voff, poff(ch) + q * pstr(ch));
          if (slc.value) { SB(); __builtin_amdgcn_s_sleep(slc.value); SB(); }
        }
        SB();
      };
      constexpr std::integral_constant<int, 0> I0{};
      constexpr std::integral_constant<int, 1> I1{};
      auto base = [&](f32x4& a, f32x4& b, int ks, int tt, int u) {
        a = *(LAS f32x4*)(L + L_B2 + (u >> 2) * 128 + g * 16); b = *(LAS f32x4*)(L + L_B2 + (u >> 2) * 128 + 64 + g * 16);
        acc8(a, b, *(const LAS v4u*)(L + L_X + tt * REV + (u < 8 ? u >> 2 : ks) * 1024 + voff));
        if (u < 8) acc8(a, b, *(const LAS v4u*)(L + L_OWN + u * 1024 + voff));
      };
      auto finish = [&](auto nc, f32x4 a, f32x4 b, int ks, int tt, int u, auto&& mid) {
        constexpr int N = nc.value;
        v4u pv[N];
        const int po = pbc + j * 8 * 9216 + u * 1024;
#pragma unroll
        for (int i = 0; i < N; ++i) pv[i] = ldc(rs, voff, po + (i + ((N == 7 && i >= j) ? 1 : 0)) * 9216);
        SB();
        mid(a, b);
#pragma unroll
        for (int i = 0; i < N; ++i) acc8(a, b, pv[i]);
        const v4u o = ppack(a, b);
        if (Ly == 0) stc128(o, rs, voff, X2_OFF + c * XBC + tt * REV + ks * 1024);
        else {
          u16* p = out + (size_t)(2 * (64 * (c >> 1) + 16 * tt + m) + (c & 1)) * D_ + 32 * ks + 4 * g;
          *(v2u*)p = v2u{o[0], o[1]}; *(v2u*)(p + 16) = v2u{o[2], o[3]};
        }
      };
      auto nomid = [](f32x4&, f32x4&) {};
      if (role == 0) {
        const StreamF mb = mkF(j, hl);
        __builtin_amdgcn_s_setprio(1);
        {
          f32x4 acc[4][4];
          seg<4, 4, 0, 2, 0, R_ / 2, true>({acc[0], acc[1], acc[2], acc[3]}, L + L_X + voff, rw, mb, voff);
          if (Ly == 0) fillh<1>(rw, mb, voff);
          fillr<R_, 8>(rw, mb, voff);
          if (hl == 0) {
            poll8<0>(ws, 5 * Ly + 1, c, c, tag, lane);
            *(volatile LAS unsigned*)(L + L_W + 60) = Ly + 1;
            if (Ly == 0 && blockIdx.x == 0 && lane == 0) __hip_atomic_store((gu64*)(ws + FL_OFF), tag, RLX_AGENT);
          } else lds_wait_ge<1>(L, 15, Ly + 1);
          stage8(rs, X1_OFF + c * XBC, L, j, w, lane);
          lds_wait_ge<0>(L, 16, 4 * (Ly + 1));
          seg<4, 4, 2, 18, 8, 72, false>({acc[0], acc[1], acc[2], acc[3]}, L + L_X + voff, rw, mb, voff, WordHook{L, 4u * (Ly + 1), hl});
          float rstd[4], mr[4];
          stats(rstd, mr);
          constexpr int Q4 = R_ / 4;
          hpair(I0, I0, acc[0], acc[1], rstd, mr, 4 * hl, [&] { fillr<72, Q4>(rw, mb, voff); });
          fillr<72 + Q4, Q4>(rw, mb, voff);
          hpair(I0, std::integral_constant<int, F4PK>{}, acc[2], acc[3], rstd, mr, 4 * hl + 2, [&] { fillr<72 + 2 * Q4, Q4>(rw, mb, voff); });
          fillr<72 + 3 * Q4, R_ - 3 * Q4>(rw, mb, voff);
        }
        if (EARLY) bar();
        {
          f32x4 acc[4][4];
          if constexpr (EARLY > 0) seg<4, 4, 0, EARLY ? EARLY : 1, 72, 162, true>({acc[0], acc[1], acc[2], acc[3]}, L + L_H + voff, rw, mb, voff);
          bar();
          seg<2, 4, EARLY, 18, StreamF::P1, 162, EARLY == 0>({acc[0], acc[1]}, L + L_H + voff, rw, mb, voff);
          pstore2(acc[0], acc[1], 0, std::integral_constant<int, TRICKLE>{});
          seg<2, 4, EARLY, 18, StreamF::P1 + StreamF::N2, 162, EARLY == 0>({acc[2], acc[3]}, L + L_H + voff, rw, mb, voff);
          __builtin_amdgcn_s_setprio(0);
          pstore2(acc[2], acc[3], 1, std::integral_constant<int, TRICKLE>{});
        }
        arrive<4>(ws, L, 0, (5 * Ly + 2) * 32 + c, j, tag, lane);
        lds_wait_ge<3>(L, 4 + hl, Ly + 1);
        f32x4 own[4];
        seg<1, 4, 0, 18, 144, 162, true>({own}, L + L_H + voff, rw, mb, voff);
#pragma unroll
        for (int tt = 0; tt < 4; ++tt) *(LAS v2u*)(L + L_OWN + ((hl >> 1) * 4 + tt) * 1024 + voff + 8 * (hl & 1)) = pk4(own[tt]);
        asm volatile("s_waitcnt lgkmcnt(0)" ::: "memory");
        if (lane == 0) __hip_atomic_fetch_add((LAS unsigned*)(L + L_W + 44), 1u, __ATOMIC_RELAXED, __HIP_MEMORY_SCOPE_WORKGROUP);
        lds_wait_ge<1>(L, 2, Ly + 1);
        f32x4 sa, sb;
        finish(std::integral_constant<int, 7>{}, sa, sb, 2 * j, hl, hl,
               [&](f32x4& a, f32x4& b) { lds_wait_ge<1>(L, 11, 4 * (Ly + 1)); base(a, b, 2 * j, hl, hl); });
        if (hl == 3) {
          base(sa, sb, 16 + (j >> 2), j & 3, 8);
          if (j >= 4) lds_wait_ge<1>(L, 3, Ly + 1);
          finish(std::integral_constant<int, 8>{}, sa, sb, 16 + (j >> 2), j & 3, 8, nomid);
        }
      }
      int role2 = role;
      asm volatile("" : "+s"(role2));
      if (role2 != 0) {
        const StreamS mb = mkS(j, hl);
        {
          f32x4 acc[5][4];
          seg<5, 4, 0, 2, 0, R_ / 2, true>({acc[0], acc[1], acc[2], acc[3], acc[4]}, L + L_X + voff, rw, mb, voff);
          lds_wait_ge<1>(L, 15, Ly + 1);
          stage8(rs, X1_OFF + c * XBC, L, j, w, lane);
          lds_wait_ge<1>(L, 16, 4 * (Ly + 1));
          lds_wait_ge<1>(L, 17, 4 * (Ly + 1));
          float rstd[4], mr[4];
          stats(rstd, mr);
          lds_wait_ge<2>(L, 20 + hl, 4 * (Ly + 1));
          fillr<R_ / 2, R_ / 2 + 10>(rw, mb, voff);
          seg<5, 4, 2, 18, 10, 90, false>({acc[0], acc[1], acc[2], acc[3], acc[4]}, L + L_X + voff, rw, mb, voff);
          if (EARLY) bar();
          fillr<90, R_ / 4>(rw, mb, voff);
          hpair(std::integral_constant<int, G1PK>{}, std::integral_constant<int, G2PK>{}, acc[0], acc[1], rstd, mr, 16 + 2 * hl, [] {});
          fillr<90 + R_ / 4, R_ / 4>(rw, mb, voff);
          v2u o[4];
          gtile(I1, acc[4], rstd, mr, 32 + hl, o);
          fillr<90 + 2 * (R_ / 4), R_ / 4>(rw, mb, voff);
          hpair(I1, I1, acc[2], acc[3], rstd, mr, 24 + 2 * hl, [] {});
          fillr<90 + 3 * (R_ / 4), R_ - 3 * (R_ / 4)>(rw, mb, voff);
#pragma unroll
          for (int tt = 0; tt < 4; ++tt) *(LAS v2u*)(L + L_H + tt * REV + (16 + (hl >> 1)) * 1024 + voff + 8 * (hl & 1)) = o[tt];
        }
        bar();
        {
          f32x4 acc[2][4];
          seg<2, 4, 0, 18, 90, 162, true>({acc[0], acc[1]}, L + L_H + voff, rw, mb, voff);
          __builtin_amdgcn_s_setprio(1);
          StoreHook hk{{ppack(acc[0][0], acc[1][0]), ppack(acc[0][1], acc[1][1]), ppack(acc[0][2], acc[1][2]), ppack(acc[0][3], acc[1][3])}, rs, voff, poff(0), pstr(0)};
          seg<2, 4, 0, 18, 126, 162, true>({acc[0], acc[1]}, L + L_H + voff, rw, mb, voff, hk);
          *(volatile LAS unsigned*)(L + L_W + 16 + hl * 4) = Ly + 1;
          pstore2(acc[0], acc[1], 1, I0);
        }
        u64 fv = 0;
        if (hl == 1) fv = __hip_atomic_load(flagp(ws, (5 * Ly + 2) * 32 + c, lane & 7), RLX_AGENT);
        arrive<4>(ws, L, 1, (5 * Ly + 3) * 32 + c, j, tag, lane);
        if (hl == 1) {
          if (!__all(fv == tag)) poll8<0>(ws, 5 * Ly + 2, c, c, tag, lane);
          *(volatile LAS unsigned*)(L + L_W + 8) = Ly + 1;
        }
        if (Ly == 0) fillh<0>(rw, wq0, voff);
        lds_wait_ge<1>(L, 11, 4 * (Ly + 1));
        f32x4 sa, sb;
        base(sa, sb, 2 * j + 1, hl, 4 + hl);
        SB();
        if (hl == 0) { poll8<0>(ws, 5 * Ly + 3, c, c, tag, lane); *(volatile LAS unsigned*)(L + L_W + 12) = Ly + 1; }
        else lds_wait_ge<1>(L, 3, Ly + 1);
        finish(std::integral_constant<int, 7>{}, sa, sb, 2 * j + 1, hl, 4 + hl, nomid);
        __builtin_amdgcn_s_setprio(0);
      }
      if (Ly == 0) {
        const int cprev = c >= 2 ? c - 2 : c;
        arrive<8>(ws, L, 14, 4 * 32 + c, j, tag, lane);
        if (role == 0) { lds_wait_ge<2>(L, 14, 8); fillh<0>(rw, wq0, voff); }
        if (role) { fillh<1>(rw, wq0, voff); __builtin_amdgcn_s_setprio(1); }
        await(ws, 4, c, cprev, tag, w, lane);
        stage(rs, X2_OFF + c * XBC, X2_OFF + cprev * XBC + 3 * REV, L, role ^ 1, hl, lane);
        if (role == 0) fillh<1>(rw, wq0, voff);
        NoHook nh;
        qkv(std::integral_constant<int, 1>{}, nh);
      }
    }
  }
}

extern "C" void kernel_launch(void* const* d_in, const int* in_sizes, int n_in, void* d_out, int out_size, void* d_ws,
                              size_t ws_size, hipStream_t stream) {
  const u16* x = (const u16*)d_in[0];
  const u16* wqkv = (const u16*)d_in[1];
  const u16* wo = (const u16*)d_in[2];
  const u16* w1 = (const u16*)d_in[4];
  const u16* b1 = (const u16*)d_in[5];
  const u16* w2 = (const u16*)d_in[6];
  const u16* b2 = (const u16*)d_in[7];
  (void)hipFuncSetAttribute((const void*)k_main, hipFuncAttributeMaxDynamicSharedMemorySize, LDS_BYTES);
  k_main<<<256, 512, LDS_BYTES, stream>>>((char*)d_ws, x, wqkv, wo, w1, b1, w2, b2, (u16*)d_out);
}
```

```cpp
#include <hip/hip_runtime.h>
#include <hip/hip_bf16.h>
#include <type_traits>

#define GAS __attribute__((address_space(1)))
#define LAS __attribute__((address_space(3)))
typedef unsigned short u16;
typedef unsigned v4u __attribute__((ext_vector_type(4)));
typedef unsigned v2u __attribute__((ext_vector_type(2)));
typedef float f32x4 __attribute__((ext_vector_type(4)));
typedef float f32x2 __attribute__((ext_vector_type(2)));
typedef short bf16x8 __attribute__((ext_vector_type(8)));
typedef __bf16 bf16x2 __attribute__((ext_vector_type(2)));
typedef GAS unsigned gu32;
typedef unsigned long long u64;
typedef GAS u64 gu64;
typedef __amdgpu_buffer_rsrc_t rsrc_t;
#define RLX_AGENT __ATOMIC_RELAXED, __HIP_MEMORY_SCOPE_AGENT

#define TRICKLE 4
#define G2PK 0
#define GOS 8
#define JR1 1
#define PV 3
#define PVS 1
#define EARLY 8
#define G1PK 0
#define F4PK 0
#define FALL4 0
#define WOF 1
#define QP 1
#define XD2 2
#define WIN1 0
#define GSPREAD 2

constexpr int S_ = 2048, D_ = 576, F_ = 4608;
constexpr int REV = 18 * 1024;
constexpr int XBC = 4 * REV;
constexpr int XBSZ = 32 * XBC;
constexpr int FL_OFF = 0;
constexpr int WQ_OFF = 131072;
constexpr int WO_OFF = WQ_OFF + 128 * REV;
constexpr int W1_OFF = WO_OFF + 64 * REV;
constexpr int W2_OFF = W1_OFF + 288 * REV;
constexpr int CSP_OFF = W2_OFF + 288 * REV;
constexpr int X2_OFF = CSP_OFF + 8 * 18 * 2304 + 4096 - (8 * 18 * 2304) % 4096;
constexpr int OB_OFF = X2_OFF + XBSZ;
constexpr int X1_OFF = OB_OFF + XBSZ;
constexpr int PB_OFF = X1_OFF + XBSZ;
constexpr int PBSZ = 32 * 8 * 8 * 9216;
constexpr int WS_BYTES = PB_OFF + PBSZ;
constexpr int L_X = 0, L_H = 73728, L_ATT = 92160, ATT_STR = 10240, A_Q = 0, A_K = 2560, A_V = 5120, A_E = 7680;
constexpr int L_RES = 137216, L_OWN = 147456, L_CS1 = L_OWN + 8192, L_B1 = L_CS1 + 2304, L_SP = L_B1 + 2304,
              L_SH = L_SP + 2048, L_B2 = L_SH + 768, L_W = L_B2 + 384, L_END = L_W + 128;
static_assert(L_END <= 163840, "lds");
constexpr int LDS_BYTES = 163840;
constexpr int CODE_SHARE = 2176;

__device__ __forceinline__ float bflo(unsigned u) { return __uint_as_float(u << 16); }
__device__ __forceinline__ float bfhi(unsigned u) { return __uint_as_float(u & 0xffff0000u); }
__device__ __forceinline__ unsigned pk2(float a, float b) {
  f32x2 v = {a, b};
  return __builtin_bit_cast(unsigned, __builtin_convertvector(v, bf16x2));
}
__device__ __forceinline__ v2u pk4(f32x4 v) { return v2u{pk2(v[0], v[1]), pk2(v[2], v[3])}; }
__device__ __forceinline__ f32x4 unpk4(unsigned a, unsigned b) { return f32x4{bflo(a), bfhi(a), bflo(b), bfhi(b)}; }
__device__ __forceinline__ f32x4 mfma(v4u a, v4u b, f32x4 c) {
  return __builtin_amdgcn_mfma_f32_16x16x32_bf16(__builtin_bit_cast(bf16x8, a), __builtin_bit_cast(bf16x8, b), c, 0, 0, 0);
}
__device__ __forceinline__ v4u ldc(rsrc_t r, int voff, int soff) { return __builtin_amdgcn_raw_buffer_load_b128(r, voff, soff, 16); }
__device__ __forceinline__ void stc128(v4u v, rsrc_t r, int voff, int soff) {
  __builtin_amdgcn_raw_buffer_store_b128(v, r, voff, soff, 16);
  asm volatile("s_nop 1" :: "v"(v));
}
__device__ __forceinline__ v2u ldc64(rsrc_t r, int voff, int soff) { return __builtin_amdgcn_raw_buffer_load_b64(r, voff, soff, 16); }
__device__ __forceinline__ void stc64(v2u v, rsrc_t r, int voff, int soff) { __builtin_amdgcn_raw_buffer_store_b64(v, r, voff, soff, 16); }
__device__ __forceinline__ void stc32(unsigned v, rsrc_t r, int voff, int soff) { __builtin_amdgcn_raw_buffer_store_b32(v, r, voff, soff, 16); }
#define SB() __builtin_amdgcn_sched_barrier(0)
__device__ __forceinline__ void bar() {
  asm volatile("s_waitcnt lgkmcnt(0)" ::: "memory");
  __builtin_amdgcn_s_barrier();
  asm volatile("" ::: "memory");
}
__device__ __forceinline__ float red16max(float x) {
  auto r = __builtin_amdgcn_permlane16_swap(__float_as_uint(x), __float_as_uint(x), false, false);
  x = fmaxf(__uint_as_float(r[0]), __uint_as_float(r[1]));
  auto q = __builtin_amdgcn_permlane32_swap(__float_as_uint(x), __float_as_uint(x), false, false);
  return fmaxf(__uint_as_float(q[0]), __uint_as_float(q[1]));
}
__device__ __forceinline__ float red16sum(float x) {
  auto r = __builtin_amdgcn_permlane16_swap(__float_as_uint(x), __float_as_uint(x), false, false);
  x = __uint_as_float(r[0]) + __uint_as_float(r[1]);
  auto q = __builtin_amdgcn_permlane32_swap(__float_as_uint(x), __float_as_uint(x), false, false);
  return __uint_as_float(q[0]) + __uint_as_float(q[1]);
}

__device__ __forceinline__ gu64* flagp(char* ws, int line, int i) { return (gu64*)(ws + FL_OFF + 256 + line * 256 + i * 8); }
__device__ __forceinline__ void publish(char* ws, int line, int i, u64 tag, int tid) {
  asm volatile("s_waitcnt vmcnt(0)" ::: "memory");
  bar();
  if (tid == 0) __hip_atomic_store(flagp(ws, line, i), tag, RLX_AGENT);
}
template <int SL> __device__ __forceinline__ void poll(gu64* p, u64 tag) {
#pragma unroll 1
  for (unsigned spins = 0; spins < (1u << 19); ++spins) {
    const u64 v = __hip_atomic_load(p, RLX_AGENT);
    if (__all(v == tag)) break;
    if (SL) __builtin_amdgcn_s_sleep(SL);
  }
  __builtin_amdgcn_fence(__ATOMIC_ACQUIRE, "wavefront");
}
template <int SL> __device__ __forceinline__ void poll8(char* ws, int ph, int c, int c2, u64 tag, int lane) {
  poll<SL>(flagp(ws, ph * 32 + ((lane & 8) ? c2 : c), lane & 7), tag);
}
__device__ __forceinline__ void await(char* ws, int ph, int c, int c2, u64 tag, int w, int lane) {
  if (w == 0) poll8<0>(ws, ph, c, c2, tag, lane);
  bar();
}

__device__ __forceinline__ float sfma(float a, float b, float c) { float d; asm("v_fma_f32 %0, %1, %2, %3" : "=v"(d) : "v"(a), "v"(b), "v"(c)); return d; }
__device__ __forceinline__ float sfnma(float a, float b, float c) { float d; asm("v_fma_f32 %0, -%1, %2, %3" : "=v"(d) : "v"(a), "v"(b), "v"(c)); return d; }
__device__ __forceinline__ float sfmas(float a, float s) { float d; asm("v_fma_f32 %0, %1, %2, %2" : "=v"(d) : "v"(a), "s"(s)); return d; }
__device__ __forceinline__ float ssqs(float a, float s) { float d; asm("v_fma_f32 %0, %1, %1, %2" : "=v"(d) : "v"(a), "s"(s)); return d; }
__device__ __forceinline__ float smul(float a, float b) { float d; asm("v_mul_f32 %0, %1, %2" : "=v"(d) : "v"(a), "v"(b)); return d; }
__device__ __forceinline__ float sadd(float a, float b) { float d; asm("v_add_f32 %0, %1, %2" : "=v"(d) : "v"(a), "v"(b)); return d; }
__device__ __forceinline__ void stat2(v4u x, float& s1, float& s2) {
  const bf16x2 ones = __builtin_bit_cast(bf16x2, 0x3f803f80u);
#pragma unroll
  for (int i = 0; i < 4; ++i) {
    const unsigned u = x[i];
    const bf16x2 v = __builtin_bit_cast(bf16x2, u);
    s1 = __builtin_amdgcn_fdot2_f32_bf16(v, ones, s1, false);
    s2 = __builtin_amdgcn_fdot2_f32_bf16(v, v, s2, false);
  }
}
__device__ __forceinline__ void stat_fin(float s1, float s2, float scale, float& rstd, float& mr) {
  const float inv = 1.0f / D_, mean = smul(s1, inv);
  const float var = fmaxf(sfnma(mean, mean, smul(s2, inv)), 0.f);
  float r = __builtin_amdgcn_rsqf(sadd(var, 1e-5f));
  asm volatile("s_nop 0" : "+v"(r));
  rstd = smul(r, scale);
  mr = smul(mean, rstd);
}
__device__ __forceinline__ void stat_own(const LAS char* L, int tt, int m, float scale, float& rstd, float& mr) {
  const f32x4 p = *(const LAS f32x4*)(L + L_SP + tt * 256 + m * 16);
  stat_fin(sadd(p[0], p[2]), sadd(p[1], p[3]), scale, rstd, mr);
}
__device__ __forceinline__ void stat_own4(const LAS char* L, int tt, int m, float scale, float& rstd, float& mr) {
  const f32x4 p = *(const LAS f32x4*)(L + L_SP + tt * 256 + m * 16), q = *(const LAS f32x4*)(L + L_SP + 1024 + tt * 256 + m * 16);
  stat_fin(sadd(sadd(p[0], p[2]), sadd(q[0], q[2])), sadd(sadd(p[1], p[3]), sadd(q[1], q[3])), scale, rstd, mr);
}
__device__ __forceinline__ void stat_halo(const LAS char* L, int m, float& rstd, float& mr) {
  const f32x4 p = *(const LAS f32x4*)(L + L_SH + m * 48), q = *(const LAS f32x4*)(L + L_SH + m * 48 + 16), r = *(const LAS f32x4*)(L + L_SH + m * 48 + 32);
  stat_fin(sadd(sadd(sadd(p[0], p[2]), sadd(q[0], q[2])), sadd(r[0], r[2])), sadd(sadd(sadd(p[1], p[3]), sadd(q[1], q[3])), sadd(r[1], r[3])), 1.0f, rstd, mr);
}
__device__ __forceinline__ void ones_row(LAS char* L, int lane) {
  if ((lane & 15) == 0) {
#pragma unroll
    for (int s = 0; s < 18; ++s) *(LAS v4u*)(L + L_X + s * 1024 + lane * 16) = v4u{0x3f803f80u, 0x3f803f80u, 0x3f803f80u, 0x3f803f80u};
  }
}
template <int CTRL> __device__ __forceinline__ float dppadd(float x) {
  return x + __uint_as_float(__builtin_amdgcn_update_dpp(0u, __float_as_uint(x), CTRL, 0xf, 0xf, true));
}
__device__ __forceinline__ float red8sum(float x) { return dppadd<0x141>(dppadd<0x4E>(dppadd<0xB1>(x))); }
__device__ __forceinline__ void stage(rsrc_t rs, int xo, int ho, LAS char* L, int kh, int tt, int lane) {
  const int voff = lane * 16, hw = kh * 3 + min(tt, 2);
  v4u x[9], h[3];
#pragma unroll
  for (int s = 0; s < 3; ++s) h[s] = ldc(rs, voff + s * 1024, ho + hw * 3072);
#pragma unroll
  for (int s = 0; s < 9; ++s) x[s] = ldc(rs, voff + (s & 3) * 1024, xo + tt * REV + kh * 9216 + (s >> 2) * 4096);
  SB();
  const v4u ones = {0x3f803f80u, 0x3f803f80u, 0x3f803f80u, 0x3f803f80u};
  float s1[3] = {0.f, 0.f, 0.f}, s2[3] = {0.f, 0.f, 0.f};
#pragma unroll
  for (int s = 0; s < 3; ++s) {
    *(LAS v4u*)(L + L_X + hw * 3072 + s * 1024 + voff) = (lane & 15) ? h[s] : ones;
    stat2(h[s], s1[2], s2[2]);
    SB();
  }
#pragma unroll
  for (int s = 0; s < 9; ++s) {
    *(LAS v4u*)(L + L_X + (tt + 1) * REV + kh * 9216 + s * 1024 + voff) = x[s];
    stat2(x[s], s1[s & 1], s2[s & 1]);
    SB();
  }
  const float a = red16sum(sadd(s1[0], s1[1])), b = red16sum(sadd(s2[0], s2[1])), ha = red16sum(s1[2]), hb = red16sum(s2[2]);
  if (lane < 16) {
    *(LAS f32x2*)(L + L_SP + tt * 256 + lane * 16 + kh * 8) = f32x2{a, b};
    *(LAS f32x2*)(L + L_SH + lane * 48 + hw * 8) = f32x2{ha, hb};
  }
  asm volatile("s_waitcnt lgkmcnt(0)" ::: "memory");
  if (lane == 0) __hip_atomic_fetch_add((LAS unsigned*)(L + L_W + 96 + kh * 4), 1u, __ATOMIC_RELAXED, __HIP_MEMORY_SCOPE_WORKGROUP);
}

__device__ __forceinline__ void stage8(rsrc_t rs, int xo, LAS char* L, int j, int w, int lane) {
  const int tt = w & 3, kh = w >> 2, voff = lane * 16;
  v4u x[8];
#pragma unroll
  for (int i = 0; i < 4; ++i) {
    const int q = 2 + 8 * kh + 2 * i, ks = q < 16 ? (q + 2 * j) & 15 : q;
    x[2 * i] = ldc(rs, voff, xo + tt * REV + ks * 1024);
    x[2 * i + 1] = ldc(rs, voff + 1024, xo + tt * REV + ks * 1024);
  }
  SB();
  float s1[2] = {0.f, 0.f}, s2[2] = {0.f, 0.f};
#pragma unroll
  for (int s = 0; s < 8; ++s) {
    *(LAS v4u*)(L + L_X + tt * REV + (2 + 8 * kh + s) * 1024 + voff) = x[s];
    stat2(x[s], s1[s & 1], s2[s & 1]);
    SB();
  }
  const float a = red16sum(s1[0] + s1[1]), b = red16sum(s2[0] + s2[1]);
  if (lane < 16) *(LAS f32x2*)(L + L_SP + tt * 256 + lane * 16 + kh * 8) = f32x2{a, b};
  asm volatile("s_waitcnt lgkmcnt(0)" ::: "memory");
  if (lane == 0) __hip_atomic_fetch_add((LAS unsigned*)(L + L_W + 64 + kh * 4), 1u, __ATOMIC_RELAXED, __HIP_MEMORY_SCOPE_WORKGROUP);
}

#define RING 28
constexpr int R_ = RING;
constexpr int RB = 256 - 4 * R_;
template <int SLOT, int OFF> __device__ __forceinline__ void rload(v4u rs, int voff, int so) {
  asm volatile("buffer_load_dwordx4 v[%c3:%c4], %0, %1, %2 offen offset:%c5 sc1"
               :: "v"(voff), "s"(rs), "s"(so), "i"(RB + 4 * SLOT), "i"(RB + 4 * SLOT + 3), "i"(OFF));
}
template <int SLOT, int OFF> __device__ __forceinline__ void rput(unsigned lds) {
  asm volatile("ds_write_b128 %0, v[%c1:%c2] offset:%c3" :: "v"(lds), "i"(RB + 4 * SLOT), "i"(RB + 4 * SLOT + 3), "i"(OFF) : "memory");
}
template <int N> __device__ __forceinline__ void rwait() { asm volatile("s_waitcnt vmcnt(%c0)" :: "i"(N)); }
template <int SLOT> __device__ __forceinline__ void rmfma(f32x4& acc, v4u x) {
  asm volatile("v_mfma_f32_16x16x32_bf16 %0, v[%c2:%c3], %1, %0" : "+v"(acc) : "v"(x), "i"(RB + 4 * SLOT), "i"(RB + 4 * SLOT + 3));
}
template <int SLOT> __device__ __forceinline__ void rmfma0(f32x4& acc, v4u x) {
  asm volatile("v_mfma_f32_16x16x32_bf16 %0, v[%c2:%c3], %1, 0" : "=&v"(acc) : "v"(x), "i"(RB + 4 * SLOT), "i"(RB + 4 * SLOT + 3));
}
template <int I, int N, class Fn> __device__ __forceinline__ void sfor(Fn&& f) {
  if constexpr (I < N) { f(std::integral_constant<int, I>{}); sfor<I + 1, N>(f); }
}
constexpr int cmin(int a, int b) { return a < b ? a : b; }
struct StreamQ { int b[1]; static constexpr int id(int) { return 0; } static constexpr int kb(int P) { return P; } };
struct StreamW {
  int b[3];
  static constexpr int id(int P) { return P < 36 ? 0 : P < 54 ? 1 : 2; }
  static constexpr int kb(int P) { return P < 36 ? P : P < 54 ? P - 36 : P - 54; }
};
struct StreamF {
  int b[4];
  static constexpr int E = FALL4 ? 18 : EARLY, P1 = 72 + 4 * E, N2 = FALL4 ? 2 : 2 * (18 - E);
  static constexpr int id(int P) { return P < 72 ? 0 : P < P1 ? 1 + ((P - 72) % 4) / 2 : P < 144 ? 1 + (P - P1) / N2 : 3; }
  static constexpr int kb(int P) { return P < 72 ? (P % 4) * 18 + P / 4 : P < P1 ? (P % 2) * 18 + (P - 72) / 4 : P < 144 ? (P % 2) * 18 + E + ((P - P1) % N2) / 2 : P - 144; }
};
struct StreamS {
  int b[4];
  static constexpr int id(int P) { return P < 90 ? (P % 5 < 4 ? 0 : 3) : 1 + (P - 90) / 36; }
  static constexpr int kb(int P) { return P < 90 ? (P % 5 < 4 ? ((P % 5) % 2 + 8 * ((P % 5) / 2)) * 18 : 0) + P / 5 : (P % 2) * 18 + ((P - 90) % 36) / 2; }
};
template <int P, class St> __device__ __forceinline__ void sload(v4u r, const St& st, int voff) {
  rload<P % R_, (St::kb(P) & 3) * 1024>(r, voff, st.b[St::id(P)] + (St::kb(P) >> 2) * 4096);
}
template <int P0, int N, class St> __device__ __forceinline__ void fillr(v4u r, const St& st, int voff) {
  sfor<P0, P0 + N>([&](auto pc) { sload<pc.value>(r, st, voff); });
  SB();
}
template <int P, int TOTAL, class St> __device__ __forceinline__ void refill(v4u r, const St& st, int voff) {
  if constexpr (P + R_ < TOTAL) sload<P + R_>(r, st, voff);
}
__device__ __forceinline__ StreamQ mkQ(int j, int hl, int role) { return StreamQ{{WQ_OFF + ((4 * j + hl) * 4 + 2 * role) * REV}}; }
__device__ __forceinline__ int w2pair(int j, int d, int role) { return W2_OFF + (36 * j + (d != j ? 4 * d : 32) + 2 * role) * REV; }
__device__ __forceinline__ StreamF mkF(int j, int hl) { return StreamF{{W1_OFF + (36 * j + 4 * hl) * REV, w2pair(j, 2 * hl, 0), w2pair(j, 2 * hl + 1, 0), W2_OFF + (40 * j + hl) * REV}}; }
__device__ __forceinline__ StreamS mkS(int j, int hl) { return StreamS{{W1_OFF + (36 * j + 16 + 2 * hl) * REV, w2pair(j, 2 * hl, 1), w2pair(j, 2 * hl + 1, 1), W1_OFF + (36 * j + 32 + hl) * REV}}; }
struct NoHook { template <int S> static constexpr int extra() { return 0; } template <int S> __device__ __forceinline__ void at() {} };
template <int NT, int NK, int S0, int S1, int P0, int TOTAL, bool INIT, int I0, int NKL, class St, class Hook>
__device__ __forceinline__ void segx(f32x4* const (&acc)[NT], const LAS char* const (&xb)[NK], v4u r, const St& mb, int voff, Hook& hook) {
  constexpr int XD = NT >= 4 ? 1 : (NT == 1 ? 4 : (NK == 4 ? XD2 : 2));
  v4u xf[XD][NK];
#pragma unroll
  for (int d = 0; d < XD; ++d)
#pragma unroll
    for (int tt = 0; tt < NK; ++tt)
      if (S0 + d < S1) xf[d][tt] = *(const LAS v4u*)(xb[tt] + (S0 + d) * 1024);
  SB();
  asm volatile("s_nop 1");
  sfor<S0, S1>([&](auto sc) {
    constexpr int s = sc.value, p = P0 + (s - S0) * NT, d = (s - S0) % XD;
    constexpr int RS = (R_ - I0 + 3) / 4;
    constexpr int lag = RS > 1 ? RS : 1;
    constexpr int k = s - S0;
    constexpr int issued = k < RS ? I0 + 4 * k : cmin(TOTAL, R_ + (k < lag ? P0 : p - lag * NT));
    rwait<issued - (p + NT) + Hook::template extra<s>()>();
    if constexpr (k < RS) sfor<I0 + 4 * k, cmin(R_, I0 + 4 * k + 4)>([&](auto qc) { sload<qc.value>(r, mb, voff); });
    hook.template at<s>();
    sfor<0, NK>([&](auto kc) {
      constexpr int tt = kc.value;
      sfor<0, NT>([&](auto tc) {
        constexpr int t = tc.value;
        if constexpr (t == NT - 1 && tt >= NKL) {}
        else if constexpr (INIT && s == S0) rmfma0<(p + t) % R_>(acc[t][tt], xf[d][tt]);
        else rmfma<(p + t) % R_>(acc[t][tt], xf[d][tt]);
        constexpr int idx = tt * NT + t;
        if constexpr (k >= lag && idx % NK == 0 && idx / NK < NT) refill<p - lag * NT + idx / NK, TOTAL>(r, mb, voff);
      });
      SB();
      if constexpr (s + XD < S1) { xf[d][tt] = *(const LAS v4u*)(xb[tt] + (s + XD) * 1024); SB(); }
    });
  });
  constexpr int LAG = (R_ - I0 + 3) / 4 > 1 ? (R_ - I0 + 3) / 4 : 1;
  sfor<0, LAG * NT>([&](auto tc) { refill<P0 + (S1 - LAG - S0) * NT + tc.value, TOTAL>(r, mb, voff); });
  asm volatile("s_nop 7\n\ts_nop 1");
  SB();
}
template <int NT, int NK, int S0, int S1, int P0, int TOTAL, bool INIT, class St, class Hook = NoHook>
__device__ __forceinline__ void seg(f32x4* const (&acc)[NT], const LAS char* xb, v4u r, const St& mb, int voff, Hook hook = Hook{}) {
  const LAS char* x2 = xb + 2 * REV;
  asm volatile("" : "+v"(x2));
  segx<NT, 4, S0, S1, P0, TOTAL, INIT, R_, 4>(acc, {xb, xb + REV, x2, x2 + REV}, r, mb, voff, hook);
}
template <int LO, class St> __device__ __forceinline__ void fillh(v4u r, const St& st, int voff) { fillr<LO ? R_ / 2 : 0, R_ / 2>(r, st, voff); }
__device__ __forceinline__ v2u trread(const LAS char* p) {
  typedef short s4 __attribute__((ext_vector_type(4)));
  return __builtin_bit_cast(v2u, __builtin_amdgcn_ds_read_tr16_b64_v4i16((LAS s4*)p));
}
constexpr float AL = 0.46866869f, KK = 2.3022082f / AL;
#define EACH16 _Pragma("unroll") for (int tt = 0; tt < 4; ++tt) _Pragma("unroll") for (int r = 0; r < 4; ++r)
__device__ __forceinline__ void gelu16(f32x4 (&a)[4], const float (&rstd)[4], const float (&mr)[4], f32x4 cs, f32x4 bb, v2u (&o)[4]) {
  float e[4][4], u[4][4];
  EACH16 e[tt][r] = sfnma(cs[r], mr[tt], bb[r]);
  SB();
  EACH16 u[tt][r] = sfma(a[tt][r], rstd[tt], e[tt][r]);
  SB();
  EACH16 e[tt][r] = ssqs(u[tt][r], KK);
  SB();
  EACH16 e[tt][r] = smul(e[tt][r], u[tt][r]);
  SB();
  EACH16 e[tt][r] = __builtin_amdgcn_exp2f(-e[tt][r]);
  SB();
  EACH16 e[tt][r] = sfmas(e[tt][r], AL);
  SB();
  EACH16 e[tt][r] = __builtin_amdgcn_rcpf(e[tt][r]);
  SB();
  EACH16 u[tt][r] = smul(u[tt][r], e[tt][r]);
  SB();
#pragma unroll
  for (int tt = 0; tt < 4; ++tt) o[tt] = v2u{pk2(u[tt][0], u[tt][1]), pk2(u[tt][2], u[tt][3])};
  SB();
}
__device__ __forceinline__ void gelu16p(f32x4 (&a)[4], const float (&rstd)[4], const float (&mr)[4], f32x4 cs, f32x4 bb, v2u (&o)[4]) {
  f32x4 e[4];
#pragma unroll
  for (int tt = 0; tt < 4; ++tt) e[tt] = bb - cs * mr[tt];
  SB();
#pragma unroll
  for (int tt = 0; tt < 4; ++tt) a[tt] = a[tt] * rstd[tt] + e[tt];
  SB();
#pragma unroll
  for (int tt = 0; tt < 4; ++tt) e[tt] = a[tt] * a[tt] + KK;
  SB();
#pragma unroll
  for (int tt = 0; tt < 4; ++tt) e[tt] = e[tt] * a[tt];
  SB();
  EACH16 e[tt][r] = __builtin_amdgcn_exp2f(-e[tt][r]);
  SB();
#pragma unroll
  for (int tt = 0; tt < 4; ++tt) e[tt] = e[tt] * AL + AL;
  SB();
  EACH16 e[tt][r] = __builtin_amdgcn_rcpf(e[tt][r]);
  SB();
#pragma unroll
  for (int tt = 0; tt < 4; ++tt) a[tt] = a[tt] * e[tt];
  SB();
#pragma unroll
  for (int tt = 0; tt < 4; ++tt) o[tt] = pk4(a[tt]);
  SB();
}
struct StoreHook {
  v4u d[4]; rsrc_t rs; int voff, po0, po1;
  template <int S> static constexpr int extra() { return S <= 6 ? 0 : S <= 9 ? 1 : S <= 12 ? 2 : S <= 15 ? 3 : 4; }
  template <int S> __device__ __forceinline__ void at() {
    if constexpr (S == 6 || S == 9 || S == 12 || S == 15) {
      constexpr int q = (S - 6) / 3;
      SB(); stc128(d[q], rs, voff, po0 + q * po1); SB();
    }
  }
};
template <int SL> __device__ __forceinline__ void lds_wait_ge(LAS char* L, int word, unsigned want) {
  volatile LAS unsigned* p = (volatile LAS unsigned*)(L + L_W + word * 4);
#pragma unroll 1
  for (unsigned spins = 0; spins < (1u << 22); ++spins) {
    if (__builtin_amdgcn_readfirstlane(*p) >= want) break;
    if (SL) __builtin_amdgcn_s_sleep(SL);
  }
  asm volatile("" ::: "memory");
}
template <int N> __device__ __forceinline__ void arrive(char* ws, LAS char* L, int word, int line, int i, u64 tag, int lane) {
  asm volatile("s_waitcnt vmcnt(0)" ::: "memory");
  if (lane == 0) {
    const unsigned old = __hip_atomic_fetch_add((LAS unsigned*)(L + L_W + word * 4), 1u, __ATOMIC_RELAXED, __HIP_MEMORY_SCOPE_WORKGROUP);
    if (old % N == N - 1) __hip_atomic_store(flagp(ws, line, i), tag, RLX_AGENT);
  }
}
__device__ __forceinline__ void acc8(f32x4& a, f32x4& b, v4u p) {
  a[0] = sadd(a[0], bflo(p[0])); a[1] = sadd(a[1], bfhi(p[0])); a[2] = sadd(a[2], bflo(p[1])); a[3] = sadd(a[3], bfhi(p[1]));
  b[0] = sadd(b[0], bflo(p[2])); b[1] = sadd(b[1], bfhi(p[2])); b[2] = sadd(b[2], bflo(p[3])); b[3] = sadd(b[3], bfhi(p[3]));
}

#define FRESH() int lane = lane0, c = c0, j = j0, w = w0; asm volatile("" : "+v"(lane)); asm volatile("" : "+s"(c), "+s"(j), "+s"(w)); \
  const int g = lane >> 4, m = lane & 15, voff = lane * 16, role = w >> 2, hl = w & 3; (void)g; (void)m; (void)voff; (void)role; (void)hl;

template <bool CS> __device__ __forceinline__ unsigned jtr(v4u* r, LAS char* scr, int lane) {
  const int g = lane >> 4, m = lane & 15;
#pragma unroll
  for (int i = 0; i < 4; ++i) *(LAS v4u*)(scr + i * 1024 + lane * 16) = r[i];
  const LAS char* p = scr + (4 * g + (m >> 2)) * 128 + (m & 3) * 8;
  float cs4[4] = {0.f, 0.f, 0.f, 0.f};
#pragma unroll
  for (int t = 0; t < 4; ++t) { const v2u lo = trread(p + t * 32), hi = trread(p + t * 32 + 2048); r[t] = v4u{lo[0], lo[1], hi[0], hi[1]}; }
  if (CS) {
#pragma unroll
    for (int t = 0; t < 4; ++t) { float dummy = 0.f; stat2(r[t], cs4[t], dummy); cs4[t] = red16sum(cs4[t]); }
  }
  return __float_as_uint(g < 2 ? (g == 0 ? cs4[0] : cs4[1]) : (g == 2 ? cs4[2] : cs4[3]));
}
struct JobHook {
  v4u f[8]; unsigned cs; rsrc_t rs; int lane, d1, d2, dc;
  template <int S> static constexpr int extra() { return S <= 9 ? 0 : S - 9; }
  template <int S> __device__ __forceinline__ void at() {
    if constexpr (S == 9) { SB(); stc32(cs, rs, lane * 4, dc); SB(); }
    if constexpr (S > 9) { SB(); stc128(f[S - 10], rs, lane * 16, (S < 14 ? d1 : d2) + ((S - 10) & 3) * REV); SB(); }
  }
};
struct HalfHook {
  LAS char* L;
  template <int S> static constexpr int extra() { return 0; }
  template <int S> __device__ __forceinline__ void at() { if constexpr (S == 7) { SB(); lds_wait_ge<1>(L, 25, 4u); SB(); } }
};
struct WordHook {
  LAS char* L; unsigned want; int hl;
  template <int S> static constexpr int extra() { return 0; }
  template <int S> __device__ __forceinline__ void at() {
    if constexpr (S == 9) { SB(); lds_wait_ge<1>(L, 17, want); SB(); }
    if constexpr (S == GOS) { SB(); *(volatile LAS unsigned*)(L + L_W + 80 + hl * 4) = want; SB(); }
  }
};

__global__ __launch_bounds__(512, 2) __attribute__((amdgpu_num_vgpr(RB / 2 - 1)))
void k_main(char* __restrict__ ws, const u16* __restrict__ xin, const u16* __restrict__ wqkv, const u16* __restrict__ wo,
            const u16* __restrict__ w1, const u16* __restrict__ b1, const u16* __restrict__ w2, const u16* __restrict__ b2,
            u16* __restrict__ out) {
  extern __shared__ __attribute__((aligned(16))) char smem[];
  {
    u64 pc;
    asm volatile("s_getpc_b64 %0\n\tglobal_load_dword v%c2, %1, %0" : "=&s"(pc)
                 : "v"(((blockIdx.x >> 3) + min((int)threadIdx.x, CODE_SHARE / 128 - 1) * 32) * 128), "i"(RB - 1));
    SB();
  }
  LAS char* L = (LAS char*)smem;
  const int tid = threadIdx.x, lane0 = tid & 63, w0 = __builtin_amdgcn_readfirstlane(tid >> 6);
  int c0, j0;
  {
    constexpr int G = GSPREAD, per = 8 / G;
    const int b = blockIdx.x, x = b & 7, r = b >> 3;
    j0 = (x % G) * per + (r % per);
    c0 = (x / G) * (4 * G) + r / per;
  }
  const rsrc_t rs = __builtin_amdgcn_make_buffer_rsrc((void*)ws, 0, WS_BYTES, 0x00020000);
  const v4u rw = {(unsigned)(size_t)ws, (unsigned)((size_t)ws >> 32) & 0xffffu, (unsigned)WS_BYTES, 0x00020000u};
  asm volatile("" ::: "v255");
  u64 tag = 0;
  auto qkv = [&](auto lc, auto& jh) {
    constexpr int Ly = lc.value;
    FRESH();
    auto keep = [&]() {
      const LAS char* xr = L + L_X + (hl + 1) * REV + voff;
      const v4u ra = *(const LAS v4u*)(xr + (2 * j + role) * 1024), rb = *(const LAS v4u*)(xr + (16 + (j >> 2)) * 1024);
      *(LAS v4u*)(L + L_RES + w * 1024 + voff) = ra;
      if (w == (j & 3)) *(LAS v4u*)(L + L_RES + 8192 + voff) = rb;
    };
    const StreamQ qb = mkQ(j, hl, role);
    constexpr int I0 = Ly == 0 ? 8 : R_;
    auto fix = [&](f32x4 a, const float (&cs)[4], float rstd, float mr) {
      float u[4];
#pragma unroll
      for (int r = 0; r < 4; ++r) u[r] = smul(a[r], rstd);
#pragma unroll
      for (int r = 0; r < 4; ++r) u[r] = sfnma(cs[r], mr, u[r]);
      return v2u{pk2(u[0], u[1]), pk2(u[2], u[3])};
    };
    auto bcast = [&](f32x4 a, float (&cs)[4]) {
#pragma unroll
      for (int r = 0; r < 4; ++r) cs[r] = __uint_as_float(__builtin_amdgcn_update_dpp(0u, __float_as_uint(a[r]), 0x150, 0xf, 0xf, false));
    };
    if (role == 0) {
      f32x4 a0[5], a1[5];
      const LAS char* xa = L + L_X + voff, *xc = xa + 3 * REV;
      asm volatile("" : "+v"(xc));
      if (Ly) { lds_wait_ge<1>(L, 24, 4u); lds_wait_ge<1>(L, 25, 4u); }
      keep();
      float rstd[5], mr[5], cs0[4], cs1[4];
      stat_halo(L, m, rstd[0], mr[0]);
#pragma unroll
      for (int t = 1; t < 5; ++t) stat_own(L, t - 1, m, 1.0f, rstd[t], mr[t]);
      segx<2, 5, 0, 18, 0, 36, true, I0, 5>({a0, a1}, {xa, xa + REV, xa + 2 * REV, xc, xc + REV}, rw, qb, voff, jh);
      LAS char* A = L + L_ATT + hl * ATT_STR + lane * 8;
      bcast(a0[0], cs0); bcast(a1[0], cs1);
      SB();
#pragma unroll
      for (int t = 0; t < 5; ++t) {
        if (t) *(LAS v2u*)(A + A_Q + t * 512) = pk4(a0[t] * rstd[t] - f32x4{cs0[0], cs0[1], cs0[2], cs0[3]} * mr[t]);
        *(LAS v2u*)(A + A_K + t * 512) = pk4(a1[t] * rstd[t] - f32x4{cs1[0], cs1[1], cs1[2], cs1[3]} * mr[t]);
      }
    }
    int role2 = role;
    asm volatile("" : "+s"(role2));
    if (role2) {
      f32x4 a0[5], a1[5];
      const int ta = (hl & 1) ? 3 : 1, tb = 4 - ta;
      const LAS char* x0 = L + L_X + voff, *xa = x0 + ta * REV, *xc = x0 + tb * REV;
      HalfHook hk{L};
      const StreamW qw{{qb.b[0], WO_OFF + (8 * j + hl) * REV, WO_OFF + (8 * j + 4 + 2 * (j >> 2)) * REV + hl * 9216}};
      __builtin_amdgcn_s_setprio(1);
      if (Ly) lds_wait_ge<0>(L, 24, 4u);
      if constexpr (Ly == 0) segx<2, 5, 0, 18, 0, 63, true, I0, 3>({a0, a1}, {x0, xa, xa + REV, xc, xc + REV}, rw, qw, voff, jh);
      else segx<2, 5, 0, 18, 0, WIN1 ? 63 : 36, true, I0, 3>({a0, a1}, {x0, xa, xa + REV, xc, xc + REV}, rw, qw, voff, hk);
      __builtin_amdgcn_s_setprio(0);
      keep();
      LAS char* A = L + L_ATT + hl * ATT_STR + A_V + m * 32 + g * 8, *E = L + L_ATT + (hl & 2) * ATT_STR + A_E + m * 32 + g * 8;
      float rstd[5], mr[5], cs0[4], cs1[4];
      int to[5] = {0, ta, ta + 1, tb, tb + 1};
      stat_halo(L, m, rstd[0], mr[0]);
#pragma unroll
      for (int t = 1; t < 5; ++t) stat_own(L, to[t] - 1, m, 1.0f, rstd[t], mr[t]);
      bcast(a0[0], cs0); bcast(a1[0], cs1);
      SB();
#pragma unroll
      for (int t = 0; t < 5; ++t) {
        *(LAS v2u*)(A + to[t] * 512) = fix(a0[t], cs0, rstd[t], mr[t]);
        if (t < 3) *(LAS v2u*)(E + to[t] * 512) = fix(a1[t], cs1, rstd[t], mr[t]);
      }
      if (Ly && !WIN1) fillr<36, 27>(rw, qw, voff);
    }
  };
  {
    FRESH();
    const int r8 = lane >> 3, q8 = lane & 7;
    JobHook jh;
    jh.rs = rs; jh.lane = lane; jh.d1 = jh.d2 = jh.dc = WS_BYTES;
    u64 gen = 0;
    {
      asm volatile("global_load_dword v%c2, %0, %1" :: "v"(((2 * (64 * (c >> 1) + 8 * j + w) + (c & 1)) * D_ + min(lane, 8) * 64) * 2), "s"(xin), "i"(RB - 1));
    }
    auto jobs = [&](int q0) {
      const int q = min(q0, 161), s = (q * 7282) >> 16, B = q - 9 * s;
      const u16* p1 = w1 + ((32 * s + r8) * F_ + 576 * j + 64 * B + 8 * q8);
      const u16* p2 = w2 + ((576 * j + 32 * s + r8) * D_ + 64 * B + 8 * q8);
#pragma unroll
      for (int i = 0; i < 4; ++i) jh.f[i] = *(const v4u*)(p1 + i * 8 * F_);
#pragma unroll
      for (int i = 0; i < 4; ++i) jh.f[4 + i] = *(const v4u*)(p2 + i * 8 * D_);
      if (q0 < 162) {
        jh.d1 = W1_OFF + (36 * j + 4 * B) * REV + (s < 16 ? (s - 2 * j) & 15 : s) * 1024;
        jh.d2 = W2_OFF + (36 * j + 4 * B) * REV + s * 1024;
        jh.dc = CSP_OFF + (j * 18 + s) * 2304 + B * 256;
      }
      SB();
    };
    if (w >= 5) {
      if (c < 18) {
        if (QP) __builtin_amdgcn_s_setprio(3);
        const int sec = w - 5;
        const u16* src = wqkv + (32 * c) * (3 * D_) + 72 * j;
        LAS char* scr = L + L_ATT + 32768 + sec * 4736;
        v4u qr[5], jo[4];
#pragma unroll
        for (int i = 0; i < 5; ++i) {
          const int ch = min(64 * i + lane, 287), row = (ch * 7282) >> 16;
          qr[i] = *(const v4u*)(src + row * (3 * D_) + sec * D_ + (ch - 9 * row) * 8);
        }
        {
          typedef unsigned v4u8 __attribute__((ext_vector_type(4), aligned(8)));
          const u16* ob = sec == 2 ? src : wo;
          const int e0 = (lane >> 2) * (3 * D_) + min(lane & 3, 2) * D_ + 16;
          const int ra = 36 * c + r8, rb = 36 * (8 * (c - 16) + 2 * (r8 >> 2)) + 16 + (r8 & 3), o0 = (c < 16 ? ra : rb) * D_ + 64 * (sec ? 8 : j) + 8 * q8;
          const int v0 = sec == 2 ? e0 : o0;
          const int s1 = sec == 2 ? 16 * 3 * D_ : (c < 16 ? 8 : 144) * D_, s2 = sec == 2 ? 36 : (c < 16 ? 20 : 36) * D_;
#pragma unroll
          for (int i = 0; i < 4; ++i) jo[i] = *(const v4u8*)(ob + (v0 + (i & 1) * s1 + (i >> 1) * s2));
        }
        SB();
        gen = __hip_atomic_load((gu64*)(ws + FL_OFF), RLX_AGENT);
        SB();
        bar();
#pragma unroll
        for (int i = 0; i < 5; ++i) *(LAS v4u*)(scr + min(64 * i + lane, 287) * 16) = qr[i];
        const int qd = WQ_OFF + 16 * j * REV + (sec >> 1) * 2 * REV + (2 * c + (sec & 1)) * 1024;
#pragma unroll
        for (int h = 0; h < 4; ++h) {
          const LAS char* p = scr + (4 * g + (m >> 2)) * 144 + (h >> 1) * 72 + (h & 1) * 40 + (m & 3) * 8;
          const v2u lo = trread(p), hi = trread(p + 16 * 144);
          stc128(v4u{lo[0], lo[1], hi[0], hi[1]}, rs, voff, qd + h * 4 * REV);
        }
        if (sec == 2) {
#pragma unroll
          for (int i = 0; i < 4; ++i) *(LAS v2u*)(scr + i * 512 + lane * 8) = (lane & 3) == 3 ? v2u{0u, 0u} : v2u{jo[i][0], jo[i][1]};
#pragma unroll
          for (int p = 0; p < 2; ++p) {
            const v2u lo = trread(scr + p * 1024 + lane * 8), hi = trread(scr + p * 1024 + 512 + lane * 8);
            jo[2 * p] = v4u{lo[0], lo[1], hi[0], hi[1]}; jo[2 * p + 1] = jo[2 * p];
          }
        } else jtr<false>(jo, scr, lane);
        const int od = sec == 2 ? qd + 1024 : WO_OFF + (8 * j + 4 * sec) * REV + c * 1024, os = sec == 2 ? 4 * REV : REV;
#pragma unroll
        for (int t = 0; t < 4; ++t) stc128(jo[t], rs, voff, od + t * os);
        arrive<3>(ws, L, 9, 320 + j, c, gen + 1, lane);
        lds_wait_ge<1>(L, 9, 3u);
        __builtin_amdgcn_s_setprio(0);
        jobs(112 + c * 3 + sec);
      } else {
        bar();
        gen = __hip_atomic_load((gu64*)(ws + FL_OFF), RLX_AGENT);
        asm volatile("" : "+v"(gen));
        __builtin_amdgcn_s_sleep(PVS);
        jobs((c - 18) * 8 + w);
      }
    } else {
      if (tid < 32) *(LAS unsigned*)(L + L_W + tid * 4) = 0u;
      bar();
      asm volatile("s_waitcnt vmcnt(0)");
      const int cc = (w == 0 && c >= 2) ? c - 2 : c, tt = w == 0 ? 3 : w - 1;
      const u16* src = xin + ((2 * (64 * (cc >> 1) + 16 * tt + r8) + (cc & 1)) * D_ + 8 * q8);
      v4u x[2][9];
#pragma unroll
      for (int i = 0; i < 9; ++i) { x[0][i] = *(const v4u*)(src + 64 * i); x[1][i] = *(const v4u*)(src + 16 * D_ + 64 * i); }
      SB();
      float s1[2] = {0.f, 0.f}, s2[2] = {0.f, 0.f};
      LAS char* dst = L + L_X + w * REV + (q8 >> 2) * 1024 + ((q8 & 1) * 32 + r8) * 16 + ((q8 >> 1) & 1) * 8;
#pragma unroll
      for (int i = 0; i < 9; ++i) {
#pragma unroll
        for (int h = 0; h < 2; ++h) {
          *(LAS v2u*)(dst + i * 2048 + h * 128) = v2u{x[h][i][0], x[h][i][1]};
          *(LAS v2u*)(dst + i * 2048 + h * 128 + 256) = v2u{x[h][i][2], x[h][i][3]};
        }
        SB();
      }
      if (c >= 18) jobs((c - 18) * 8 + w);
#pragma unroll
      for (int i = 0; i < 9; ++i) { stat2(x[0][i], s1[0], s2[0]); stat2(x[1][i], s1[1], s2[1]); }
      if (w == 0) ones_row(L, lane);
#pragma unroll
      for (int h = 0; h < 2; ++h) { s1[h] = red8sum(s1[h]); s2[h] = red8sum(s2[h]); }
      if (q8 < 2) {
        const f32x4 p = {q8 ? s1[1] : s1[0], q8 ? s2[1] : s2[0], 0.f, 0.f}, z = {0.f, 0.f, 0.f, 0.f};
        if (w) *(LAS f32x4*)(L + L_SP + (w - 1) * 256 + (q8 * 8 + r8) * 16) = p;
        else { LAS char* d = L + L_SH + (q8 * 8 + r8) * 48; *(LAS f32x4*)d = p; *(LAS f32x4*)(d + 16) = z; *(LAS f32x4*)(d + 32) = z; }
      }
    }
    jh.cs = jtr<true>(jh.f, L + L_ATT + w * 4096, lane);
    jtr<false>(jh.f + 4, L + L_ATT + w * 4096, lane);
    if (w == 6) {
      unsigned bv[5];
#pragma unroll
      for (int i = 0; i < 5; ++i) bv[i] = *(const unsigned*)(b1 + 576 * j + min(lane + 64 * i, 287) * 2);
      const unsigned b2v = *(const unsigned*)(b2 + (lane < 32 ? 64 * j + 2 * lane : 512 + 32 * (j >> 2) + 2 * (lane & 15)));
#pragma unroll
      for (int i = 0; i < 5; ++i) *(LAS f32x2*)(L + L_B1 + min(lane + 64 * i, 287) * 8) = f32x2{AL * bflo(bv[i]), AL * bfhi(bv[i])};
      if (lane < 48) *(LAS f32x2*)(L + L_B2 + lane * 8) = f32x2{bflo(b2v), bfhi(b2v)};
    }
    if (w == 7) {
      poll<1>(flagp(ws, 320 + j, min(lane, 17)), gen + 1);
      if (lane == 0) *(LAS u64*)(L + L_W + 48) = gen + 1;
    }
    bar();
    fillr<0, 8>(rw, mkQ(j, hl, role), voff);
    tag = *(LAS u64*)(L + L_W + 48);
    tag = ((u64)(unsigned)__builtin_amdgcn_readfirstlane((unsigned)(tag >> 32)) << 32) | (unsigned)__builtin_amdgcn_readfirstlane((unsigned)tag);
    qkv(std::integral_constant<int, 0>{}, jh);
  }

#pragma unroll 1
  for (int Ly = 0; Ly < 2; ++Ly) {
    {
      FRESH();
      const float sc = 0.23570226039551584f * 1.4426950408889634f, NEG = -3.0e38f;
      f32x4 bp, bc;
#pragma unroll
      for (int r = 0; r < 4; ++r) {
        const int dk = m - (4 * g + r);
        bp[r] = (dk <= -9 && !(c < 2 && hl == 0)) ? 0.f : NEG;
        bc[r] = (unsigned)dk <= 7u ? 0.f : NEG;
      }
      asm volatile("" : "+v"(bp), "+v"(bc));
      bar();
      const LAS char* A = L + L_ATT + 2 * role * ATT_STR + hl * 512 + lane * 8;
      const LAS char* Ee = L + L_ATT + 2 * role * ATT_STR + hl * 512 + A_E + m * 32;
      v2u qm[2], km[2][2], vm[2][2], em[2], xk[2];
#pragma unroll
      for (int q = 0; q < 2; ++q) {
        qm[q] = *(const LAS v2u*)(A + q * ATT_STR + A_Q + 512);
        xk[q] = *(const LAS v2u*)(Ee + q * 512 + 8);
#pragma unroll
        for (int i = 0; i < 2; ++i) km[q][i] = *(const LAS v2u*)(A + q * ATT_STR + A_K + i * 512);
      }
      const v2u xq = *(const LAS v2u*)(Ee + 512);
#pragma unroll
      for (int q = 0; q < 2; ++q) {
        em[q] = trread(A + A_E + q * 512);
#pragma unroll
        for (int i = 0; i < 2; ++i) vm[q][i] = trread(A + q * ATT_STR + A_V + i * 512);
      }
      SB();
      const f32x4 z = {0.f, 0.f, 0.f, 0.f};
      f32x4 sp[2], sq[2];
#pragma unroll
      for (int q = 0; q < 2; ++q) {
        const v4u qf = {qm[q][0], qm[q][1], g == 0 ? xq[q] : 0u, 0u};
        sp[q] = mfma(v4u{km[q][0][0], km[q][0][1], xk[0][q], 0u}, qf, z);
        sq[q] = mfma(v4u{km[q][1][0], km[q][1][1], xk[1][q], 0u}, qf, z);
      }
      float mx[2], sum[2];
#pragma unroll
      for (int q = 0; q < 2; ++q) {
        sp[q] = sp[q] * sc + bp; sq[q] = sq[q] * sc + bc;
        mx[q] = fmaxf(fmaxf(fmaxf(sp[q][0], sp[q][1]), fmaxf(sp[q][2], sp[q][3])), fmaxf(fmaxf(sq[q][0], sq[q][1]), fmaxf(sq[q][2], sq[q][3])));
      }
#pragma unroll
      for (int q = 0; q < 2; ++q) mx[q] = red16max(mx[q]);
#pragma unroll
      for (int q = 0; q < 2; ++q) {
        sp[q] = sp[q] - mx[q]; sq[q] = sq[q] - mx[q];
#pragma unroll
        for (int r = 0; r < 4; ++r) { sp[q][r] = __builtin_amdgcn_exp2f(sp[q][r]); sq[q][r] = __builtin_amdgcn_exp2f(sq[q][r]); }
        const f32x4 t4 = sp[q] + sq[q];
        sum[q] = (t4[0] + t4[1]) + (t4[2] + t4[3]);
      }
#pragma unroll
      for (int q = 0; q < 2; ++q) sum[q] = __builtin_amdgcn_rcpf(red16sum(sum[q]));
      f32x4 o[2], oe[2];
#pragma unroll
      for (int q = 0; q < 2; ++q) {
        const v2u pp = pk4(sp[q] * sum[q]), pc = pk4(sq[q] * sum[q]);
        const v4u pf = {pp[0], pp[1], pc[0], pc[1]};
        o[q] = mfma(v4u{vm[q][0][0], vm[q][0][1], vm[q][1][0], vm[q][1][1]}, pf, z);
        oe[q] = mfma(v4u{em[0][0], em[0][1], em[1][0], em[1][1]}, pf, z);
      }
      const int ob = OB_OFF + c * XBC + hl * REV;
      const v2u oa = pk4(o[0]), oc = pk4(o[1]);
      stc128(v4u{oa[0], oa[1], oc[0], oc[1]}, rs, voff, ob + (2 * j + role) * 1024);
      if (g == 2) stc64(v2u{pk2(oe[0][0], oe[0][1]), pk2(oe[1][2], oe[1][3])}, rs, m * 8, ob + 16384 + (2 * j + role) * 128);
      SB();
      publish(ws, (5 * Ly + 0) * 32 + c, j, tag, tid);
      if (Ly == 0 && tid == 0) __hip_atomic_store(flagp(ws, 328 + j, c), tag, RLX_AGENT);
      if (role) {
        const unsigned la = (unsigned)(size_t)(L + L_X + hl * REV + voff), lb = (unsigned)(size_t)(L + L_X + 4 * REV + hl * 9216 + voff);
        sfor<0, 27>([&](auto ic) {
          constexpr int i = ic.value;
          rput<(36 + i) % R_, (i < 18 ? i : i - 18) * 1024>(i < 18 ? la : lb);
        });
      }
      if (Ly && role == 0) fillh<0>(rw, mkF(j, hl), voff);
    }
    {
      FRESH();
      const LAS char* wb = L + L_X + role * 9216 + voff;
      const LAS char* wb2 = wb + 3 * REV;
      asm volatile("" : "+v"(wb2));
      const bool ext = hl == (j & 3);
      f32x4 acc[6];
      {
        const v4u ra = *(LAS v4u*)(L + L_RES + w * 1024 + voff), rb = *(LAS v4u*)(L + L_RES + 8192 + voff);
        acc[0] = unpk4(role ? 0u : ra[0], role ? 0u : ra[1]); acc[1] = unpk4(role ? 0u : ra[2], role ? 0u : ra[3]);
        acc[2] = unpk4(role ? ra[0] : 0u, role ? ra[1] : 0u); acc[3] = unpk4(role ? ra[2] : 0u, role ? ra[3] : 0u);
        acc[4] = unpk4(role ? 0u : rb[0], role ? 0u : rb[1]); acc[5] = unpk4(role ? 0u : rb[2], role ? 0u : rb[3]);
      }
      SB();
      await(ws, 5 * Ly + 0, c, c, tag, w, lane);
      v4u of[9];
      const int ob = OB_OFF + c * XBC + hl * REV + role * 9216;
#pragma unroll
      for (int s = 0; s < 7; ++s) of[s] = ldc(rs, voff + (s & 3) * 1024, ob + (s >> 2) * 4096);
      {
        const int va = role ? g * 256 + m * 8 : voff, vb = va + (role ? 128 : 8);
#pragma unroll
        for (int s = 7; s < 9; ++s) {
          const v2u lo = ldc64(rs, va + (s - 7) * 1024, ob + 7168), hi = ldc64(rs, vb + (s - 7) * 1024, ob + 7168);
          of[s] = v4u{lo[0], lo[1], hi[0], hi[1]};
        }
      }
      SB();
      auto proj = [&](auto nc) {
        constexpr int N = nc.value;
        v4u wf[2][N];
        auto wld = [&](int s) {
#pragma unroll
          for (int i = 0; i < N; ++i) wf[s % 2][i] = *(const LAS v4u*)((i < 3 ? wb + i * REV : wb2 + (i - 3) * REV) + s * 1024);
        };
        wld(0); wld(1);
        SB();
#pragma unroll
        for (int s = 0; s < 9; ++s) {
#pragma unroll
          for (int i = 0; i < N; ++i) acc[i] = mfma(wf[s % 2][i], of[s], acc[i]);
          SB();
          if (s + 2 < 9) { wld(s + 2); SB(); }
        }
      };
      if (ext) { asm volatile("; six" ::: "memory"); proj(std::integral_constant<int, 6>{}); }
      else { asm volatile("; four" ::: "memory"); proj(std::integral_constant<int, 4>{}); }
      LAS char* R = L + L_X + 6 * REV + hl * 6144 + voff;
      *(LAS f32x4*)(R + role * 2048) = role ? acc[0] : acc[2];
      *(LAS f32x4*)(R + role * 2048 + 1024) = role ? acc[1] : acc[3];
      if (ext && role) { *(LAS f32x4*)(R + 4096) = acc[4]; *(LAS f32x4*)(R + 5120) = acc[5]; }
      if (role == 0) {
        if (Ly == 0) { poll<1>(flagp(ws, 328 + j, lane & 31), tag); fillh<0>(rw, mkF(j, hl), voff); }
        else fillh<1>(rw, mkF(j, hl), voff);
      }
      bar();
      {
        const f32x4 pa = *(LAS f32x4*)(R + 2048 - role * 2048), pb = *(LAS f32x4*)(R + 3072 - role * 2048);
        const v2u oa = pk4((role ? acc[2] : acc[0]) + pa), ob2 = pk4((role ? acc[3] : acc[1]) + pb);
        const int xb = X1_OFF + c * XBC + hl * REV;
        const v4u xo = {oa[0], oa[1], ob2[0], ob2[1]};
        stc128(xo, rs, voff, xb + (2 * j + role) * 1024);
        if (ext && role == 0) {
          const v2u ea = pk4(acc[4] + *(LAS f32x4*)(R + 4096)), eb = pk4(acc[5] + *(LAS f32x4*)(R + 5120));
          stc128(v4u{ea[0], ea[1], eb[0], eb[1]}, rs, voff, xb + (16 + (j >> 2)) * 1024);
        }
        SB();
        *(LAS v4u*)(L + L_X + hl * REV + role * 1024 + voff) = xo;
        float s1 = 0.f, s2 = 0.f;
        stat2(xo, s1, s2);
        s1 = red16sum(s1); s2 = red16sum(s2);
        if (lane < 16) *(LAS f32x2*)(L + L_SP + 1024 + hl * 256 + lane * 16 + role * 8) = f32x2{s1, s2};
      }
      publish(ws, (5 * Ly + 1) * 32 + c, j, tag, tid);
      if (role) {
        fillh<0>(rw, mkS(j, hl), voff);
        if (Ly == 0 && hl) {
          const int o4 = ((hl - 1) * 48 + min(lane, 47)) * 16;
          v4u v[18];
#pragma unroll
          for (int s = 0; s < 18; ++s) v[s] = ldc(rs, o4, CSP_OFF + (j * 18 + s) * 2304);
          SB();
          f32x4 a = __builtin_bit_cast(f32x4, v[0]);
#pragma unroll
          for (int s = 1; s < 18; ++s)
#pragma unroll
            for (int r = 0; r < 4; ++r) { const unsigned u = v[s][r]; a[r] = sadd(a[r], __uint_as_float(u)); }
          *(LAS f32x4*)(L + L_CS1 + o4) = a;
        }
      }
    }
    {
      FRESH();
      const int pbc = PB_OFF + c * (64 * 9216);
      const StreamQ wq0 = mkQ(j, hl, role);
      auto stats = [&](float (&rstd)[4], float (&mr)[4]) {
#pragma unroll
        for (int tt = 0; tt < 4; ++tt) stat_own4(L, tt, m, AL, rstd[tt], mr[tt]);
      };
      auto gtile = [&](auto pkc, f32x4 (&x)[4], const float (&rstd)[4], const float (&mr)[4], int T, v2u (&o)[4]) {
        const f32x4 cs = *(LAS f32x4*)(L + L_CS1 + T * 64 + g * 16), bb = *(LAS f32x4*)(L + L_B1 + T * 64 + g * 16);
        if constexpr (pkc.value) gelu16p(x, rstd, mr, cs, bb, o); else gelu16(x, rstd, mr, cs, bb, o);
      };
      auto hpair = [&](auto pk0, auto pk1, f32x4 (&x)[4], f32x4 (&y)[4], const float (&rstd)[4], const float (&mr)[4], int T, auto&& mid) {
        v2u oa[4], ob[4];
        gtile(pk0, x, rstd, mr, T, oa);
        mid();
        gtile(pk1, y, rstd, mr, T + 1, ob);
#pragma unroll
        for (int tt = 0; tt < 4; ++tt) *(LAS v4u*)(L + L_H + tt * REV + (T >> 1) * 1024 + voff) = v4u{oa[tt][0], oa[tt][1], ob[tt][0], ob[tt][1]};
        SB();
      };
      auto poff = [&](int ch) { const int d = 2 * hl + ch; return pbc + (d != j ? (d * 8 + j) * 9216 + role * 4096 : (role * 32 + j) * 9216 + 8192); };
      auto pstr = [&](int ch) { return 2 * hl + ch != j ? 1024 : 8 * 9216; };
      auto ppack = [&](f32x4 x, f32x4 y) { const v2u a = pk4(x), b = pk4(y); return v4u{a[0], a[1], b[0], b[1]}; };
      auto pstore2 = [&](f32x4 (&x)[4], f32x4 (&y)[4], int ch, auto slc) {
#pragma unroll
        for (int q = 0; q < 4; ++q) {
          stc128(ppack(x[q], y[q]), rs, voff, poff(ch) + q * pstr(ch));
          if (slc.value) { SB(); __builtin_amdgcn_s_sleep(slc.value); SB(); }
        }
        SB();
      };
      constexpr std::integral_constant<int, 0> I0{};
      constexpr std::integral_constant<int, 1> I1{};
      auto base = [&](f32x4& a, f32x4& b, int ks, int tt, int u) {
        a = *(LAS f32x4*)(L + L_B2 + (u >> 2) * 128 + g * 16); b = *(LAS f32x4*)(L + L_B2 + (u >> 2) * 128 + 64 + g * 16);
        acc8(a, b, *(const LAS v4u*)(L + L_X + tt * REV + (u < 8 ? u >> 2 : ks) * 1024 + voff));
        if (u < 8) acc8(a, b, *(const LAS v4u*)(L + L_OWN + u * 1024 + voff));
      };
      auto finish = [&](auto nc, f32x4 a, f32x4 b, int ks, int tt, int u, auto&& mid) {
        constexpr int N = nc.value;
        v4u pv[N];
        const int po = pbc + j * 8 * 9216 + u * 1024;
#pragma unroll
        for (int i = 0; i < N; ++i) pv[i] = ldc(rs, voff, po + (i + ((N == 7 && i >= j) ? 1 : 0)) * 9216);
        SB();
        mid(a, b);
#pragma unroll
        for (int i = 0; i < N; ++i) acc8(a, b, pv[i]);
        const v4u o = ppack(a, b);
        if (Ly == 0) stc128(o, rs, voff, X2_OFF + c * XBC + tt * REV + ks * 1024);
        else {
          u16* p = out + (size_t)(2 * (64 * (c >> 1) + 16 * tt + m) + (c & 1)) * D_ + 32 * ks + 4 * g;
          *(v2u*)p = v2u{o[0], o[1]}; *(v2u*)(p + 16) = v2u{o[2], o[3]};
        }
      };
      auto nomid = [](f32x4&, f32x4&) {};
      if (role == 0) {
        const StreamF mb = mkF(j, hl);
        __builtin_amdgcn_s_setprio(1);
        {
          f32x4 acc[4][4];
          seg<4, 4, 0, 2, 0, R_ / 2, true>({acc[0], acc[1], acc[2], acc[3]}, L + L_X + voff, rw, mb, voff);
          if (Ly == 0) fillh<1>(rw, mb, voff);
          fillr<R_, 8>(rw, mb, voff);
          if (hl == 0) {
            poll8<0>(ws, 5 * Ly + 1, c, c, tag, lane);
            *(volatile LAS unsigned*)(L + L_W + 60) = Ly + 1;
            if (Ly == 0 && blockIdx.x == 0 && lane == 0) __hip_atomic_store((gu64*)(ws + FL_OFF), tag, RLX_AGENT);
          } else lds_wait_ge<1>(L, 15, Ly + 1);
          stage8(rs, X1_OFF + c * XBC, L, j, w, lane);
          lds_wait_ge<0>(L, 16, 4 * (Ly + 1));
          seg<4, 4, 2, 18, 8, 72, false>({acc[0], acc[1], acc[2], acc[3]}, L + L_X + voff, rw, mb, voff, WordHook{L, 4u * (Ly + 1), hl});
          float rstd[4], mr[4];
          stats(rstd, mr);
          constexpr int Q4 = R_ / 4;
          hpair(I0, I0, acc[0], acc[1], rstd, mr, 4 * hl, [&] { fillr<72, Q4>(rw, mb, voff); });
          fillr<72 + Q4, Q4>(rw, mb, voff);
          hpair(I0, std::integral_constant<int, F4PK>{}, acc[2], acc[3], rstd, mr, 4 * hl + 2, [&] { fillr<72 + 2 * Q4, Q4>(rw, mb, voff); });
          fillr<72 + 3 * Q4, R_ - 3 * Q4>(rw, mb, voff);
        }
        if (EARLY) bar();
        {
          f32x4 acc[4][4];
          if constexpr (EARLY > 0) seg<4, 4, 0, EARLY ? EARLY : 1, 72, 162, true>({acc[0], acc[1], acc[2], acc[3]}, L + L_H + voff, rw, mb, voff);
          bar();
          seg<2, 4, EARLY, 18, StreamF::P1, 162, EARLY == 0>({acc[0], acc[1]}, L + L_H + voff, rw, mb, voff);
          pstore2(acc[0], acc[1], 0, std::integral_constant<int, TRICKLE>{});
          seg<2, 4, EARLY, 18, StreamF::P1 + StreamF::N2, 162, EARLY == 0>({acc[2], acc[3]}, L + L_H + voff, rw, mb, voff);
          __builtin_amdgcn_s_setprio(0);
          pstore2(acc[2], acc[3], 1, std::integral_constant<int, TRICKLE>{});
        }
        arrive<4>(ws, L, 0, (5 * Ly + 2) * 32 + c, j, tag, lane);
        lds_wait_ge<3>(L, 4 + hl, Ly + 1);
        f32x4 own[4];
        seg<1, 4, 0, 18, 144, 162, true>({own}, L + L_H + voff, rw, mb, voff);
#pragma unroll
        for (int tt = 0; tt < 4; ++tt) *(LAS v2u*)(L + L_OWN + ((hl >> 1) * 4 + tt) * 1024 + voff + 8 * (hl & 1)) = pk4(own[tt]);
        asm volatile("s_waitcnt lgkmcnt(0)" ::: "memory");
        if (lane == 0) __hip_atomic_fetch_add((LAS unsigned*)(L + L_W + 44), 1u, __ATOMIC_RELAXED, __HIP_MEMORY_SCOPE_WORKGROUP);
        lds_wait_ge<1>(L, 2, Ly + 1);
        f32x4 sa, sb;
        finish(std::integral_constant<int, 7>{}, sa, sb, 2 * j, hl, hl,
               [&](f32x4& a, f32x4& b) { lds_wait_ge<1>(L, 11, 4 * (Ly + 1)); base(a, b, 2 * j, hl, hl); });
        if (hl == 3) {
          base(sa, sb, 16 + (j >> 2), j & 3, 8);
          if (j >= 4) lds_wait_ge<1>(L, 3, Ly + 1);
          finish(std::integral_constant<int, 8>{}, sa, sb, 16 + (j >> 2), j & 3, 8, nomid);
        }
      }
      int role2 = role;
      asm volatile("" : "+s"(role2));
      if (role2 != 0) {
        const StreamS mb = mkS(j, hl);
        {
          f32x4 acc[5][4];
          seg<5, 4, 0, 2, 0, R_ / 2, true>({acc[0], acc[1], acc[2], acc[3], acc[4]}, L + L_X + voff, rw, mb, voff);
          lds_wait_ge<1>(L, 15, Ly + 1);
          stage8(rs, X1_OFF + c * XBC, L, j, w, lane);
          lds_wait_ge<1>(L, 16, 4 * (Ly + 1));
          lds_wait_ge<1>(L, 17, 4 * (Ly + 1));
          float rstd[4], mr[4];
          stats(rstd, mr);
          lds_wait_ge<2>(L, 20 + hl, 4 * (Ly + 1));
          fillr<R_ / 2, R_ / 2 + 10>(rw, mb, voff);
          seg<5, 4, 2, 18, 10, 90, false>({acc[0], acc[1], acc[2], acc[3], acc[4]}, L + L_X + voff, rw, mb, voff);
          if (EARLY) bar();
          fillr<90, R_ / 4>(rw, mb, voff);
          hpair(std::integral_constant<int, G1PK>{}, std::integral_constant<int, G2PK>{}, acc[0], acc[1], rstd, mr, 16 + 2 * hl, [] {});
          fillr<90 + R_ / 4, R_ / 4>(rw, mb, voff);
          v2u o[4];
          gtile(I1, acc[4], rstd, mr, 32 + hl, o);
          fillr<90 + 2 * (R_ / 4), R_ / 4>(rw, mb, voff);
          hpair(I1, I1, acc[2], acc[3], rstd, mr, 24 + 2 * hl, [] {});
          fillr<90 + 3 * (R_ / 4), R_ - 3 * (R_ / 4)>(rw, mb, voff);
#pragma unroll
          for (int tt = 0; tt < 4; ++tt) *(LAS v2u*)(L + L_H + tt * REV + (16 + (hl >> 1)) * 1024 + voff + 8 * (hl & 1)) = o[tt];
        }
        bar();
        {
          f32x4 acc[2][4];
          seg<2, 4, 0, 18, 90, 162, true>({acc[0], acc[1]}, L + L_H + voff, rw, mb, voff);
          __builtin_amdgcn_s_setprio(1);
          StoreHook hk{{ppack(acc[0][0], acc[1][0]), ppack(acc[0][1], acc[1][1]), ppack(acc[0][2], acc[1][2]), ppack(acc[0][3], acc[1][3])}, rs, voff, poff(0), pstr(0)};
          seg<2, 4, 0, 18, 126, 162, true>({acc[0], acc[1]}, L + L_H + voff, rw, mb, voff, hk);
          *(volatile LAS unsigned*)(L + L_W + 16 + hl * 4) = Ly + 1;
          pstore2(acc[0], acc[1], 1, I0);
        }
        u64 fv = 0;
        if (hl == 1) fv = __hip_atomic_load(flagp(ws, (5 * Ly + 2) * 32 + c, lane & 7), RLX_AGENT);
        arrive<4>(ws, L, 1, (5 * Ly + 3) * 32 + c, j, tag, lane);
        if (hl == 1) {
          if (!__all(fv == tag)) poll8<0>(ws, 5 * Ly + 2, c, c, tag, lane);
          *(volatile LAS unsigned*)(L + L_W + 8) = Ly + 1;
        }
        if (Ly == 0) fillh<0>(rw, wq0, voff);
        lds_wait_ge<1>(L, 11, 4 * (Ly + 1));
        f32x4 sa, sb;
        base(sa, sb, 2 * j + 1, hl, 4 + hl);
        SB();
        if (hl == 0) { poll8<0>(ws, 5 * Ly + 3, c, c, tag, lane); *(volatile LAS unsigned*)(L + L_W + 12) = Ly + 1; }
        else lds_wait_ge<1>(L, 3, Ly + 1);
        finish(std::integral_constant<int, 7>{}, sa, sb, 2 * j + 1, hl, 4 + hl, nomid);
        __builtin_amdgcn_s_setprio(0);
      }
      if (Ly == 0) {
        const int cprev = c >= 2 ? c - 2 : c;
        arrive<8>(ws, L, 14, 4 * 32 + c, j, tag, lane);
        if (role == 0) { lds_wait_ge<2>(L, 14, 8); fillh<0>(rw, wq0, voff); }
        if (role) { fillh<1>(rw, wq0, voff); __builtin_amdgcn_s_setprio(1); }
        await(ws, 4, c, cprev, tag, w, lane);
        stage(rs, X2_OFF + c * XBC, X2_OFF + cprev * XBC + 3 * REV, L, role ^ 1, hl, lane);
        if (role == 0) fillh<1>(rw, wq0, voff);
        NoHook nh;
        qkv(std::integral_constant<int, 1>{}, nh);
      }
    }
  }
}

extern "C" void kernel_launch(void* const* d_in, const int* in_sizes, int n_in, void* d_out, int out_size, void* d_ws,
                              size_t ws_size, hipStream_t stream) {
  const u16* x = (const u16*)d_in[0];
  const u16* wqkv = (const u16*)d_in[1];
  const u16* wo = (const u16*)d_in[2];
  const u16* w1 = (const u16*)d_in[4];
  const u16* b1 = (const u16*)d_in[5];
  const u16* w2 = (const u16*)d_in[6];
  const u16* b2 = (const u16*)d_in[7];
  (void)hipFuncSetAttribute((const void*)k_main, hipFuncAttributeMaxDynamicSharedMemorySize, LDS_BYTES);
  k_main<<<256, 512, LDS_BYTES, stream>>>((char*)d_ws, x, wqkv, wo, w1, b1, w2, b2, (u16*)d_out);
}
```

```cpp
#include <hip/hip_runtime.h>
#include <type_traits>

#define GAS __attribute__((address_space(1)))
#define LAS __attribute__((address_space(3)))
typedef unsigned short u16;
typedef unsigned v4u __attribute__((ext_vector_type(4)));
typedef unsigned v2u __attribute__((ext_vector_type(2)));
typedef float f32x4 __attribute__((ext_vector_type(4)));
typedef float f32x2 __attribute__((ext_vector_type(2)));
typedef short bf16x8 __attribute__((ext_vector_type(8)));
typedef __bf16 bf16x2 __attribute__((ext_vector_type(2)));
typedef unsigned long long u64;
typedef GAS u64 gu64;
typedef __amdgpu_buffer_rsrc_t rsrc_t;
#define RLX_AGENT __ATOMIC_RELAXED, __HIP_MEMORY_SCOPE_AGENT

constexpr int D_ = 576, F_ = 4608;
constexpr int REV = 18 * 1024;
constexpr int XBC = 4 * REV;
constexpr int XBSZ = 32 * XBC;
constexpr int FL_OFF = 0;
constexpr int WQ_OFF = 131072;
constexpr int WO_OFF = WQ_OFF + 128 * REV;
constexpr int W1_OFF = WO_OFF + 64 * REV;
constexpr int W2_OFF = W1_OFF + 288 * REV;
constexpr int CSP_OFF = W2_OFF + 288 * REV;
constexpr int X2_OFF = CSP_OFF + 8 * 18 * 2304 + 4096 - (8 * 18 * 2304) % 4096;
constexpr int OB_OFF = X2_OFF + XBSZ;
constexpr int X1_OFF = OB_OFF + XBSZ;
constexpr int PB_OFF = X1_OFF + XBSZ;
constexpr int PBSZ = 32 * 8 * 8 * 9216;
constexpr int WS_BYTES = PB_OFF + PBSZ;
constexpr int L_X = 0, L_H = 73728, L_ATT = 92160, ATT_STR = 10240, A_Q = 0, A_K = 2560, A_V = 5120, A_E = 7680;
constexpr int L_RES = 137216, L_OWN = 147456, L_CS1 = L_OWN + 8192, L_B1 = L_CS1 + 2304, L_SP = L_B1 + 2304,
              L_SH = L_SP + 2048, L_B2 = L_SH + 768, L_W = L_B2 + 384, L_END = L_W + 128;
static_assert(L_END <= 163840, "lds");
constexpr int LDS_BYTES = 163840;
constexpr int CODE_SHARE = 2048;

__device__ __forceinline__ float bflo(unsigned u) { return __uint_as_float(u << 16); }
__device__ __forceinline__ float bfhi(unsigned u) { return __uint_as_float(u & 0xffff0000u); }
__device__ __forceinline__ unsigned pk2(float a, float b) {
  f32x2 v = {a, b};
  return __builtin_bit_cast(unsigned, __builtin_convertvector(v, bf16x2));
}
__device__ __forceinline__ v2u pk4(f32x4 v) { return v2u{pk2(v[0], v[1]), pk2(v[2], v[3])}; }
__device__ __forceinline__ f32x4 unpk4(unsigned a, unsigned b) { return f32x4{bflo(a), bfhi(a), bflo(b), bfhi(b)}; }
__device__ __forceinline__ f32x4 mfma(v4u a, v4u b, f32x4 c) {
  return __builtin_amdgcn_mfma_f32_16x16x32_bf16(__builtin_bit_cast(bf16x8, a), __builtin_bit_cast(bf16x8, b), c, 0, 0, 0);
}
__device__ __forceinline__ v4u ldc(rsrc_t r, int voff, int soff) { return __builtin_amdgcn_raw_buffer_load_b128(r, voff, soff, 16); }
__device__ __forceinline__ void stc128(v4u v, rsrc_t r, int voff, int soff) {
  __builtin_amdgcn_raw_buffer_store_b128(v, r, voff, soff, 16);
  asm volatile("s_nop 1" :: "v"(v));
}
__device__ __forceinline__ v2u ldc64(rsrc_t r, int voff, int soff) { return __builtin_amdgcn_raw_buffer_load_b64(r, voff, soff, 16); }
__device__ __forceinline__ void stc64(v2u v, rsrc_t r, int voff, int soff) { __builtin_amdgcn_raw_buffer_store_b64(v, r, voff, soff, 16); }
__device__ __forceinline__ void stc32(unsigned v, rsrc_t r, int voff, int soff) { __builtin_amdgcn_raw_buffer_store_b32(v, r, voff, soff, 16); }
#define SB() __builtin_amdgcn_sched_barrier(0)
__device__ __forceinline__ void bar() {
  asm volatile("s_waitcnt lgkmcnt(0)" ::: "memory");
  __builtin_amdgcn_s_barrier();
  asm volatile("" ::: "memory");
}
__device__ __forceinline__ float red16max(float x) {
  auto r = __builtin_amdgcn_permlane16_swap(__float_as_uint(x), __float_as_uint(x), false, false);
  x = fmaxf(__uint_as_float(r[0]), __uint_as_float(r[1]));
  auto q = __builtin_amdgcn_permlane32_swap(__float_as_uint(x), __float_as_uint(x), false, false);
  return fmaxf(__uint_as_float(q[0]), __uint_as_float(q[1]));
}
__device__ __forceinline__ float red16sum(float x) {
  auto r = __builtin_amdgcn_permlane16_swap(__float_as_uint(x), __float_as_uint(x), false, false);
  x = __uint_as_float(r[0]) + __uint_as_float(r[1]);
  auto q = __builtin_amdgcn_permlane32_swap(__float_as_uint(x), __float_as_uint(x), false, false);
  return __uint_as_float(q[0]) + __uint_as_float(q[1]);
}


__device__ __forceinline__ gu64* flagp(char* ws, int line, int i) { return (gu64*)(ws + FL_OFF + 256 + line * 256 + i * 8); }
__device__ __forceinline__ void publish(char* ws, int line, int i, u64 tag, int tid) {
  asm volatile("s_waitcnt vmcnt(0)" ::: "memory");
  bar();
  if (tid == 0) __hip_atomic_store(flagp(ws, line, i), tag, RLX_AGENT);
}
template <int SL> __device__ __forceinline__ void poll(gu64* p, u64 tag) {
#pragma unroll 1
  for (unsigned spins = 0; spins < (1u << 19); ++spins) {
    const u64 v = __hip_atomic_load(p, RLX_AGENT);
    if (__all(v == tag)) break;
    if (SL) __builtin_amdgcn_s_sleep(SL);
  }
  __builtin_amdgcn_fence(__ATOMIC_ACQUIRE, "wavefront");
}
template <int SL> __device__ __forceinline__ void poll8(char* ws, int ph, int c, int c2, u64 tag, int lane) {
  poll<SL>(flagp(ws, ph * 32 + ((lane & 8) ? c2 : c), lane & 7), tag);
}
__device__ __forceinline__ void await(char* ws, int ph, int c, int c2, u64 tag, int w, int lane) {
  if (w == 0) poll8<0>(ws, ph, c, c2, tag, lane);
  bar();
}

__device__ __forceinline__ float sfma(float a, float b, float c) { float d; asm("v_fma_f32 %0, %1, %2, %3" : "=v"(d) : "v"(a), "v"(b), "v"(c)); return d; }
__device__ __forceinline__ float sfnma(float a, float b, float c) { float d; asm("v_fma_f32 %0, -%1, %2, %3" : "=v"(d) : "v"(a), "v"(b), "v"(c)); return d; }
__device__ __forceinline__ float sfmas(float a, float s) { float d; asm("v_fma_f32 %0, %1, %2, %2" : "=v"(d) : "v"(a), "s"(s)); return d; }
__device__ __forceinline__ float ssqs(float a, float s) { float d; asm("v_fma_f32 %0, %1, %1, %2" : "=v"(d) : "v"(a), "s"(s)); return d; }
__device__ __forceinline__ float smul(float a, float b) { float d; asm("v_mul_f32 %0, %1, %2" : "=v"(d) : "v"(a), "v"(b)); return d; }
__device__ __forceinline__ float sadd(float a, float b) { float d; asm("v_add_f32 %0, %1, %2" : "=v"(d) : "v"(a), "v"(b)); return d; }
__device__ __forceinline__ void stat2(v4u x, float& s1, float& s2) {
  const bf16x2 ones = __builtin_bit_cast(bf16x2, 0x3f803f80u);
#pragma unroll
  for (int i = 0; i < 4; ++i) {
    const unsigned u = x[i];
    const bf16x2 v = __builtin_bit_cast(bf16x2, u);
    s1 = __builtin_amdgcn_fdot2_f32_bf16(v, ones, s1, false);
    s2 = __builtin_amdgcn_fdot2_f32_bf16(v, v, s2, false);
  }
}
__device__ __forceinline__ void stat_fin(float s1, float s2, float scale, float& rstd, float& mr) {
  const float inv = 1.0f / D_, mean = smul(s1, inv);
  const float var = fmaxf(sfnma(mean, mean, smul(s2, inv)), 0.f);
  float r = __builtin_amdgcn_rsqf(sadd(var, 1e-5f));
  asm volatile("s_nop 0" : "+v"(r));
  rstd = smul(r, scale);
  mr = smul(mean, rstd);
}
__device__ __forceinline__ void stat_own(const LAS char* L, int tt, int m, float scale, float& rstd, float& mr) {
  const f32x4 p = *(const LAS f32x4*)(L + L_SP + tt * 256 + m * 16);
  stat_fin(sadd(p[0], p[2]), sadd(p[1], p[3]), scale, rstd, mr);
}
__device__ __forceinline__ void stat_own4(const LAS char* L, int tt, int m, float scale, float& rstd, float& mr) {
  const f32x4 p = *(const LAS f32x4*)(L + L_SP + tt * 256 + m * 16), q = *(const LAS f32x4*)(L + L_SP + 1024 + tt * 256 + m * 16);
  stat_fin(sadd(sadd(p[0], p[2]), sadd(q[0], q[2])), sadd(sadd(p[1], p[3]), sadd(q[1], q[3])), scale, rstd, mr);
}
__device__ __forceinline__ void stat_halo(const LAS char* L, int m, float& rstd, float& mr) {
  const f32x4 p = *(const LAS f32x4*)(L + L_SH + m * 48), q = *(const LAS f32x4*)(L + L_SH + m * 48 + 16), r = *(const LAS f32x4*)(L + L_SH + m * 48 + 32);
  stat_fin(sadd(sadd(sadd(p[0], p[2]), sadd(q[0], q[2])), sadd(r[0], r[2])), sadd(sadd(sadd(p[1], p[3]), sadd(q[1], q[3])), sadd(r[1], r[3])), 1.0f, rstd, mr);
}
__device__ __forceinline__ void ones_row(LAS char* L, int lane) {
  if ((lane & 15) == 0) {
#pragma unroll
    for (int s = 0; s < 18; ++s) *(LAS v4u*)(L + L_X + s * 1024 + lane * 16) = v4u{0x3f803f80u, 0x3f803f80u, 0x3f803f80u, 0x3f803f80u};
  }
}
template <int CTRL> __device__ __forceinline__ float dppadd(float x) {
  return x + __uint_as_float(__builtin_amdgcn_update_dpp(0u, __float_as_uint(x), CTRL, 0xf, 0xf, true));
}
__device__ __forceinline__ float red8sum(float x) { return dppadd<0x141>(dppadd<0x4E>(dppadd<0xB1>(x))); }
__device__ __forceinline__ void stage(rsrc_t rs, int xo, int ho, LAS char* L, int kh, int tt, int lane) {
  const int voff = lane * 16, hw = kh * 3 + min(tt, 2);
  v4u x[9], h[3];
#pragma unroll
  for (int s = 0; s < 3; ++s) h[s] = ldc(rs, voff + s * 1024, ho + hw * 3072);
#pragma unroll
  for (int s = 0; s < 9; ++s) x[s] = ldc(rs, voff + (s & 3) * 1024, xo + tt * REV + kh * 9216 + (s >> 2) * 4096);
  SB();
  const v4u ones = {0x3f803f80u, 0x3f803f80u, 0x3f803f80u, 0x3f803f80u};
  float s1[3] = {0.f, 0.f, 0.f}, s2[3] = {0.f, 0.f, 0.f};
#pragma unroll
  for (int s = 0; s < 3; ++s) {
    *(LAS v4u*)(L + L_X + hw * 3072 + s * 1024 + voff) = (lane & 15) ? h[s] : ones;
    stat2(h[s], s1[2], s2[2]);
    SB();
  }
#pragma unroll
  for (int s = 0; s < 9; ++s) {
    *(LAS v4u*)(L + L_X + (tt + 1) * REV + kh * 9216 + s * 1024 + voff) = x[s];
    stat2(x[s], s1[s & 1], s2[s & 1]);
    SB();
  }
  asm volatile("s_nop 2" : "+v"(s1[0]), "+v"(s1[1]), "+v"(s2[0]), "+v"(s2[1]));
  const float a = red16sum(sadd(s1[0], s1[1])), b = red16sum(sadd(s2[0], s2[1])), ha = red16sum(s1[2]), hb = red16sum(s2[2]);
  if (lane < 16) {
    *(LAS f32x2*)(L + L_SP + tt * 256 + lane * 16 + kh * 8) = f32x2{a, b};
    *(LAS f32x2*)(L + L_SH + lane * 48 + hw * 8) = f32x2{ha, hb};
  }
  asm volatile("s_waitcnt lgkmcnt(0)" ::: "memory");
  if (lane == 0) __hip_atomic_fetch_add((LAS unsigned*)(L + L_W + 96 + kh * 4), 1u, __ATOMIC_RELAXED, __HIP_MEMORY_SCOPE_WORKGROUP);
}

__device__ __forceinline__ void stage8(rsrc_t rs, int xo, LAS char* L, int j, int w, int lane) {
  const int tt = w & 3, kh = w >> 2, voff = lane * 16;
  v4u x[8];
#pragma unroll
  for (int i = 0; i < 4; ++i) {
    const int q = 2 + 8 * kh + 2 * i, ks = q < 16 ? (q + 2 * j) & 15 : q;
    x[2 * i] = ldc(rs, voff, xo + tt * REV + ks * 1024);
    x[2 * i + 1] = ldc(rs, voff + 1024, xo + tt * REV + ks * 1024);
  }
  SB();
  float s1[2] = {0.f, 0.f}, s2[2] = {0.f, 0.f};
#pragma unroll
  for (int s = 0; s < 8; ++s) {
    *(LAS v4u*)(L + L_X + tt * REV + (2 + 8 * kh + s) * 1024 + voff) = x[s];
    stat2(x[s], s1[s & 1], s2[s & 1]);
    SB();
  }
  const float a = red16sum(s1[0] + s1[1]), b = red16sum(s2[0] + s2[1]);
  if (lane < 16) *(LAS f32x2*)(L + L_SP + tt * 256 + lane * 16 + kh * 8) = f32x2{a, b};
  asm volatile("s_waitcnt lgkmcnt(0)" ::: "memory");
  if (lane == 0) __hip_atomic_fetch_add((LAS unsigned*)(L + L_W + 64 + kh * 4), 1u, __ATOMIC_RELAXED, __HIP_MEMORY_SCOPE_WORKGROUP);
}

constexpr int R_ = 28;
constexpr int RB = 256 - 4 * R_;
template <int SLOT, int OFF> __device__ __forceinline__ void rload(v4u rs, int voff, int so) {
  asm volatile("buffer_load_dwordx4 v[%c3:%c4], %0, %1, %2 offen offset:%c5 sc1"
               :: "v"(voff), "s"(rs), "s"(so), "i"(RB + 4 * SLOT), "i"(RB + 4 * SLOT + 3), "i"(OFF));
}
template <int SLOT, int OFF> __device__ __forceinline__ void rput(unsigned lds) {
  asm volatile("ds_write_b128 %0, v[%c1:%c2] offset:%c3" :: "v"(lds), "i"(RB + 4 * SLOT), "i"(RB + 4 * SLOT + 3), "i"(OFF) : "memory");
}
template <int N> __device__ __forceinline__ void rwait() { asm volatile("s_waitcnt vmcnt(%c0)" :: "i"(N)); }
template <int SLOT> __device__ __forceinline__ void rmfma(f32x4& acc, v4u x) {
  asm volatile("v_mfma_f32_16x16x32_bf16 %0, v[%c2:%c3], %1, %0" : "+v"(acc) : "v"(x), "i"(RB + 4 * SLOT), "i"(RB + 4 * SLOT + 3));
}
template <int SLOT> __device__ __forceinline__ void rmfma0(f32x4& acc, v4u x) {
  asm volatile("v_mfma_f32_16x16x32_bf16 %0, v[%c2:%c3], %1, 0" : "=&v"(acc) : "v"(x), "i"(RB + 4 * SLOT), "i"(RB + 4 * SLOT + 3));
}
template <int I, int N, class Fn> __device__ __forceinline__ void sfor(Fn&& f) {
  if constexpr (I < N) { f(std::integral_constant<int, I>{}); sfor<I + 1, N>(f); }
}
constexpr int cmin(int a, int b) { return a < b ? a : b; }
struct StreamQ { int b[1]; static constexpr int id(int) { return 0; } static constexpr int kb(int P) { return P; } };
struct StreamF {
  int b[4];
  static constexpr int E = 8, P1 = 72 + 4 * E, N2 = 2 * (18 - E);
  static constexpr int id(int P) { return P < 72 ? 0 : P < P1 ? 1 + ((P - 72) % 4) / 2 : P < 144 ? 1 + (P - P1) / N2 : 3; }
  static constexpr int kb(int P) { return P < 72 ? (P % 4) * 18 + P / 4 : P < P1 ? (P % 2) * 18 + (P - 72) / 4 : P < 144 ? (P % 2) * 18 + E + ((P - P1) % N2) / 2 : P - 144; }
};
struct StreamS {
  int b[4];
  static constexpr int id(int P) { return P < 90 ? (P % 5 < 4 ? 0 : 3) : 1 + (P - 90) / 36; }
  static constexpr int kb(int P) { return P < 90 ? (P % 5 < 4 ? ((P % 5) % 2 + 8 * ((P % 5) / 2)) * 18 : 0) + P / 5 : (P % 2) * 18 + ((P - 90) % 36) / 2; }
};
template <int P, class St> __device__ __forceinline__ void sload(v4u r, const St& st, int voff) {
  rload<P % R_, (St::kb(P) & 3) * 1024>(r, voff, st.b[St::id(P)] + (St::kb(P) >> 2) * 4096);
}
template <int P0, int N, class St> __device__ __forceinline__ void fillr(v4u r, const St& st, int voff) {
  sfor<P0, P0 + N>([&](auto pc) { sload<pc.value>(r, st, voff); });
  SB();
}
template <int P, int TOTAL, class St> __device__ __forceinline__ void refill(v4u r, const St& st, int voff) {
  if constexpr (P + R_ < TOTAL) sload<P + R_>(r, st, voff);
}
__device__ __forceinline__ StreamQ mkQ(int j, int hl, int role) { return StreamQ{{WQ_OFF + ((4 * j + hl) * 4 + 2 * role) * REV}}; }
__device__ __forceinline__ int w2pair(int j, int d, int role) { return W2_OFF + (36 * j + (d != j ? 4 * d : 32) + 2 * role) * REV; }
__device__ __forceinline__ StreamF mkF(int j, int hl) { return StreamF{{W1_OFF + (36 * j + 4 * hl) * REV, w2pair(j, 2 * hl, 0), w2pair(j, 2 * hl + 1, 0), W2_OFF + (40 * j + hl) * REV}}; }
__device__ __forceinline__ StreamS mkS(int j, int hl) { return StreamS{{W1_OFF + (36 * j + 16 + 2 * hl) * REV, w2pair(j, 2 * hl, 1), w2pair(j, 2 * hl + 1, 1), W1_OFF + (36 * j + 32 + hl) * REV}}; }
struct NoHook { template <int S> static constexpr int extra() { return 0; } template <int S> __device__ __forceinline__ void at() {} };
template <int NT, int NK, int S0, int S1, int P0, int TOTAL, bool INIT, int I0, int NKL, class St, class Hook>
__device__ __forceinline__ void segx(f32x4* const (&acc)[NT], const LAS char* const (&xb)[NK], v4u r, const St& mb, int voff, Hook& hook) {
  constexpr int XD = NT >= 4 ? 1 : (NT == 1 ? 4 : 2);
  v4u xf[XD][NK];
#pragma unroll
  for (int d = 0; d < XD; ++d)
#pragma unroll
    for (int tt = 0; tt < NK; ++tt)
      if (S0 + d < S1) xf[d][tt] = *(const LAS v4u*)(xb[tt] + (S0 + d) * 1024);
  SB();
  asm volatile("s_nop 1");
  sfor<S0, S1>([&](auto sc) {
    constexpr int s = sc.value, p = P0 + (s - S0) * NT, d = (s - S0) % XD;
    constexpr int RS = (R_ - I0 + 3) / 4;
    constexpr int lag = RS > 1 ? RS : 1;
    constexpr int k = s - S0;
    constexpr int issued = k < RS ? I0 + 4 * k : cmin(TOTAL, R_ + (k < lag ? P0 : p - lag * NT));
    rwait<issued - (p + NT) + Hook::template extra<s>()>();
    if constexpr (k < RS) sfor<I0 + 4 * k, cmin(R_, I0 + 4 * k + 4)>([&](auto qc) { sload<qc.value>(r, mb, voff); });
    hook.template at<s>();
    sfor<0, NK>([&](auto kc) {
      constexpr int tt = kc.value;
      sfor<0, NT>([&](auto tc) {
        constexpr int t = tc.value;
        if constexpr (t == NT - 1 && tt >= NKL) {}
        else if constexpr (INIT && s == S0) rmfma0<(p + t) % R_>(acc[t][tt], xf[d][tt]);
        else rmfma<(p + t) % R_>(acc[t][tt], xf[d][tt]);
        constexpr int idx = tt * NT + t;
        if constexpr (k >= lag && idx % NK == 0 && idx / NK < NT) refill<p - lag * NT + idx / NK, TOTAL>(r, mb, voff);
      });
      SB();
      if constexpr (s + XD < S1) { xf[d][tt] = *(const LAS v4u*)(xb[tt] + (s + XD) * 1024); SB(); }
    });
  });
  static_assert(I0 == R_ || P0 + (S1 - S0 - (R_ - I0 + 3) / 4) * NT + R_ >= TOTAL, "ramp: all refills are out inside the loop");
  sfor<0, NT>([&](auto tc) { refill<P0 + (S1 - 1 - S0) * NT + tc.value, TOTAL>(r, mb, voff); });
  asm volatile("s_nop 7\n\ts_nop 1");
  SB();
}
template <int NT, int NK, int S0, int S1, int P0, int TOTAL, bool INIT, class St, class Hook = NoHook>
__device__ __forceinline__ void seg(f32x4* const (&acc)[NT], const LAS char* xb, v4u r, const St& mb, int voff, Hook hook = Hook{}) {
  const LAS char* x2 = xb + 2 * REV;
  asm volatile("" : "+v"(x2));
  segx<NT, 4, S0, S1, P0, TOTAL, INIT, R_, 4>(acc, {xb, xb + REV, x2, x2 + REV}, r, mb, voff, hook);
}
template <int LO, class St> __device__ __forceinline__ void fillh(v4u r, const St& st, int voff) { fillr<LO ? R_ / 2 : 0, R_ / 2>(r, st, voff); }
__device__ __forceinline__ v2u trread(const LAS char* p) {
  typedef short s4 __attribute__((ext_vector_type(4)));
  return __builtin_bit_cast(v2u, __builtin_amdgcn_ds_read_tr16_b64_v4i16((LAS s4*)p));
}
constexpr float AL = 0.46866869f, KK = 2.3022082f / AL;
#define EACH16 _Pragma("unroll") for (int tt = 0; tt < 4; ++tt) _Pragma("unroll") for (int r = 0; r < 4; ++r)
__device__ __forceinline__ void gelu16(f32x4 (&a)[4], const float (&rstd)[4], const float (&mr)[4], f32x4 cs, f32x4 bb, v2u (&o)[4]) {
  float e[4][4], u[4][4];
  EACH16 e[tt][r] = sfnma(cs[r], mr[tt], bb[r]);
  SB();
  EACH16 u[tt][r] = sfma(a[tt][r], rstd[tt], e[tt][r]);
  SB();
  EACH16 e[tt][r] = ssqs(u[tt][r], KK);
  SB();
  EACH16 e[tt][r] = smul(e[tt][r], u[tt][r]);
  SB();
  EACH16 e[tt][r] = __builtin_amdgcn_exp2f(-e[tt][r]);
  SB();
  EACH16 e[tt][r] = sfmas(e[tt][r], AL);
  SB();
  EACH16 e[tt][r] = __builtin_amdgcn_rcpf(e[tt][r]);
  SB();
  EACH16 u[tt][r] = smul(u[tt][r], e[tt][r]);
  SB();
#pragma unroll
  for (int tt = 0; tt < 4; ++tt) o[tt] = v2u{pk2(u[tt][0], u[tt][1]), pk2(u[tt][2], u[tt][3])};
  SB();
}
__device__ __forceinline__ void gelu16p(f32x4 (&a)[4], const float (&rstd)[4], const float (&mr)[4], f32x4 cs, f32x4 bb, v2u (&o)[4]) {
  f32x4 e[4];
#pragma unroll
  for (int tt = 0; tt < 4; ++tt) e[tt] = bb - cs * mr[tt];
  SB();
#pragma unroll
  for (int tt = 0; tt < 4; ++tt) a[tt] = a[tt] * rstd[tt] + e[tt];
  SB();
#pragma unroll
  for (int tt = 0; tt < 4; ++tt) e[tt] = a[tt] * a[tt] + KK;
  SB();
#pragma unroll
  for (int tt = 0; tt < 4; ++tt) e[tt] = e[tt] * a[tt];
  SB();
  EACH16 e[tt][r] = __builtin_amdgcn_exp2f(-e[tt][r]);
  SB();
#pragma unroll
  for (int tt = 0; tt < 4; ++tt) e[tt] = e[tt] * AL + AL;
  SB();
  EACH16 e[tt][r] = __builtin_amdgcn_rcpf(e[tt][r]);
  SB();
#pragma unroll
  for (int tt = 0; tt < 4; ++tt) a[tt] = a[tt] * e[tt];
  SB();
#pragma unroll
  for (int tt = 0; tt < 4; ++tt) o[tt] = pk4(a[tt]);
  SB();
}
struct StoreHook {
  v4u d[4]; rsrc_t rs; int voff, po0, po1;
  template <int S> static constexpr int extra() { return S <= 6 ? 0 : S <= 9 ? 1 : S <= 12 ? 2 : S <= 15 ? 3 : 4; }
  template <int S> __device__ __forceinline__ void at() {
    if constexpr (S == 6 || S == 9 || S == 12 || S == 15) {
      constexpr int q = (S - 6) / 3;
      SB(); stc128(d[q], rs, voff, po0 + q * po1); SB();
    }
  }
};
template <int SL> __device__ __forceinline__ void lds_wait_ge(LAS char* L, int word, unsigned want) {
  volatile LAS unsigned* p = (volatile LAS unsigned*)(L + L_W + word * 4);
#pragma unroll 1
  for (unsigned spins = 0; spins < (1u << 22); ++spins) {
    if (__builtin_amdgcn_readfirstlane(*p) >= want) break;
    if (SL) __builtin_amdgcn_s_sleep(SL);
  }
  asm volatile("" ::: "memory");
}
template <int N> __device__ __forceinline__ void arrive(char* ws, LAS char* L, int word, int line, int i, u64 tag, int lane) {
  asm volatile("s_waitcnt vmcnt(0)" ::: "memory");
  if (lane == 0) {
    const unsigned old = __hip_atomic_fetch_add((LAS unsigned*)(L + L_W + word * 4), 1u, __ATOMIC_RELAXED, __HIP_MEMORY_SCOPE_WORKGROUP);
    if (old % N == N - 1) __hip_atomic_store(flagp(ws, line, i), tag, RLX_AGENT);
  }
}
__device__ __forceinline__ void acc8(f32x4& a, f32x4& b, v4u p) {
  a[0] = sadd(a[0], bflo(p[0])); a[1] = sadd(a[1], bfhi(p[0])); a[2] = sadd(a[2], bflo(p[1])); a[3] = sadd(a[3], bfhi(p[1]));
  b[0] = sadd(b[0], bflo(p[2])); b[1] = sadd(b[1], bfhi(p[2])); b[2] = sadd(b[2], bflo(p[3])); b[3] = sadd(b[3], bfhi(p[3]));
}

#define FRESH() int lane = lane0, c = c0, j = j0, w = w0; asm volatile("" : "+v"(lane)); asm volatile("" : "+s"(c), "+s"(j), "+s"(w)); \
  const int g = lane >> 4, m = lane & 15, voff = lane * 16, role = w >> 2, hl = w & 3; (void)g; (void)m; (void)voff; (void)role; (void)hl;

template <bool CS> __device__ __forceinline__ unsigned jtr(v4u* r, LAS char* scr, int lane) {
  const int g = lane >> 4, m = lane & 15;
#pragma unroll
  for (int i = 0; i < 4; ++i) *(LAS v4u*)(scr + i * 1024 + lane * 16) = r[i];
  const LAS char* p = scr + (4 * g + (m >> 2)) * 128 + (m & 3) * 8;
  float cs4[4] = {0.f, 0.f, 0.f, 0.f};
#pragma unroll
  for (int t = 0; t < 4; ++t) { const v2u lo = trread(p + t * 32), hi = trread(p + t * 32 + 2048); r[t] = v4u{lo[0], lo[1], hi[0], hi[1]}; }
  if (CS) {
#pragma unroll
    for (int t = 0; t < 4; ++t) { float dummy = 0.f; stat2(r[t], cs4[t], dummy); cs4[t] = red16sum(cs4[t]); }
  }
  return __float_as_uint(g < 2 ? (g == 0 ? cs4[0] : cs4[1]) : (g == 2 ? cs4[2] : cs4[3]));
}
__device__ __forceinline__ void wo_req(v4u rw, int j, int hl, int voff) {
  const int wa = WO_OFF + (8 * j + hl) * REV, wb = WO_OFF + (8 * j + 4 + 2 * (j >> 2)) * REV + hl * 9216;
  sfor<0, 27>([&](auto ic) { constexpr int i = ic.value, q = i < 18 ? i : i - 18; rload<i, (q & 3) * 1024>(rw, voff, (i < 18 ? wa : wb) + (q >> 2) * 4096); });
  SB();
}
struct HalfHook {
  LAS char* L;
  template <int S> static constexpr int extra() { return 0; }
  template <int S> __device__ __forceinline__ void at() { if constexpr (S == 7) { SB(); lds_wait_ge<1>(L, 25, 4u); SB(); } }
};
struct WordHook {
  LAS char* L; unsigned want; int hl;
  template <int S> static constexpr int extra() { return 0; }
  template <int S> __device__ __forceinline__ void at() {
    if constexpr (S == 9) { SB(); lds_wait_ge<1>(L, 17, want); SB(); }
    if constexpr (S == 11) { SB(); *(volatile LAS unsigned*)(L + L_W + 80 + hl * 4) = want; SB(); }
  }
};

__global__ __launch_bounds__(512, 2) __attribute__((amdgpu_num_vgpr(RB / 2 - 1)))
void k_main(char* __restrict__ ws, const u16* __restrict__ xin, const u16* __restrict__ wqkv, const u16* __restrict__ wo,
            const u16* __restrict__ w1, const u16* __restrict__ b1, const u16* __restrict__ w2, const u16* __restrict__ b2,
            u16* __restrict__ out) {
  extern __shared__ __attribute__((aligned(16))) char smem[];
  {
    u64 pc;
    asm volatile("s_getpc_b64 %0\n\tglobal_load_dword v%c2, %1, %0" : "=&s"(pc)
                 : "v"((blockIdx.x >> 3) * CODE_SHARE + min((int)threadIdx.x * 128, CODE_SHARE - 128)), "i"(RB - 1));
    SB();
  }
  LAS char* L = (LAS char*)smem;
  const int tid = threadIdx.x, lane0 = tid & 63, w0 = __builtin_amdgcn_readfirstlane(tid >> 6);
  int c0, j0;
  {
    const int b = blockIdx.x, x = b & 7, r = b >> 3;
    j0 = (x >> 1) * 2 + (r & 1);
    c0 = (x & 1) * 16 + (r >> 1);
  }
  const rsrc_t rs = __builtin_amdgcn_make_buffer_rsrc((void*)ws, 0, WS_BYTES, 0x00020000);
  const v4u rw = {(unsigned)(size_t)ws, (unsigned)((size_t)ws >> 32) & 0xffffu, (unsigned)WS_BYTES, 0x00020000u};
  asm volatile("" ::: "v255");
  u64 tag = 0;
  auto qkv = [&](auto lc) {
    NoHook jh;
    constexpr int Ly = lc.value;
    FRESH();
    auto keep = [&]() {
      const LAS char* xr = L + L_X + (hl + 1) * REV + voff;
      const v4u ra = *(const LAS v4u*)(xr + (2 * j + role) * 1024), rb = *(const LAS v4u*)(xr + (16 + (j >> 2)) * 1024);
      *(LAS v4u*)(L + L_RES + w * 1024 + voff) = ra;
      if (w == (j & 3)) *(LAS v4u*)(L + L_RES + 8192 + voff) = rb;
    };
    const StreamQ qb = mkQ(j, hl, role);
    constexpr int I0 = Ly == 0 ? 8 : R_;
    auto fix = [&](f32x4 a, const float (&cs)[4], float rstd, float mr) {
      float u[4];
#pragma unroll
      for (int r = 0; r < 4; ++r) u[r] = smul(a[r], rstd);
#pragma unroll
      for (int r = 0; r < 4; ++r) u[r] = sfnma(cs[r], mr, u[r]);
      return v2u{pk2(u[0], u[1]), pk2(u[2], u[3])};
    };
    auto bcast = [&](f32x4 a, float (&cs)[4]) {
#pragma unroll
      for (int r = 0; r < 4; ++r) cs[r] = __uint_as_float(__builtin_amdgcn_update_dpp(0u, __float_as_uint(a[r]), 0x150, 0xf, 0xf, false));
    };
    if (role == 0) {
      f32x4 a0[5], a1[5];
      const LAS char* xa = L + L_X + voff, *xc = xa + 3 * REV;
      asm volatile("" : "+v"(xc));
      if (Ly) { lds_wait_ge<1>(L, 24, 4u); lds_wait_ge<1>(L, 25, 4u); }
      keep();
      float rstd[5], mr[5], cs0[4], cs1[4];
      stat_halo(L, m, rstd[0], mr[0]);
#pragma unroll
      for (int t = 1; t < 5; ++t) stat_own(L, t - 1, m, 1.0f, rstd[t], mr[t]);
      segx<2, 5, 0, 18, 0, 36, true, I0, 5>({a0, a1}, {xa, xa + REV, xa + 2 * REV, xc, xc + REV}, rw, qb, voff, jh);
      LAS char* A = L + L_ATT + hl * ATT_STR + lane * 8;
      bcast(a0[0], cs0); bcast(a1[0], cs1);
      SB();
#pragma unroll
      for (int t = 0; t < 5; ++t) {
        if (t) *(LAS v2u*)(A + A_Q + t * 512) = pk4(a0[t] * rstd[t] - f32x4{cs0[0], cs0[1], cs0[2], cs0[3]} * mr[t]);
        *(LAS v2u*)(A + A_K + t * 512) = pk4(a1[t] * rstd[t] - f32x4{cs1[0], cs1[1], cs1[2], cs1[3]} * mr[t]);
      }
    }
    int role2 = role;
    asm volatile("" : "+s"(role2));
    if (role2) {
      f32x4 a0[5], a1[5];
      const int ta = (hl & 1) ? 3 : 1, tb = 4 - ta;
      const LAS char* x0 = L + L_X + voff, *xa = x0 + ta * REV, *xc = x0 + tb * REV;
      HalfHook hk{L};
      __builtin_amdgcn_s_setprio(1);
      if (Ly) lds_wait_ge<0>(L, 24, 4u);
      if constexpr (Ly == 0) segx<2, 5, 0, 18, 0, 36, true, I0, 3>({a0, a1}, {x0, xa, xa + REV, xc, xc + REV}, rw, qb, voff, jh);
      else segx<2, 5, 0, 18, 0, 36, true, I0, 3>({a0, a1}, {x0, xa, xa + REV, xc, xc + REV}, rw, qb, voff, hk);
      __builtin_amdgcn_s_setprio(0);
      if (Ly == 0) wo_req(rw, j, hl, voff);
      keep();
      LAS char* A = L + L_ATT + hl * ATT_STR + A_V + m * 32 + g * 8, *E = L + L_ATT + (hl & 2) * ATT_STR + A_E + m * 32 + g * 8;
      float rstd[5], mr[5], cs0[4], cs1[4];
      int to[5] = {0, ta, ta + 1, tb, tb + 1};
      stat_halo(L, m, rstd[0], mr[0]);
#pragma unroll
      for (int t = 1; t < 5; ++t) stat_own(L, to[t] - 1, m, 1.0f, rstd[t], mr[t]);
      bcast(a0[0], cs0); bcast(a1[0], cs1);
      SB();
#pragma unroll
      for (int t = 0; t < 5; ++t) {
        *(LAS v2u*)(A + to[t] * 512) = fix(a0[t], cs0, rstd[t], mr[t]);
        if (t < 3) *(LAS v2u*)(E + to[t] * 512) = fix(a1[t], cs1, rstd[t], mr[t]);
      }
      if (Ly) wo_req(rw, j, hl, voff);
    }
  };
  {
    FRESH();
    const int r8 = lane >> 3, q8 = lane & 7;
    const bool late = c & 1;
    const int rk = c >> 1;
    {
      asm volatile("global_load_dword v%c2, %0, %1" :: "v"(((2 * (64 * (c >> 1) + 8 * j + w) + (c & 1)) * D_ + min(lane, 8) * 64) * 2), "s"(xin), "i"(RB - 1));
    }
    const int qt = rk + 16 * (7 - w);
    const int et = w == 3 ? rk : rk + 2;
    const int ot = w < 3 ? rk * 2 + w - 1 : 32 + rk - 10;
    const bool doq = late && w >= 4 && qt < 54, doe = late && (w == 3 || (w == 4 && rk >= 14)), doo = late && (w == 1 || w == 2 || (w == 4 && rk >= 10 && rk < 14));
    LAS char* scr = L + L_X + w * 4736;
    if (doq) {
      const int ks = (qt * 21846) >> 16, sec = qt - 3 * ks;
      const u16* src = wqkv + (32 * ks) * (3 * D_) + 72 * j + sec * D_;
      v4u qr[5];
#pragma unroll
      for (int i = 0; i < 5; ++i) {
        const int ch = min(64 * i + lane, 287), row = (ch * 7282) >> 16;
        qr[i] = *(const v4u*)(src + row * (3 * D_) + (ch - 9 * row) * 8);
      }
      SB();
#pragma unroll
      for (int i = 0; i < 5; ++i) *(LAS v4u*)(scr + min(64 * i + lane, 287) * 16) = qr[i];
      const int qd = WQ_OFF + 16 * j * REV + (sec >> 1) * 2 * REV + (2 * ks + (sec & 1)) * 1024;
#pragma unroll
      for (int h = 0; h < 4; ++h) {
        const LAS char* p = scr + (4 * g + (m >> 2)) * 144 + (h >> 1) * 72 + (h & 1) * 40 + (m & 3) * 8;
        const v2u lo = trread(p), hi = trread(p + 16 * 144);
        stc128(v4u{lo[0], lo[1], hi[0], hi[1]}, rs, voff, qd + h * 4 * REV);
      }
    } else if (doe) {
      const u16* src = wqkv + (32 * et + (lane >> 2)) * (3 * D_) + 72 * j + min(lane & 3, 2) * D_ + 16;
      v2u xr[4];
#pragma unroll
      for (int i = 0; i < 4; ++i) xr[i] = *(const v2u*)(src + (i & 1) * 16 * 3 * D_ + (i >> 1) * 36);
      SB();
#pragma unroll
      for (int i = 0; i < 4; ++i) *(LAS v2u*)(scr + i * 512 + lane * 8) = (lane & 3) == 3 ? v2u{0u, 0u} : xr[i];
#pragma unroll
      for (int p = 0; p < 2; ++p) {
        const v2u lo = trread(scr + p * 1024 + lane * 8), hi = trread(scr + p * 1024 + 512 + lane * 8);
        const int md = WQ_OFF + (16 * j + 8 * p + 2) * REV + (2 * et + 1) * 1024;
        stc128(v4u{lo[0], lo[1], hi[0], hi[1]}, rs, voff, md);
        stc128(v4u{lo[0], lo[1], hi[0], hi[1]}, rs, voff, md + 4 * REV);
      }
    } else if (doo) {
      const int ks = ot >> 1;
      v4u jo[4];
      const int ra = 36 * ks + r8, rb = 36 * (8 * (ks - 16) + 2 * (r8 >> 2)) + 16 + (r8 & 3), o0 = (ks < 16 ? ra : rb) * D_ + 64 * ((ot & 1) ? 8 : j) + 8 * q8;
      const int s1 = (ks < 16 ? 8 : 144) * D_, s2 = (ks < 16 ? 20 : 36) * D_;
#pragma unroll
      for (int i = 0; i < 4; ++i) jo[i] = *(const v4u*)(wo + (o0 + (i & 1) * s1 + (i >> 1) * s2));
      SB();
      jtr<false>(jo, scr, lane);
#pragma unroll
      for (int t = 0; t < 4; ++t) stc128(jo[t], rs, voff, WO_OFF + (8 * j + 4 * (ot & 1) + t) * REV + ks * 1024);
    }
    u64 gen = 0;
    const int gw = late ? 0 : 5;
    if (w == gw) {
      gen = __hip_atomic_load((gu64*)(ws + FL_OFF), RLX_AGENT) + 0x9E3779B97F4A7C15ull;
      if (lane < 32) *(LAS unsigned*)(L + L_W + lane * 4) = lane == 12 ? (unsigned)gen : (lane == 13 ? (unsigned)(gen >> 32) : 0u);
    }
    if (late) { publish(ws, 320 + j, rk, gen, tid); if (w) __builtin_amdgcn_s_sleep(2); }
    else if (w < 5) asm volatile("s_waitcnt vmcnt(0)");
    const int q0 = late ? rk * 8 + w : (w >= 5 ? 128 + rk + 16 * (7 - w) : 999), q = min(q0, 161), js = (q * 7282) >> 16, jB = q - 9 * js;
    v4u jf[8];
    if (q0 < 162) {
      const u16* p1 = w1 + ((32 * js + r8) * F_ + 576 * j + 64 * jB + 8 * q8);
      const u16* p2 = w2 + ((576 * j + 32 * js + r8) * D_ + 64 * jB + 8 * q8);
#pragma unroll
      for (int i = 0; i < 4; ++i) jf[i] = *(const v4u*)(p1 + i * 8 * F_);
#pragma unroll
      for (int i = 0; i < 4; ++i) jf[4 + i] = *(const v4u*)(p2 + i * 8 * D_);
    }
    SB();
    auto jobs_out = [&]() {
      const unsigned cs = jtr<true>(jf, L + L_ATT + w * 4096, lane);
      jtr<false>(jf + 4, L + L_ATT + w * 4096, lane);
      const int d1 = W1_OFF + (36 * j + 4 * jB) * REV + (js < 16 ? (js - 2 * j) & 15 : js) * 1024, d2 = W2_OFF + (36 * j + 4 * jB) * REV + js * 1024;
#pragma unroll
      for (int t = 0; t < 8; ++t) stc128(jf[t], rs, voff, (t < 4 ? d1 : d2) + (t & 3) * REV);
      stc32(cs, rs, lane * 4, CSP_OFF + (j * 18 + js) * 2304 + jB * 256);
    };
    if (w < 5) {
      const int cc = (w == 0 && c >= 2) ? c - 2 : c, tt = w == 0 ? 3 : w - 1;
      const u16* src = xin + ((2 * (64 * (cc >> 1) + 16 * tt + r8) + (cc & 1)) * D_ + 8 * q8);
      v4u x[2][9];
#pragma unroll
      for (int i = 0; i < 9; ++i) { x[0][i] = *(const v4u*)(src + 64 * i); x[1][i] = *(const v4u*)(src + 16 * D_ + 64 * i); }
      SB();
      float s1[2] = {0.f, 0.f}, s2[2] = {0.f, 0.f};
      LAS char* dst = L + L_X + w * REV + (q8 >> 2) * 1024 + ((q8 & 1) * 32 + r8) * 16 + ((q8 >> 1) & 1) * 8;
#pragma unroll
      for (int i = 0; i < 9; ++i) {
#pragma unroll
        for (int h = 0; h < 2; ++h) {
          *(LAS v2u*)(dst + i * 2048 + h * 128) = v2u{x[h][i][0], x[h][i][1]};
          *(LAS v2u*)(dst + i * 2048 + h * 128 + 256) = v2u{x[h][i][2], x[h][i][3]};
          stat2(x[h][i], s1[h], s2[h]);
        }
        SB();
      }
      if (w == 0) ones_row(L, lane);
#pragma unroll
      for (int h = 0; h < 2; ++h) { s1[h] = red8sum(s1[h]); s2[h] = red8sum(s2[h]); }
      if (q8 < 2) {
        const f32x4 p = {q8 ? s1[1] : s1[0], q8 ? s2[1] : s2[0], 0.f, 0.f}, z = {0.f, 0.f, 0.f, 0.f};
        if (w) *(LAS f32x4*)(L + L_SP + (w - 1) * 256 + (q8 * 8 + r8) * 16) = p;
        else { LAS char* d = L + L_SH + (q8 * 8 + r8) * 48; *(LAS f32x4*)d = p; *(LAS f32x4*)(d + 16) = z; *(LAS f32x4*)(d + 32) = z; }
      }
    }
    if (late && w == 0) { poll<2>(flagp(ws, 320 + j, lane & 15), gen); *(volatile LAS unsigned*)(L + L_W + 36) = 1u; }
    if (q0 < 162) jobs_out();
    if (w == 5) {
      unsigned bv[5];
#pragma unroll
      for (int i = 0; i < 5; ++i) bv[i] = *(const unsigned*)(b1 + 576 * j + min(lane + 64 * i, 287) * 2);
      const unsigned b2v = *(const unsigned*)(b2 + (lane < 32 ? 64 * j + 2 * lane : 512 + 32 * (j >> 2) + 2 * (lane & 15)));
#pragma unroll
      for (int i = 0; i < 5; ++i) *(LAS f32x2*)(L + L_B1 + min(lane + 64 * i, 287) * 8) = f32x2{AL * bflo(bv[i]), AL * bfhi(bv[i])};
      if (lane < 48) *(LAS f32x2*)(L + L_B2 + lane * 8) = f32x2{bflo(b2v), bfhi(b2v)};
    }
    if (late) { lds_wait_ge<1>(L, 9, 1u); fillr<0, 8>(rw, mkQ(j, hl, role), voff); }
    else if (w == gw) poll<2>(flagp(ws, 320 + j, lane & 15), gen);
    publish(ws, 328 + j, c, gen, tid - gw * 64);
    if (!late) fillr<0, 8>(rw, mkQ(j, hl, role), voff);
    tag = *(LAS u64*)(L + L_W + 48);
    tag = ((u64)(unsigned)__builtin_amdgcn_readfirstlane((unsigned)(tag >> 32)) << 32) | (unsigned)__builtin_amdgcn_readfirstlane((unsigned)tag);
    qkv(std::integral_constant<int, 0>{});
  }

#pragma unroll 1
  for (int Ly = 0; Ly < 2; ++Ly) {
    {
      FRESH();
      const float sc = 0.23570226039551584f * 1.4426950408889634f, NEG = -3.0e38f;
      f32x4 bp, bc;
#pragma unroll
      for (int r = 0; r < 4; ++r) {
        const int dk = m - (4 * g + r);
        bp[r] = (dk <= -9 && !(c < 2 && hl == 0)) ? 0.f : NEG;
        bc[r] = (unsigned)dk <= 7u ? 0.f : NEG;
      }
      asm volatile("" : "+v"(bp), "+v"(bc));
      bar();
      const LAS char* A = L + L_ATT + 2 * role * ATT_STR + hl * 512 + lane * 8;
      const LAS char* Ee = L + L_ATT + 2 * role * ATT_STR + hl * 512 + A_E + m * 32;
      v2u qm[2], km[2][2], vm[2][2], em[2], xk[2];
#pragma unroll
      for (int q = 0; q < 2; ++q) {
        qm[q] = *(const LAS v2u*)(A + q * ATT_STR + A_Q + 512);
        xk[q] = *(const LAS v2u*)(Ee + q * 512 + 8);
#pragma unroll
        for (int i = 0; i < 2; ++i) km[q][i] = *(const LAS v2u*)(A + q * ATT_STR + A_K + i * 512);
      }
      const v2u xq = *(const LAS v2u*)(Ee + 512);
#pragma unroll
      for (int q = 0; q < 2; ++q) {
        em[q] = trread(A + A_E + q * 512);
#pragma unroll
        for (int i = 0; i < 2; ++i) vm[q][i] = trread(A + q * ATT_STR + A_V + i * 512);
      }
      SB();
      const f32x4 z = {0.f, 0.f, 0.f, 0.f};
      f32x4 sp[2], sq[2];
#pragma unroll
      for (int q = 0; q < 2; ++q) {
        const v4u qf = {qm[q][0], qm[q][1], g == 0 ? xq[q] : 0u, 0u};
        sp[q] = mfma(v4u{km[q][0][0], km[q][0][1], xk[0][q], 0u}, qf, z);
        sq[q] = mfma(v4u{km[q][1][0], km[q][1][1], xk[1][q], 0u}, qf, z);
      }
      float mx[2], sum[2];
#pragma unroll
      for (int q = 0; q < 2; ++q) {
        sp[q] = sp[q] * sc + bp; sq[q] = sq[q] * sc + bc;
        mx[q] = fmaxf(fmaxf(fmaxf(sp[q][0], sp[q][1]), fmaxf(sp[q][2], sp[q][3])), fmaxf(fmaxf(sq[q][0], sq[q][1]), fmaxf(sq[q][2], sq[q][3])));
      }
#pragma unroll
      for (int q = 0; q < 2; ++q) mx[q] = red16max(mx[q]);
#pragma unroll
      for (int q = 0; q < 2; ++q) {
        sp[q] = sp[q] - mx[q]; sq[q] = sq[q] - mx[q];
#pragma unroll
        for (int r = 0; r < 4; ++r) { sp[q][r] = __builtin_amdgcn_exp2f(sp[q][r]); sq[q][r] = __builtin_amdgcn_exp2f(sq[q][r]); }
        const f32x4 t4 = sp[q] + sq[q];
        sum[q] = (t4[0] + t4[1]) + (t4[2] + t4[3]);
      }
#pragma unroll
      for (int q = 0; q < 2; ++q) sum[q] = __builtin_amdgcn_rcpf(red16sum(sum[q]));
      f32x4 o[2], oe[2];
#pragma unroll
      for (int q = 0; q < 2; ++q) {
        const v2u pp = pk4(sp[q] * sum[q]), pc = pk4(sq[q] * sum[q]);
        const v4u pf = {pp[0], pp[1], pc[0], pc[1]};
        o[q] = mfma(v4u{vm[q][0][0], vm[q][0][1], vm[q][1][0], vm[q][1][1]}, pf, z);
        oe[q] = mfma(v4u{em[0][0], em[0][1], em[1][0], em[1][1]}, pf, z);
      }
      const int ob = OB_OFF + c * XBC + hl * REV;
      const v2u oa = pk4(o[0]), oc = pk4(o[1]);
      stc128(v4u{oa[0], oa[1], oc[0], oc[1]}, rs, voff, ob + (2 * j + role) * 1024);
      if (g == 2) stc64(v2u{pk2(oe[0][0], oe[0][1]), pk2(oe[1][2], oe[1][3])}, rs, m * 8, ob + 16384 + (2 * j + role) * 128);
      SB();
      publish(ws, (5 * Ly + 0) * 32 + c, j, tag, tid);
      if (role) {
        const unsigned la = (unsigned)(size_t)(L + L_X + hl * REV + voff), lb = (unsigned)(size_t)(L + L_X + 4 * REV + hl * 9216 + voff);
        sfor<0, 27>([&](auto ic) {
          constexpr int i = ic.value;
          rput<i, (i < 18 ? i : i - 18) * 1024>(i < 18 ? la : lb);
        });
      }
      if (Ly == 0 && role == 0) poll<1>(flagp(ws, 328 + j, lane & 31), tag);
      if (role == 0) fillh<0>(rw, mkF(j, hl), voff);
    }
    {
      FRESH();
      const LAS char* wb = L + L_X + role * 9216 + voff;
      const LAS char* wb2 = wb + 3 * REV;
      asm volatile("" : "+v"(wb2));
      const bool ext = hl == (j & 3);
      f32x4 acc[6];
      {
        const v4u ra = *(LAS v4u*)(L + L_RES + w * 1024 + voff), rb = *(LAS v4u*)(L + L_RES + 8192 + voff);
        acc[0] = unpk4(role ? 0u : ra[0], role ? 0u : ra[1]); acc[1] = unpk4(role ? 0u : ra[2], role ? 0u : ra[3]);
        acc[2] = unpk4(role ? ra[0] : 0u, role ? ra[1] : 0u); acc[3] = unpk4(role ? ra[2] : 0u, role ? ra[3] : 0u);
        acc[4] = unpk4(role ? 0u : rb[0], role ? 0u : rb[1]); acc[5] = unpk4(role ? 0u : rb[2], role ? 0u : rb[3]);
      }
      SB();
      await(ws, 5 * Ly + 0, c, c, tag, w, lane);
      v4u of[9];
      const int ob = OB_OFF + c * XBC + hl * REV + role * 9216;
#pragma unroll
      for (int s = 0; s < 7; ++s) of[s] = ldc(rs, voff + (s & 3) * 1024, ob + (s >> 2) * 4096);
      {
        const int va = role ? g * 256 + m * 8 : voff, vb = va + (role ? 128 : 8);
#pragma unroll
        for (int s = 7; s < 9; ++s) {
          const v2u lo = ldc64(rs, va + (s - 7) * 1024, ob + 7168), hi = ldc64(rs, vb + (s - 7) * 1024, ob + 7168);
          of[s] = v4u{lo[0], lo[1], hi[0], hi[1]};
        }
      }
      SB();
      auto proj = [&](auto nc) {
        constexpr int N = nc.value;
        v4u wf[2][N];
        auto wld = [&](int s) {
#pragma unroll
          for (int i = 0; i < N; ++i) wf[s % 2][i] = *(const LAS v4u*)((i < 3 ? wb + i * REV : wb2 + (i - 3) * REV) + s * 1024);
        };
        wld(0); wld(1);
        SB();
#pragma unroll
        for (int s = 0; s < 9; ++s) {
#pragma unroll
          for (int i = 0; i < N; ++i) acc[i] = mfma(wf[s % 2][i], of[s], acc[i]);
          SB();
          if (s + 2 < 9) { wld(s + 2); SB(); }
        }
      };
      if (ext) { asm volatile("; six" ::: "memory"); proj(std::integral_constant<int, 6>{}); }
      else { asm volatile("; four" ::: "memory"); proj(std::integral_constant<int, 4>{}); }
      LAS char* R = L + L_X + 6 * REV + hl * 6144 + voff;
      *(LAS f32x4*)(R + role * 2048) = role ? acc[0] : acc[2];
      *(LAS f32x4*)(R + role * 2048 + 1024) = role ? acc[1] : acc[3];
      if (ext && role) { *(LAS f32x4*)(R + 4096) = acc[4]; *(LAS f32x4*)(R + 5120) = acc[5]; }
      if (role == 0) fillh<1>(rw, mkF(j, hl), voff);
      bar();
      {
        const f32x4 pa = *(LAS f32x4*)(R + 2048 - role * 2048), pb = *(LAS f32x4*)(R + 3072 - role * 2048);
        const v2u oa = pk4((role ? acc[2] : acc[0]) + pa), ob2 = pk4((role ? acc[3] : acc[1]) + pb);
        const int xb = X1_OFF + c * XBC + hl * REV;
        const v4u xo = {oa[0], oa[1], ob2[0], ob2[1]};
        stc128(xo, rs, voff, xb + (2 * j + role) * 1024);
        if (ext && role == 0) {
          const v2u ea = pk4(acc[4] + *(LAS f32x4*)(R + 4096)), eb = pk4(acc[5] + *(LAS f32x4*)(R + 5120));
          stc128(v4u{ea[0], ea[1], eb[0], eb[1]}, rs, voff, xb + (16 + (j >> 2)) * 1024);
        }
        SB();
        *(LAS v4u*)(L + L_X + hl * REV + role * 1024 + voff) = xo;
        float s1 = 0.f, s2 = 0.f;
        stat2(xo, s1, s2);
        s1 = red16sum(s1); s2 = red16sum(s2);
        if (lane < 16) *(LAS f32x2*)(L + L_SP + 1024 + hl * 256 + lane * 16 + role * 8) = f32x2{s1, s2};
      }
      publish(ws, (5 * Ly + 1) * 32 + c, j, tag, tid);
      if (role) {
        fillh<0>(rw, mkS(j, hl), voff);
        if (Ly == 0 && hl) {
          const int o4 = ((hl - 1) * 48 + min(lane, 47)) * 16;
          v4u v[18];
#pragma unroll
          for (int s = 0; s < 18; ++s) v[s] = ldc(rs, o4, CSP_OFF + (j * 18 + s) * 2304);
          SB();
          f32x4 a = __builtin_bit_cast(f32x4, v[0]);
#pragma unroll
          for (int s = 1; s < 18; ++s)
#pragma unroll
            for (int r = 0; r < 4; ++r) { const unsigned u = v[s][r]; a[r] = sadd(a[r], __uint_as_float(u)); }
          *(LAS f32x4*)(L + L_CS1 + o4) = a;
        }
      }
    }
    {
      FRESH();
      const int pbc = PB_OFF + c * (64 * 9216);
      const StreamQ wq0 = mkQ(j, hl, role);
      auto stats = [&](float (&rstd)[4], float (&mr)[4]) {
#pragma unroll
        for (int tt = 0; tt < 4; ++tt) stat_own4(L, tt, m, AL, rstd[tt], mr[tt]);
      };
      auto gtile = [&](auto pkc, f32x4 (&x)[4], const float (&rstd)[4], const float (&mr)[4], int T, v2u (&o)[4]) {
        const f32x4 cs = *(LAS f32x4*)(L + L_CS1 + T * 64 + g * 16), bb = *(LAS f32x4*)(L + L_B1 + T * 64 + g * 16);
        if constexpr (pkc.value) gelu16p(x, rstd, mr, cs, bb, o); else gelu16(x, rstd, mr, cs, bb, o);
      };
      auto hpair = [&](auto pk0, auto pk1, f32x4 (&x)[4], f32x4 (&y)[4], const float (&rstd)[4], const float (&mr)[4], int T, auto&& mid) {
        v2u oa[4], ob[4];
        gtile(pk0, x, rstd, mr, T, oa);
        mid();
        gtile(pk1, y, rstd, mr, T + 1, ob);
#pragma unroll
        for (int tt = 0; tt < 4; ++tt) *(LAS v4u*)(L + L_H + tt * REV + (T >> 1) * 1024 + voff) = v4u{oa[tt][0], oa[tt][1], ob[tt][0], ob[tt][1]};
        SB();
      };
      auto poff = [&](int ch) { const int d = 2 * hl + ch; return pbc + (d != j ? (d * 8 + j) * 9216 + role * 4096 : (role * 32 + j) * 9216 + 8192); };
      auto pstr = [&](int ch) { return 2 * hl + ch != j ? 1024 : 8 * 9216; };
      auto ppack = [&](f32x4 x, f32x4 y) { const v2u a = pk4(x), b = pk4(y); return v4u{a[0], a[1], b[0], b[1]}; };
      auto pstore2 = [&](f32x4 (&x)[4], f32x4 (&y)[4], int ch) {
#pragma unroll
        for (int q = 0; q < 4; ++q) stc128(ppack(x[q], y[q]), rs, voff, poff(ch) + q * pstr(ch));
        SB();
      };
      constexpr std::integral_constant<int, 0> I0{};
      constexpr std::integral_constant<int, 1> I1{};
      auto base = [&](f32x4& a, f32x4& b, int ks, int tt, int u) {
        a = *(LAS f32x4*)(L + L_B2 + (u >> 2) * 128 + g * 16); b = *(LAS f32x4*)(L + L_B2 + (u >> 2) * 128 + 64 + g * 16);
        acc8(a, b, *(const LAS v4u*)(L + L_X + tt * REV + (u < 8 ? u >> 2 : ks) * 1024 + voff));
        if (u < 8) acc8(a, b, *(const LAS v4u*)(L + L_OWN + u * 1024 + voff));
      };
      auto finish = [&](auto nc, f32x4 a, f32x4 b, int ks, int tt, int u, auto&& mid) {
        constexpr int N = nc.value;
        v4u pv[N];
        const int po = pbc + j * 8 * 9216 + u * 1024;
#pragma unroll
        for (int i = 0; i < N; ++i) pv[i] = ldc(rs, voff, po + (i + ((N == 7 && i >= j) ? 1 : 0)) * 9216);
        SB();
        mid(a, b);
#pragma unroll
        for (int i = 0; i < N; ++i) acc8(a, b, pv[i]);
        const v4u o = ppack(a, b);
        if (Ly == 0) stc128(o, rs, voff, X2_OFF + c * XBC + tt * REV + ks * 1024);
        else {
          u16* p = out + (size_t)(2 * (64 * (c >> 1) + 16 * tt + m) + (c & 1)) * D_ + 32 * ks + 4 * g;
          *(v2u*)p = v2u{o[0], o[1]}; *(v2u*)(p + 16) = v2u{o[2], o[3]};
        }
      };
      auto nomid = [](f32x4&, f32x4&) {};
      if (role == 0) {
        const StreamF mb = mkF(j, hl);
        __builtin_amdgcn_s_setprio(1);
        {
          f32x4 acc[4][4];
          seg<4, 4, 0, 2, 0, R_ / 2, true>({acc[0], acc[1], acc[2], acc[3]}, L + L_X + voff, rw, mb, voff);
          fillr<R_, 8>(rw, mb, voff);
          if (hl == 0) {
            poll8<0>(ws, 5 * Ly + 1, c, c, tag, lane);
            *(volatile LAS unsigned*)(L + L_W + 60) = Ly + 1;
            if (Ly == 0 && blockIdx.x == 0 && lane == 0) __hip_atomic_store((gu64*)(ws + FL_OFF), tag, RLX_AGENT);
          } else lds_wait_ge<1>(L, 15, Ly + 1);
          stage8(rs, X1_OFF + c * XBC, L, j, w, lane);
          lds_wait_ge<0>(L, 16, 4 * (Ly + 1));
          seg<4, 4, 2, 18, 8, 72, false>({acc[0], acc[1], acc[2], acc[3]}, L + L_X + voff, rw, mb, voff, WordHook{L, 4u * (Ly + 1), hl});
          float rstd[4], mr[4];
          stats(rstd, mr);
          constexpr int Q4 = R_ / 4;
          hpair(I0, I0, acc[0], acc[1], rstd, mr, 4 * hl, [&] { fillr<72, Q4>(rw, mb, voff); });
          fillr<72 + Q4, Q4>(rw, mb, voff);
          hpair(I0, I0, acc[2], acc[3], rstd, mr, 4 * hl + 2, [&] { fillr<72 + 2 * Q4, Q4>(rw, mb, voff); });
          fillr<72 + 3 * Q4, R_ - 3 * Q4>(rw, mb, voff);
        }
        bar();
        {
          f32x4 acc[4][4];
          seg<4, 4, 0, 8, 72, 162, true>({acc[0], acc[1], acc[2], acc[3]}, L + L_H + voff, rw, mb, voff);
          bar();
          seg<2, 4, 8, 18, StreamF::P1, 162, false>({acc[0], acc[1]}, L + L_H + voff, rw, mb, voff);
          pstore2(acc[0], acc[1], 0);
          seg<2, 4, 8, 18, StreamF::P1 + StreamF::N2, 162, false>({acc[2], acc[3]}, L + L_H + voff, rw, mb, voff);
          __builtin_amdgcn_s_setprio(0);
          pstore2(acc[2], acc[3], 1);
        }
        arrive<4>(ws, L, 0, (5 * Ly + 2) * 32 + c, j, tag, lane);
        lds_wait_ge<3>(L, 4 + hl, Ly + 1);
        f32x4 own[4];
        seg<1, 4, 0, 18, 144, 162, true>({own}, L + L_H + voff, rw, mb, voff);
#pragma unroll
        for (int tt = 0; tt < 4; ++tt) *(LAS v2u*)(L + L_OWN + ((hl >> 1) * 4 + tt) * 1024 + voff + 8 * (hl & 1)) = pk4(own[tt]);
        asm volatile("s_waitcnt lgkmcnt(0)" ::: "memory");
        if (lane == 0) __hip_atomic_fetch_add((LAS unsigned*)(L + L_W + 44), 1u, __ATOMIC_RELAXED, __HIP_MEMORY_SCOPE_WORKGROUP);
        lds_wait_ge<1>(L, 2, Ly + 1);
        f32x4 sa, sb;
        finish(std::integral_constant<int, 7>{}, sa, sb, 2 * j, hl, hl,
               [&](f32x4& a, f32x4& b) { lds_wait_ge<1>(L, 11, 4 * (Ly + 1)); base(a, b, 2 * j, hl, hl); });
        if (hl == 3) {
          base(sa, sb, 16 + (j >> 2), j & 3, 8);
          if (j >= 4) lds_wait_ge<1>(L, 3, Ly + 1);
          finish(std::integral_constant<int, 8>{}, sa, sb, 16 + (j >> 2), j & 3, 8, nomid);
        }
      }
      int role2 = role;
      asm volatile("" : "+s"(role2));
      if (role2 != 0) {
        const StreamS mb = mkS(j, hl);
        {
          f32x4 acc[5][4];
          seg<5, 4, 0, 2, 0, R_ / 2, true>({acc[0], acc[1], acc[2], acc[3], acc[4]}, L + L_X + voff, rw, mb, voff);
          lds_wait_ge<1>(L, 15, Ly + 1);
          stage8(rs, X1_OFF + c * XBC, L, j, w, lane);
          lds_wait_ge<1>(L, 16, 4 * (Ly + 1));
          lds_wait_ge<1>(L, 17, 4 * (Ly + 1));
          float rstd[4], mr[4];
          stats(rstd, mr);
          lds_wait_ge<2>(L, 20 + hl, 4 * (Ly + 1));
          fillr<R_ / 2, R_ / 2 + 10>(rw, mb, voff);
          seg<5, 4, 2, 18, 10, 90, false>({acc[0], acc[1], acc[2], acc[3], acc[4]}, L + L_X + voff, rw, mb, voff);
          bar();
          fillr<90, R_ / 4>(rw, mb, voff);
          hpair(I0, I0, acc[0], acc[1], rstd, mr, 16 + 2 * hl, [] {});
          fillr<90 + R_ / 4, R_ / 4>(rw, mb, voff);
          v2u o[4];
          gtile(I1, acc[4], rstd, mr, 32 + hl, o);
          fillr<90 + 2 * (R_ / 4), R_ / 4>(rw, mb, voff);
          hpair(I1, I1, acc[2], acc[3], rstd, mr, 24 + 2 * hl, [] {});
          fillr<90 + 3 * (R_ / 4), R_ - 3 * (R_ / 4)>(rw, mb, voff);
#pragma unroll
          for (int tt = 0; tt < 4; ++tt) *(LAS v2u*)(L + L_H + tt * REV + (16 + (hl >> 1)) * 1024 + voff + 8 * (hl & 1)) = o[tt];
        }
        bar();
        {
          f32x4 acc[2][4];
          seg<2, 4, 0, 18, 90, 162, true>({acc[0], acc[1]}, L + L_H + voff, rw, mb, voff);
          __builtin_amdgcn_s_setprio(1);
          StoreHook hk{{ppack(acc[0][0], acc[1][0]), ppack(acc[0][1], acc[1][1]), ppack(acc[0][2], acc[1][2]), ppack(acc[0][3], acc[1][3])}, rs, voff, poff(0), pstr(0)};
          seg<2, 4, 0, 18, 126, 162, true>({acc[0], acc[1]}, L + L_H + voff, rw, mb, voff, hk);
          *(volatile LAS unsigned*)(L + L_W + 16 + hl * 4) = Ly + 1;
          pstore2(acc[0], acc[1], 1);
        }
        u64 fv = 0;
        if (hl == 1) fv = __hip_atomic_load(flagp(ws, (5 * Ly + 2) * 32 + c, lane & 7), RLX_AGENT);
        arrive<4>(ws, L, 1, (5 * Ly + 3) * 32 + c, j, tag, lane);
        if (hl == 1) {
          if (!__all(fv == tag)) poll8<0>(ws, 5 * Ly + 2, c, c, tag, lane);
          *(volatile LAS unsigned*)(L + L_W + 8) = Ly + 1;
        }
        if (Ly == 0) fillh<0>(rw, wq0, voff);
        lds_wait_ge<1>(L, 11, 4 * (Ly + 1));
        f32x4 sa, sb;
        base(sa, sb, 2 * j + 1, hl, 4 + hl);
        SB();
        if (hl == 0) { poll8<0>(ws, 5 * Ly + 3, c, c, tag, lane); *(volatile LAS unsigned*)(L + L_W + 12) = Ly + 1; }
        else lds_wait_ge<1>(L, 3, Ly + 1);
        finish(std::integral_constant<int, 7>{}, sa, sb, 2 * j + 1, hl, 4 + hl, nomid);
        __builtin_amdgcn_s_setprio(0);
      }
      if (Ly == 0) {
        const int cprev = c >= 2 ? c - 2 : c;
        arrive<8>(ws, L, 14, 4 * 32 + c, j, tag, lane);
        if (role == 0) { lds_wait_ge<2>(L, 14, 8); fillh<0>(rw, wq0, voff); }
        if (role) { fillh<1>(rw, wq0, voff); __builtin_amdgcn_s_setprio(1); }
        await(ws, 4, c, cprev, tag, w, lane);
        stage(rs, X2_OFF + c * XBC, X2_OFF + cprev * XBC + 3 * REV, L, role ^ 1, hl, lane);
        if (role == 0) fillh<1>(rw, wq0, voff);
        qkv(std::integral_constant<int, 1>{});
      }
    }
  }
}

extern "C" void kernel_launch(void* const* d_in, const int* in_sizes, int n_in, void* d_out, int out_size, void* d_ws,
                              size_t ws_size, hipStream_t stream) {
  const u16* x = (const u16*)d_in[0];
  const u16* wqkv = (const u16*)d_in[1];
  const u16* wo = (const u16*)d_in[2];
  const u16* w1 = (const u16*)d_in[4];
  const u16* b1 = (const u16*)d_in[5];
  const u16* w2 = (const u16*)d_in[6];
  const u16* b2 = (const u16*)d_in[7];
  (void)hipFuncSetAttribute((const void*)k_main, hipFuncAttributeMaxDynamicSharedMemorySize, LDS_BYTES);
  k_main<<<256, 512, LDS_BYTES, stream>>>((char*)d_ws, x, wqkv, wo, w1, b1, w2, b2, (u16*)d_out);
}
```
